```python
import torch

def _softmax(x, axis=-1):
    return torch.softmax(x, dim=axis)
def _gelu(x):
    return torch.nn.functional.gelu(x, approximate="tanh")
def _silu(x):
    return torch.nn.functional.silu(x)
def _sigmoid(x):
    return torch.sigmoid(x)
def _layernorm(x, axis=-1, eps=1e-5):
    m = x.mean(dim=axis, keepdim=True)
    v = x.var(dim=axis, keepdim=True, unbiased=False)
    return (x - m) / torch.sqrt(v + eps)
def _scatter_add(tgt, idx, src, axis=0):
    return tgt.clone().index_add_(axis, idx, src)
def _segment_sum(src, seg, n):
    out = torch.zeros((n,) + src.shape[1:], dtype=src.dtype)
    return out.index_add_(0, seg, src)
def _pad(x, pw):
    flat = tuple(v for pair in reversed(pw) for v in pair)
    return torch.nn.functional.pad(x, flat)

kernel_name = 'recurrent_scan__ssm_conv_h3__dense_activation_silu__prompt'

def setup_inputs():

    _w = (lambda a: (lambda t: t.to(torch.bfloat16) if t.is_floating_point() else t)(torch.as_tensor(a)))
    def _A_setup():
        B, S, D = 1, 2048, 576
        ks = 4
        return {
            "q": _w((lambda *s: torch.randn(*s, dtype=torch.bfloat16) * 0.05)(B, S, D)),
            "k": _w((lambda *s: torch.randn(*s, dtype=torch.bfloat16) * 0.8)(B, S, D)),
            "v": _w((lambda *s: torch.randn(*s, dtype=torch.bfloat16) * 0.8)(B, S, D)),
            "shift_k": _w((lambda *s: torch.randn(*s, dtype=torch.bfloat16) * 0.8)(D, ks) * 0.5),
            "ssm_k": _w((lambda *s: torch.randn(*s, dtype=torch.bfloat16) * 0.8)(D, S) * (1.0 / S ** 0.5)),
            "ssm_D": _w((lambda *s: torch.randn(*s, dtype=torch.bfloat16) * 0.8)(D)),
        }
    _A_in = _A_setup()

    def _B_setup():
        B, S, D, F = 1, 2048, 576, 4608
        return {
            "x": _w((lambda *s: torch.randn(*s, dtype=torch.bfloat16) * 0.05)(B, S, D)),
            "w1": _w((lambda *s: torch.randn(*s, dtype=torch.bfloat16) * 0.8)(D, F) * (D ** -0.5)),
            "w2": _w((lambda *s: torch.randn(*s, dtype=torch.bfloat16) * 0.2)(F, D) * (F ** -0.5)),
        }
    _B_in = _B_setup()

    return {
        "A_q": _A_in["q"],
        "A_k": _A_in["k"],
        "A_v": _A_in["v"],
        "A_shift_k": _A_in["shift_k"],
        "A_ssm_k": _A_in["ssm_k"],
        "A_ssm_D": _A_in["ssm_D"],
        "B_x": _B_in["x"],
        "B_w1": _B_in["w1"],
        "B_w2": _B_in["w2"],
    }

def reference(A_q, A_k, A_v, A_shift_k, A_ssm_k, A_ssm_D, B_x, B_w1, B_w2):
    def _A_ref(q, k, v, shift_k, ssm_k, ssm_D):
        B, S, D = q.shape
        ks = shift_k.shape[-1]
        k_pad = _pad(k, ((0, 0), (ks - 1, 0), (0, 0)))
        k_shift = torch.zeros_like(k)
        for t in range(ks):
            k_shift = k_shift + k_pad[:, t:t + S, :] * shift_k[:, ks - 1 - t][None, None, :]
        kv = k_shift * v
        L = 2 * S
        kv_pad = _pad(kv, ((0, 0), (0, S), (0, 0)))
        kf_pad = _pad(ssm_k, ((0, 0), (0, S)))
        kv_f = (lambda x, axes=(-2,-1): torch.fft.rfft2(x.float(), dim=axes))(kv_pad[:, None, :, :], axes=(1, 2))
        kf_f = (lambda x, axes=(-2,-1): torch.fft.rfft2(x.float(), dim=axes))(kf_pad[:, None, :], axes=(1, 2))
        kf_f = (lambda x, *d: x.permute(*d))(kf_f, 1, 2, 0)
        y_f = kv_f * kf_f[None, :, :, :]
        y = (lambda x, s, axes=(-2,-1): torch.fft.irfft2(x, s=s, dim=axes).to(torch.bfloat16))(y_f, (1, L), axes=(1, 2))[:, 0, :S, :]
        y = y + kv * ssm_D[None, None, :]
        return q * y

    def _B_ref(x, w1, w2):
        h = torch.einsum("bsd,df->bsf", x, w1)
        h = _silu(h)
        out = torch.einsum("bsf,fd->bsd", h, w2)
        return out

    x = A_q
    xn = _layernorm(x, axis=-1)
    s = _A_ref(xn, A_k, A_v, A_shift_k, A_ssm_k, A_ssm_D)
    g = _silu(_B_ref(xn, B_w1, B_w2))
    return x + s * g
```

```cpp
#include <hip/hip_runtime.h>
#include <hip/hip_bf16.h>
#include <stdint.h>
typedef unsigned short u16;
static constexpr int S=2048, D=576, F=4608;
__device__ __forceinline__ float b2f(u16 x){ return __uint_as_float(((unsigned)x)<<16); }
__device__ __forceinline__ u16 f2b(float x){ __hip_bfloat16 b = __float2bfloat16(x); return *(u16*)&b; }
__device__ __forceinline__ float rb(float x){ return b2f(f2b(x)); }

__global__ void k_ln(const u16* x, u16* xn){
  int row = blockIdx.x; int t = threadIdx.x;
  float s=0; for(int i=t;i<D;i+=64) s+=b2f(x[row*D+i]);
  for(int o=32;o;o>>=1) s+=__shfl_xor(s,o);
  float mean = s/D; float m = rb(mean);
  float v=0; for(int i=t;i<D;i+=64){ float d=b2f(x[row*D+i])-mean; v+=d*d; }
  for(int o=32;o;o>>=1) v+=__shfl_xor(v,o);
  float var = rb(v/D);
  float sd = rb(sqrtf(rb(var+1e-5f)));
  for(int i=t;i<D;i+=64){ float d=rb(b2f(x[row*D+i])-m); xn[row*D+i]=f2b(d/sd); }
}
__global__ void k_kv(const u16* k,const u16* v,const u16* sk,u16* kv){
  int idx = blockIdx.x*blockDim.x+threadIdx.x; if(idx>=S*D) return;
  int t=idx/D, d=idx%D;
  float acc=0;
  for(int j=0;j<4;j++){ int tt=t-3+j; float kk = tt>=0? b2f(k[tt*D+d]):0.f; acc = rb(acc + rb(kk*b2f(sk[d*4+3-j]))); }
  kv[idx]=f2b(acc*b2f(v[idx]));
}
__global__ void k_conv(const u16* kv,const u16* h,const u16* Dv,u16* y){
  int idx = blockIdx.x*blockDim.x+threadIdx.x; if(idx>=S*D) return;
  int t=idx/D, d=idx%D;
  float acc=0;
  for(int j=0;j<=t;j++) acc += b2f(kv[j*D+d])*b2f(h[d*S+t-j]);
  float yy=rb(acc);
  y[idx]=f2b(yy + rb(b2f(kv[idx])*b2f(Dv[d])));
}
__global__ void k_g1(const u16* xn,const u16* w1,u16* hh){
  int idx = blockIdx.x*blockDim.x+threadIdx.x;
  int t=idx/F, f=idx%F; float acc=0;
  for(int k=0;k<D;k++) acc+=b2f(xn[t*D+k])*b2f(w1[k*F+f]);
  float h=rb(acc); hh[idx]=f2b(h/(1.f+__expf(-h)));
}
__global__ void k_g2(const u16* hh,const u16* w2,const u16* x,const u16* xn,const u16* y,u16* out){
  int idx = blockIdx.x*blockDim.x+threadIdx.x;
  int t=idx/D, d=idx%D; float acc=0;
  for(int k=0;k<F;k++) acc+=b2f(hh[t*F+k])*b2f(w2[k*D+d]);
  float o=rb(acc); float g=rb(o/(1.f+__expf(-o)));
  float s=rb(b2f(xn[idx])*b2f(y[idx]));
  out[idx]=f2b(b2f(x[idx])+rb(s*g));
}
extern "C" void kernel_launch(void* const* d_in, const int* in_sizes, int n_in,
                              void* d_out, int out_size, void* d_ws, size_t ws_size,
                              hipStream_t stream) {
  const u16* q=(const u16*)d_in[0]; const u16* k=(const u16*)d_in[1]; const u16* v=(const u16*)d_in[2];
  const u16* sk=(const u16*)d_in[3]; const u16* ssk=(const u16*)d_in[4]; const u16* Dv=(const u16*)d_in[5];
  const u16* w1=(const u16*)d_in[7]; const u16* w2=(const u16*)d_in[8];
  u16* ws=(u16*)d_ws; u16* xn=ws; u16* kv=ws+S*D; u16* y=ws+2*S*D; u16* hh=ws+3*S*D;
  k_ln<<<S,64,0,stream>>>(q,xn);
  k_kv<<<S*D/256,256,0,stream>>>(k,v,sk,kv);
  k_conv<<<S*D/256,256,0,stream>>>(kv,ssk,Dv,y);
  k_g1<<<S*F/256,256,0,stream>>>(xn,w1,hh);
  k_g2<<<S*D/256,256,0,stream>>>(hh,w2,q,xn,y,(u16*)d_out);
}
```

```cpp
#include <hip/hip_runtime.h>
#include <stdint.h>

typedef unsigned short u16;
typedef unsigned int u32;
typedef unsigned long long u64;
#define LAS __attribute__((address_space(3)))
typedef __attribute__((ext_vector_type(8))) __bf16 bf16x8;
typedef __attribute__((ext_vector_type(2))) __bf16 bf16x2;
typedef __attribute__((ext_vector_type(2))) float f32x2;
typedef __attribute__((ext_vector_type(4))) float f32x4;
typedef __attribute__((ext_vector_type(16))) float f32x16;
typedef __attribute__((ext_vector_type(2))) u32 u32x2;
typedef __attribute__((ext_vector_type(3))) u32 u32x3;
typedef __attribute__((ext_vector_type(4))) u32 u32x4;
typedef __attribute__((ext_vector_type(4))) short v4i16;
typedef __attribute__((ext_vector_type(8))) short v8i16;

static constexpr int S_ = 2048, D_ = 576, F_ = 4608;
static constexpr u32 OFF_FLAG = 0;
static constexpr u32 OFF_STATS = 65536;
static constexpr u32 OFF_CS = OFF_STATS + 8192;
static constexpr u32 OFF_UR = 131072;
static constexpr u32 OFF_Y2 = OFF_UR + 576 * 4096;
static constexpr u32 OFF_W2P = OFF_Y2 + 576 * 4096;
static constexpr u32 OFF_H = OFF_W2P + 4608 * 576 * 2;
static constexpr u32 OFF_P = OFF_H + 2048 * 4608 * 2;
static constexpr u32 WS_END = OFF_P + 256 * 6 * 9216;
static constexpr int LDS_BYTES = 163840;
static constexpr int SC = 16;

#define TS(i)
#define TSC(i)
#define TSOUT(kid)

__device__ __forceinline__ float blo(u32 v) { return __uint_as_float(v << 16); }
__device__ __forceinline__ float bhi(u32 v) { return __uint_as_float(v & 0xffff0000u); }
__device__ __forceinline__ u32 pk(float lo, float hi) {
  f32x2 t = {lo, hi};
  return __builtin_bit_cast(u32, __builtin_convertvector(t, bf16x2));
}
__device__ __forceinline__ float rb(float x) { return blo(pk(x, x)); }
template <int C> __device__ __forceinline__ float dppf(float v) {
  return __int_as_float(__builtin_amdgcn_update_dpp(0, __float_as_int(v), C, 0xf, 0xf, false));
}
__device__ __forceinline__ float swp32add(float a, float b) {
  auto r = __builtin_amdgcn_permlane32_swap(__float_as_uint(a), __float_as_uint(b), false, false);
  return __uint_as_float(r[0]) + __uint_as_float(r[1]);
}
__device__ __forceinline__ float swp16add(float a, float b) {
  auto r = __builtin_amdgcn_permlane16_swap(__float_as_uint(a), __float_as_uint(b), false, false);
  return __uint_as_float(r[0]) + __uint_as_float(r[1]);
}
__device__ __forceinline__ float wave_sum(float v) {
  v += dppf<0x128>(v); v += dppf<0x124>(v); v += dppf<0x122>(v); v += dppf<0x121>(v);
  v = swp16add(v, v);
  return swp32add(v, v);
}
__device__ __forceinline__ void glds(const void* sbase, u32 voff, u32 ldsdst) {
  asm volatile("s_mov_b32 m0, %2\n\ts_nop 0\n\tglobal_load_lds_dwordx4 %0, %1" ::"v"(voff), "s"(sbase), "s"(ldsdst) : "memory");
}
__device__ __forceinline__ void blds(u32x4 srd, u32 voff, u32 ldsdst) {
  asm volatile("s_mov_b32 m0, %2\n\ts_nop 0\n\tbuffer_load_dwordx4 %0, %1, 0 offen lds" ::"v"(voff), "s"(srd), "s"(ldsdst) : "memory");
}
template <int N> __device__ __forceinline__ void vmwait() { asm volatile("s_waitcnt vmcnt(%0)" ::"n"(N) : "memory"); }
__device__ __forceinline__ void lgwait() { asm volatile("s_waitcnt lgkmcnt(0)" ::: "memory"); }
__device__ __forceinline__ void bar() { asm volatile("s_barrier" ::: "memory"); }
__device__ __forceinline__ void sbar() { __builtin_amdgcn_sched_barrier(0); }
__device__ __forceinline__ __amdgpu_buffer_rsrc_t mkrsrc(const void* p, u32 bytes) {
  return __builtin_amdgcn_make_buffer_rsrc((void*)p, 0, bytes, 0x00020000);
}
__device__ __forceinline__ f32x4 mfma16(u32x4 a, u32x4 b, f32x4 c) {
  return __builtin_amdgcn_mfma_f32_16x16x32_bf16(__builtin_bit_cast(bf16x8, a), __builtin_bit_cast(bf16x8, b), c, 0, 0, 0);
}
__device__ __forceinline__ f32x16 mfma32(u32x4 a, u32x4 b, f32x16 c) {
  return __builtin_amdgcn_mfma_f32_32x32x16_bf16(__builtin_bit_cast(bf16x8, a), __builtin_bit_cast(bf16x8, b), c, 0, 0, 0);
}
__device__ __forceinline__ u32x2 trread(LAS char* p) {
  return __builtin_bit_cast(u32x2, __builtin_amdgcn_ds_read_tr16_b64_v4i16((LAS v4i16*)p));
}

__device__ __forceinline__ void prep_stats(const u16* x, __amdgpu_buffer_rsrc_t wr, int tok, int l) {
  const u32* xr = (const u32*)(x + tok * D_);
  int lc = l < 47 ? l : 47;
  u32x3 a = *(const u32x3*)(xr + lc * 6), c = *(const u32x3*)(xr + lc * 6 + 3);
  float e[12] = {blo(a[0]), bhi(a[0]), blo(a[1]), bhi(a[1]), blo(a[2]), bhi(a[2]),
                 blo(c[0]), bhi(c[0]), blo(c[1]), bhi(c[1]), blo(c[2]), bhi(c[2])};
  float s = 0;
#pragma unroll
  for (int i = 0; i < 12; i++) s += e[i];
  if (l >= 48) s = 0;
  float mean = wave_sum(s) * (1.0f / 576.0f);
  float v = 0;
#pragma unroll
  for (int i = 0; i < 12; i++) { float d = e[i] - mean; v += d * d; }
  if (l >= 48) v = 0;
  float var = rb(wave_sum(v) * (1.0f / 576.0f));
  float sd = rb(__builtin_sqrtf(rb(var + 1e-5f)));
  if (l == 0) __builtin_amdgcn_raw_buffer_store_b32(pk(mean, sd), wr, OFF_STATS + tok * 4, 0, SC);
}

__device__ __forceinline__ void prep_colsum(const u16* w1, __amdgpu_buffer_rsrc_t wr, int cu, int tid, LAS char* lds) {
  int cl = cu % 72, pp = cu / 72;
  int l = tid & 63, w = tid >> 6;
  const char* p = (const char*)w1 + (size_t)(pp * 192 + (tid >> 3)) * 9216 + cl * 128 + (tid & 7) * 16;
  u32x4 q0 = *(const u32x4*)p, q1 = *(const u32x4*)(p + 64 * 9216), q2 = *(const u32x4*)(p + 128 * 9216);
  float a[8];
#pragma unroll
  for (int i = 0; i < 4; i++) {
    a[2 * i] = blo(q0[i]) + blo(q1[i]) + blo(q2[i]);
    a[2 * i + 1] = bhi(q0[i]) + bhi(q1[i]) + bhi(q2[i]);
  }
  float b0 = swp32add(a[0], a[4]), b1 = swp32add(a[1], a[5]), b2 = swp32add(a[2], a[6]), b3 = swp32add(a[3], a[7]);
  float c0 = swp16add(b0, b2), c1 = swp16add(b1, b3);
  c0 += dppf<0x128>(c0); c1 += dppf<0x128>(c1);
  float r = (l & 8) ? c1 : c0;
  *(LAS float*)(lds + w * 256 + (8 * (l & 7) + (l >> 3)) * 4) = r;
  lgwait(); bar();
  if (tid < 64) {
    float s = 0;
#pragma unroll
    for (int i = 0; i < 8; i++) s += *(LAS float*)(lds + i * 256 + tid * 4);
    __builtin_amdgcn_raw_buffer_store_b32(__float_as_uint(s), wr, OFF_CS + (pp * F_ + cl * 64 + tid) * 4, 0, SC);
  }
}

__device__ __forceinline__ void prep_kv(const u16* k, const u16* v, const u16* sk, __amdgpu_buffer_rsrc_t wr, int ku, int w, int l) {
  int t = (ku / 9) * 128 + (w >> 2) * 64 + (l & 15) * 4;
  int ch = (ku % 9) * 64 + (w & 3) * 16 + (l >> 4) * 4;
  u32x2 kr[7], vr[4];
#pragma unroll
  for (int i = 0; i < 7; i++) {
    int tt = t - 3 + i;
    kr[i] = *(const u32x2*)(k + (tt < 0 ? 0 : tt) * D_ + ch);
    if (tt < 0) kr[i] = (u32x2){0, 0};
  }
#pragma unroll
  for (int i = 0; i < 4; i++) vr[i] = *(const u32x2*)(v + (t + i) * D_ + ch);
  u32x4 s0 = *(const u32x4*)(sk + ch * 4), s1 = *(const u32x4*)(sk + ch * 4 + 8);
#pragma unroll
  for (int c = 0; c < 4; c++) {
    u32 sa = c == 0 ? s0[0] : c == 1 ? s0[2] : c == 2 ? s1[0] : s1[2];
    u32 sb = c == 0 ? s0[1] : c == 1 ? s0[3] : c == 2 ? s1[1] : s1[3];
    float w0 = blo(sa), w1 = bhi(sa), w2 = blo(sb), w3 = bhi(sb);
    float kk[7], o[4];
#pragma unroll
    for (int i = 0; i < 7; i++) kk[i] = (c & 1) ? bhi(kr[i][c >> 1]) : blo(kr[i][c >> 1]);
#pragma unroll
    for (int i = 0; i < 4; i++) {
      float vv = (c & 1) ? bhi(vr[i][c >> 1]) : blo(vr[i][c >> 1]);
      o[i] = (kk[i] * w3 + kk[i + 1] * w2 + kk[i + 2] * w1 + kk[i + 3] * w0) * vv;
    }
    u32x2 r = {pk(o[3], o[2]), pk(o[1], o[0])};
    __builtin_amdgcn_raw_buffer_store_b64(r, wr, OFF_UR + (ch + c) * 4096 + 2 * (2044 - t), 0, SC);
  }
}

__device__ __forceinline__ u32x4 w2_load(const u16* w2, int u, int l) {
  int FT = u / 36, rem = u % 36, g = rem / 9, cb = rem % 9;
  int j = l >> 3;
  int f = 32 * FT + 16 * (g >> 1) + 8 * (j >> 2) + 4 * (g & 1) + (j & 3);
  return *(const u32x4*)((const char*)w2 + (size_t)f * 1152 + cb * 128 + (l & 7) * 16);
}
__device__ __forceinline__ void w2_store(u32x4 d, __amdgpu_buffer_rsrc_t wr, int u, int l) {
  int FT = u / 36, rem = u % 36, g = rem / 9, cb = rem % 9;
  auto a = __builtin_amdgcn_permlane32_swap(d[0], d[2], false, false);
  auto b = __builtin_amdgcn_permlane32_swap(d[1], d[3], false, false);
  auto c = __builtin_amdgcn_permlane16_swap(a[0], b[0], false, false);
  auto e = __builtin_amdgcn_permlane16_swap(a[1], b[1], false, false);
  u32 in[4] = {c[0], c[1], e[0], e[1]};
  u32 sel = (l & 8) ? 0x03020706u : 0x05040100u;
  u32x4 o;
#pragma unroll
  for (int i = 0; i < 4; i++) {
    u32 p = (u32)__builtin_amdgcn_update_dpp(0, (int)in[i], 0x128, 0xf, 0xf, false);
    o[i] = __builtin_amdgcn_perm(p, in[i], sel);
  }
  int c8 = l & 7, jp = l >> 3;
  int dt = 4 * cb + (c8 >> 1);
  __builtin_amdgcn_raw_buffer_store_b128(o, wr, OFF_W2P + (FT * 36 + dt) * 1024 + (g * 16 + 8 * (c8 & 1) + jp) * 16, 0, SC);
}

static constexpr u32 L_B1 = 73728, L_SP = 147456, L_CS = 161792;

template <bool DMA>
__device__ __forceinline__ void g1_pair(const u32x4 (&xf)[36], f32x16& acc, const f32x16& pa, LAS char* lds, u32 abase, u32 csoff,
                                        float nm, float cc, __amdgpu_buffer_rsrc_t wr, u32 hoff, bool dostore,
                                        const char* dsrc, u32 dvoff, u32 ddst, bool dma) {
  LAS char* a0 = lds + abase;
  LAS char* a1 = a0 + 18 * 2048;
  u32x2 ra[3], rb_[3];
  ra[0] = trread(a0); rb_[0] = trread(a0 + 512);
  ra[1] = trread(a0 + 2048); rb_[1] = trread(a0 + 2048 + 512);
  f32x4 cs[2];
  cs[0] = *(LAS f32x4*)(lds + csoff);
  float X[16], E[16];
  u32 P[8];
#pragma unroll
  for (int ks = 0; ks < 36; ks++) {
    if (ks + 2 < 36) {
      LAS char* ap = (ks + 2 < 18 ? a0 + (ks + 2) * 2048 : a1 + (ks + 2 - 18) * 2048);
      ra[(ks + 2) % 3] = trread(ap); rb_[(ks + 2) % 3] = trread(ap + 512);
    }
    u32x4 A = {ra[ks % 3][0], ra[ks % 3][1], rb_[ks % 3][0], rb_[ks % 3][1]};
    if (ks == 0) { f32x16 z = {}; acc = mfma32(A, xf[0], z); }
    else acc = mfma32(A, xf[ks], acc);
    if (ks % 8 == 4 && ks / 8 < 3) cs[(ks / 8 + 1) & 1] = *(LAS f32x4*)(lds + csoff + (ks / 8 + 1) * 32);
    if (ks % 2 == 0 && ks / 2 < 16) {
      int e = ks / 2;
      X[e] = __builtin_fmaf(nm, cs[(e >> 2) & 1][e & 3], pa[e]);
      E[e] = __builtin_amdgcn_exp2f(X[e] * cc);
    }
    if (ks % 2 == 1 && ks / 2 < 16) {
      int e = ks / 2;
      E[e] = __builtin_amdgcn_rcpf(1.0f + E[e]);
    }
    if (ks % 2 == 0 && ks >= 2 && ks / 2 - 1 < 16) {
      int e = ks / 2 - 1;
      X[e] *= E[e];
      if (e & 1) P[e >> 1] = pk(X[e - 1], X[e]);
      if (e == 7 || e == 15) {
        if (dostore) {
          u32x4 o = {P[(e >> 1) - 3], P[(e >> 1) - 2], P[(e >> 1) - 1], P[e >> 1]};
          __builtin_amdgcn_raw_buffer_store_b128(o, wr, hoff + (e == 15 ? 512 : 0), 0, SC);
        }
      }
    }
    if (DMA && ks % 2 == 1 && ks / 2 < 9) {
      if (dma) glds(dsrc + (size_t)(ks / 2) * (64 * 9216), dvoff, ddst + (ks / 2) * 8192);
    }
    sbar();
  }
}

__device__ __forceinline__ void g1_epi_final(const f32x16& pa, LAS char* lds, u32 csoff, float nm, float cc,
                                             __amdgpu_buffer_rsrc_t wr, u32 hoff) {
  u32 P[8];
#pragma unroll
  for (int q = 0; q < 4; q++) {
    f32x4 cs = *(LAS f32x4*)(lds + csoff + q * 32);
    float h[4];
#pragma unroll
    for (int i = 0; i < 4; i++) {
      float X = __builtin_fmaf(nm, cs[i], pa[4 * q + i]);
      h[i] = X * __builtin_amdgcn_rcpf(1.0f + __builtin_amdgcn_exp2f(X * cc));
    }
    P[2 * q] = pk(h[0], h[1]); P[2 * q + 1] = pk(h[2], h[3]);
  }
  __builtin_amdgcn_raw_buffer_store_b128((u32x4){P[0], P[1], P[2], P[3]}, wr, hoff, 0, SC);
  __builtin_amdgcn_raw_buffer_store_b128((u32x4){P[4], P[5], P[6], P[7]}, wr, hoff + 512, 0, SC);
}

__device__ __forceinline__ void g1_phase(const u16* x, const u16* w1, const u16* ssk, char* ws, __amdgpu_buffer_rsrc_t wr, LAS char* lds,
                                         int rt, int j, int cb, int w, int l, int tid) {
  const int tg = w & 3, fg = w >> 2;
  const int odd = j & 1;
  u32 st = *(const u32*)(ws + OFF_STATS + (rt * 128 + tg * 32 + (l & 31)) * 4);
  float csv = 0;
  if (tid < 288) {
    const float* cp = (const float*)(ws + OFF_CS) + j * 288 + tid;
    csv = cp[0] + cp[F_] + cp[2 * F_];
  }
  {
    const char* xb = (const char*)x + (size_t)rt * 128 * 1152;
#pragma unroll
    for (int h = 0; h < 2; h++) {
      u32 vo = (16 * w + 8 * h + (l >> 3)) * 1152 + (((l & 7) ^ (4 * h + (l >> 4))) * 16);
#pragma unroll
      for (int kb = 0; kb < 9; kb++) glds(xb + kb * 128, vo, kb * 16384 + (16 * w + 8 * h) * 128);
    }
  }
  const char* w1b = (const char*)w1 + j * 576;
  const char* sb = w1b + (odd ? 0 : 512);
  const char* pb = w1b + (odd ? 64 : 0);
  const u32 svo = (l >> 2) * 9216 + (l & 3) * 16;
  glds(sb + (size_t)w * 16 * 9216, svo, L_SP + w * 1024);
  if (w < 6) glds(sb + (size_t)(w + 8) * 16 * 9216, svo, L_SP + (w + 8) * 1024);
  if (tid < 288) *(LAS float*)(lds + L_CS + tid * 4) = csv;
  const float nm = -blo(st);
  const float cc = -1.44269504088896f / bhi(st);
  vmwait<0>(); lgwait(); bar();
  TS(1);
  u32x4 xf[36];
  {
    u32 row = tg * 32 + (l & 31);
#pragma unroll
    for (int q = 0; q < 4; q++) {
      u32 a = row * 128 + (((2 * q + (l >> 5)) ^ ((row >> 1) & 7)) * 16);
#pragma unroll
      for (int kb = 0; kb < 9; kb++) xf[kb * 4 + q] = *(LAS u32x4*)(lds + a + kb * 16384);
    }
  }
  lgwait(); bar();
  TS(2);
  const u32 pvo = (l >> 3) * 9216 + (((l & 7) ^ (((l >> 4) & 1) << 2)) * 16);
  glds(sb + (size_t)(14 + w) * 16 * 9216, svo, w * 1024);
  glds(sb + (size_t)(22 + w) * 16 * 9216, svo, (8 + w) * 1024);
  if (w < 6) glds(sb + (size_t)(30 + w) * 16 * 9216, svo, (16 + w) * 1024);
  const char* pw = pb + (size_t)w * 8 * 9216;
#pragma unroll
  for (int i = 0; i < 9; i++) glds(pw + (size_t)i * 64 * 9216, pvo, L_B1 + w * 1024 + i * 8192);
  f32x16 acc0 = {}, acc1 = {};
  const int kh = l >> 5;
  if (w < 4) {
    u32 sa = (8 * kh + ((l >> 2) & 3)) * 64 + ((l >> 4) & 1) * 32 + (l & 3) * 8;
    LAS char* s0 = lds + L_SP + sa;
    LAS char* s1 = lds + sa - 14 * 1024;
    u32x2 ra[3], rb_[3];
    ra[0] = trread(s0); rb_[0] = trread(s0 + 256);
    ra[1] = trread(s0 + 1024); rb_[1] = trread(s0 + 1024 + 256);
#pragma unroll
    for (int ks = 0; ks < 36; ks++) {
      if (ks == 12) { vmwait<9>(); bar(); }
      if (ks + 2 < 36) {
        LAS char* ap = (ks + 2 < 14 ? s0 : s1) + (ks + 2) * 1024;
        ra[(ks + 2) % 3] = trread(ap); rb_[(ks + 2) % 3] = trread(ap + 256);
      }
      u32x4 A = {ra[ks % 3][0], ra[ks % 3][1], rb_[ks % 3][0], rb_[ks % 3][1]};
      acc1 = mfma32(A, xf[ks], acc1);
      sbar();
    }
  } else {
    vmwait<9>(); bar();
  }
  vmwait<0>(); bar();
  TS(3);
  const u32 abase = (8 * kh + ((l >> 2) & 3)) * 128 + ((fg * 64 + ((l >> 4) & 1) * 32 + (l & 3) * 8) ^ (((l >> 3) & 1) << 6));
  const u32 hbase = OFF_H + (rt * 144 + 9 * j) * 8192 + (2 * tg + ((l >> 4) & 1)) * 1024 + (kh * 16 + (l & 15)) * 16;
  int ptile = odd ? 0 : 8;
  bool dostore = (w < 4);
#pragma unroll 1
  for (int p = 0; p < 4; p++) {
    u32 cur = (p & 1) ? 0 : L_B1, nxt = (p & 1) ? L_B1 : 0;
    g1_pair<true>(xf, acc0, acc1, lds, cur + abase, L_CS + ptile * 128 + kh * 16, nm, cc, wr, hbase + ptile * 8192, dostore,
                  pw + (p + 1) * 128, pvo, nxt + w * 1024, p < 3);
    acc1 = acc0;
    ptile = odd + 2 * p + fg;
    if (dostore) vmwait<1>(); else vmwait<0>();
    dostore = true;
    bar();
    TS(4 + p);
  }
  g1_epi_final(acc1, lds, L_CS + ptile * 128 + kh * 16, nm, cc, wr, hbase + ptile * 8192);
}

static constexpr u32 L_KC = L_B1;
static constexpr u32 L_U = L_B1 + 12 * 4160;
static constexpr u32 L_RED = 0;

__device__ __forceinline__ void conv_issue(const u16* ssk, const char* ws, LAS char* lds, int cb, int w, int l) {
#pragma unroll
  for (int i = 0; i < 6; i++) {
    int idx = w * 6 + i, cc = idx >> 2, part = idx & 3;
    int ch = cc >> 2, cp = cc & 3;
    u64 base = (u64)(ssk + (size_t)(cb * 3 + ch) * 2048);
    u32x4 srd = {(u32)base, (u32)(base >> 32) & 0xffffu, 4096u, 0x00020000u};
    blds(srd, cp * 2 + part * 1024 + l * 16, L_KC + cc * 4160 + 32 + part * 1024);
  }
  if (w < 6) {
#pragma unroll
    for (int i = 0; i < 2; i++) {
      int idx = w * 2 + i, ch = idx >> 2, part = idx & 3;
      glds(ws + OFF_UR + (size_t)(cb * 3 + ch) * 4096 + part * 1024, l * 16, L_U + ch * 4608 + part * 1024);
    }
  }
  if (w == 6 && l < 12) {
    int ch = l >> 2, cp = l & 3;
    u32x2 k0 = *(const u32x2*)(ssk + (size_t)(cb * 3 + ch) * 2048);
    u32x4 c1 = {0, 0, 0, 0};
    if (cp == 1) c1[3] = k0[0] << 16;
    if (cp == 2) c1[3] = k0[0];
    if (cp == 3) { c1[2] = k0[0] << 16; c1[3] = (k0[0] >> 16) | (k0[1] << 16); }
    *(LAS u32x4*)(lds + L_KC + l * 4160) = (u32x4){0, 0, 0, 0};
    *(LAS u32x4*)(lds + L_KC + l * 4160 + 16) = c1;
  }
  if (w < 3 && l < 32) *(LAS u32x4*)(lds + L_U + w * 4608 + 4096 + l * 16) = (u32x4){0, 0, 0, 0};
}

__device__ __forceinline__ void conv_compute(const u16* Dv, __amdgpu_buffer_rsrc_t wr, LAS char* lds, int cb, int w, int l) {
  int ch = (w < 3) ? 0 : (w < 6) ? 1 : 2;
  int r0 = (w == 0 || w == 4 || w == 6) ? 0 : (w == 1 || w == 5) ? 3 : (w == 7) ? 4 : 6;
  int nr = (w >= 6) ? 4 : (w == 2 || w == 3) ? 2 : 3;
  const int b = l & 15, g = l >> 4;
  const int cp = (b + 1) & 3;
  LAS char* ap = lds + L_KC + (ch * 4 + cp) * 4160 + 2 * (b + 1 - cp) + 16 * g + 64 * r0;
  LAS char* bp = lds + L_U + ch * 4608 + 2 * (240 - 16 * b + 8 * g) + 64 * r0;
  f32x4 acc[8];
#pragma unroll
  for (int i = 0; i < 8; i++) acc[i] = (f32x4){0, 0, 0, 0};
#pragma unroll 1
  for (int r = 0; r < nr; r++) {
    u32x4 A[8], B[8];
#pragma unroll
    for (int i = 0; i < 8; i++) {
      B[i] = *(LAS u32x4*)(bp + 512 * (7 - i));
      u32x2 lo = *(LAS u32x2*)(ap + 512 * i), hi = *(LAS u32x2*)(ap + 512 * i + 8);
      A[i] = (u32x4){lo[0], lo[1], hi[0], hi[1]};
    }
#pragma unroll
    for (int q = 0; q < 8; q++)
#pragma unroll
      for (int i = 0; i + q < 8; i++) acc[i + q] = mfma16(A[i], B[q], acc[i + q]);
    ap += 64; bp += 64;
  }
#pragma unroll
  for (int i = 0; i < 8; i++) *(LAS f32x4*)(lds + L_RED + w * 8192 + i * 1024 + l * 16) = acc[i];
  lgwait(); bar();
#pragma unroll
  for (int i = 0; i < 3; i++) {
    int u = w * 3 + i, c2 = u >> 3, blk = u & 7;
    LAS char* rp = lds + L_RED + blk * 1024 + l * 16;
    f32x4 y;
    if (c2 == 0) y = *(LAS f32x4*)(rp) + *(LAS f32x4*)(rp + 8192) + *(LAS f32x4*)(rp + 2 * 8192);
    else if (c2 == 1) y = *(LAS f32x4*)(rp + 3 * 8192) + *(LAS f32x4*)(rp + 4 * 8192) + *(LAS f32x4*)(rp + 5 * 8192);
    else y = *(LAS f32x4*)(rp + 6 * 8192) + *(LAS f32x4*)(rp + 7 * 8192);
    int t0 = 256 * blk + 16 * b + 4 * g;
    u32x2 uu = *(LAS u32x2*)(lds + L_U + c2 * 4608 + 2 * (2044 - t0));
    float dv = blo(Dv[cb * 3 + c2]);
    float o0 = rb(y[0]) + rb(bhi(uu[1]) * dv), o1 = rb(y[1]) + rb(blo(uu[1]) * dv);
    float o2 = rb(y[2]) + rb(bhi(uu[0]) * dv), o3 = rb(y[3]) + rb(blo(uu[0]) * dv);
    __builtin_amdgcn_raw_buffer_store_b64((u32x2){pk(o0, o1), pk(o2, o3)}, wr, OFF_Y2 + (cb * 3 + c2) * 4096 + t0 * 2, 0, SC);
  }
}

__device__ __forceinline__ void ldg16(u32x4& d, const char* sbase, u32 voff) {
  asm volatile("global_load_dwordx4 %0, %1, %2" : "=v"(d) : "v"(voff), "s"(sbase) : "memory");
}
__device__ __forceinline__ void ldg16o(u32x4& d, const char* sbase, u32 voff) {
  asm volatile("global_load_dwordx4 %0, %1, %2 offset:1024" : "=v"(d) : "v"(voff), "s"(sbase) : "memory");
}

__device__ __forceinline__ void g2_issue(const char* wsrc, const char* hsrc, const u32 (&cvo)[5], u32 ldst, int w, int l, u32x4 (&hb)[4]) {
#pragma unroll
  for (int i = 0; i < 4; i++) glds(wsrc, cvo[i], ldst + (w * 4 + i) * 1024);
  if (l < 32) glds(wsrc, cvo[4], ldst + (32 + (w >> 1)) * 1024 + (w & 1) * 512);
  ldg16(hb[0], hsrc, l * 16); ldg16o(hb[1], hsrc, l * 16);
  ldg16(hb[2], hsrc + 8192, l * 16); ldg16o(hb[3], hsrc + 8192, l * 16);
}

__device__ __forceinline__ void g2_phase(const u16* x, u16* out, char* ws, __amdgpu_buffer_rsrc_t wr, LAS char* lds,
                                         int rt, int kq, int dn, int w, int l, u64 tag) {
  const int tg = w & 3, kh = w >> 2;
  u32 cvo[5];
#pragma unroll
  for (int i = 0; i < 5; i++) {
    int c = i < 4 ? w * 4 + i : 32 + (w >> 1);
    int u = c / 18, hf = (c / 9) & 1, n = c % 9;
    cvo[i] = ((18 * hf + u) * 36 + n) * 1024 + l * 16 + (i == 4 ? (w & 1) * 512 : 0);
  }
  const char* wsrc = ws + OFF_W2P + (size_t)(36 * kq * 36 + 9 * dn) * 1024;
  const char* hsrc = ws + OFF_H + (size_t)((rt * 144 + 36 * kq + 18 * kh) * 8 + 2 * tg) * 1024;
  u32x4 hb[4][4];
  f32x4 acc[2][9];
#pragma unroll
  for (int n = 0; n < 9; n++) { acc[0][n] = (f32x4){0, 0, 0, 0}; acc[1][n] = (f32x4){0, 0, 0, 0}; }
#pragma unroll
  for (int it = 0; it < 3; it++) g2_issue(wsrc + it * 73728, hsrc + it * 16384, cvo, it * 36864, w, l, hb[it]);
#pragma unroll
  for (int it = 0; it < 9; it++) {
    if (it <= 6) vmwait<18>(); else if (it == 7) vmwait<9>(); else vmwait<0>();
    bar();
    asm volatile("" : "+v"(hb[it & 3][0]), "+v"(hb[it & 3][1]), "+v"(hb[it & 3][2]), "+v"(hb[it & 3][3]));
    if (it + 3 < 9) g2_issue(wsrc + (it + 3) * 73728, hsrc + (it + 3) * 16384, cvo, ((it + 3) & 3) * 36864, w, l, hb[(it + 3) & 3]);
    LAS char* ap = lds + (it & 3) * 36864 + kh * 9216 + l * 16;
    u32x4 A[3];
    A[0] = *(LAS u32x4*)(ap); A[1] = *(LAS u32x4*)(ap + 1024);
#pragma unroll
    for (int s = 0; s < 18; s++) {
      int u = s / 9, n = s % 9;
      if (s + 2 < 18) A[(s + 2) % 3] = *(LAS u32x4*)(ap + ((s + 2) / 9) * 18432 + ((s + 2) % 9) * 1024);
      acc[0][n] = mfma16(A[s % 3], hb[it & 3][u * 2], acc[0][n]);
      acc[1][n] = mfma16(A[s % 3], hb[it & 3][u * 2 + 1], acc[1][n]);
      sbar();
    }
  }
  TS(9);
  bar();
#pragma unroll
  for (int n = 0; n < 9; n++) *(LAS f32x4*)(lds + w * 9216 + n * 1024 + l * 16) = kh ? acc[0][n] : acc[1][n];
  lgwait(); bar();
  f32x4 r[9];
#pragma unroll
  for (int n = 0; n < 9; n++) r[n] = (kh ? acc[1][n] : acc[0][n]) + *(LAS f32x4*)(lds + (w ^ 4) * 9216 + n * 1024 + l * 16);
  u64* flags = (u64*)(ws + OFF_FLAG);
  const int xbit = rt >> 3, i7 = rt & 7;
  if (tg != kq) {
    int ob = 2 * tg + xbit + 8 * (dn * 8 + i7);
    int slot = ((kq - tg - 1) & 3) * 2 + kh;
    u32 po = OFF_P + (ob * 6 + slot) * 9216 + l * 16;
#pragma unroll
    for (int n = 0; n < 9; n++) __builtin_amdgcn_raw_buffer_store_b128(__builtin_bit_cast(u32x4, r[n]), wr, po + n * 1024, 0, 16);
    vmwait<0>();
    if (l == 0) __hip_atomic_store(flags + ob * 8 + slot, tag, __ATOMIC_RELAXED, __HIP_MEMORY_SCOPE_AGENT);
  } else {
#pragma unroll
    for (int n = 0; n < 9; n++) *(LAS f32x4*)(lds + 73728 + kh * 9216 + n * 1024 + l * 16) = r[n];
  }
  TS(10);
  const int myb = 2 * kq + xbit + 8 * (dn * 8 + i7);
  const int nu = (w < 2) ? 3 : 2;
  u32x2 xv[3]; u32 st[3]; u16 yv[3][4];
#pragma unroll
  for (int i = 0; i < 3; i++) {
    if (i < nu) {
      int u = w + 8 * i, tt = u / 9, n = u % 9;
      int tok = rt * 128 + kq * 32 + tt * 16 + (l & 15), d = dn * 144 + n * 16 + 4 * (l >> 4);
      xv[i] = *(const u32x2*)(x + tok * D_ + d);
      st[i] = *(const u32*)(ws + OFF_STATS + tok * 4);
      const u16* yp = (const u16*)(ws + OFF_Y2) + d * 2048 + tok;
#pragma unroll
      for (int e = 0; e < 4; e++) yv[i][e] = yp[e * 2048];
    }
  }
  if (w == 0) {
    int spins = 0;
    while (true) {
      u64 f = tag;
      if (l < 6) f = __hip_atomic_load(flags + myb * 8 + l, __ATOMIC_RELAXED, __HIP_MEMORY_SCOPE_AGENT);
      if (__all(f == tag)) break;
      if (++spins > 2000000) break;
      __builtin_amdgcn_s_sleep(1);
    }
  }
  lgwait(); bar();
  __builtin_amdgcn_fence(__ATOMIC_ACQUIRE, "agent");
  TS(11);
#pragma unroll
  for (int i = 0; i < 3; i++) {
    if (i < nu) {
      int u = w + 8 * i, tt = u / 9, n = u % 9;
      const char* pp = ws + OFF_P + (size_t)(myb * 6 + tt) * 9216 + n * 1024 + l * 16;
      f32x4 p0 = *(const f32x4*)(pp), p1 = *(const f32x4*)(pp + 2 * 9216), p2 = *(const f32x4*)(pp + 4 * 9216);
      f32x4 s = *(LAS f32x4*)(lds + 73728 + tt * 9216 + n * 1024 + l * 16) + p0 + p1 + p2;
      if (i == 0) { asm volatile("" :: "v"(s)); TS(12); asm volatile("" :: "v"(xv[0]), "v"(st[0])); TS(13); }
      float m = blo(st[i]), rs = 1.0f / bhi(st[i]);
      float res[4];
#pragma unroll
      for (int e = 0; e < 4; e++) {
        float xe = (e & 1) ? bhi(xv[i][e >> 1]) : blo(xv[i][e >> 1]);
        float o = rb(s[e] * rs);
        float g = rb(o * __builtin_amdgcn_rcpf(1.0f + __builtin_amdgcn_exp2f(o * -1.44269504088896f)));
        float xn = rb(rb(xe - m) * rs);
        float sv = rb(xn * blo(yv[i][e]));
        res[e] = xe + rb(sv * g);
      }
      int tok = rt * 128 + kq * 32 + tt * 16 + (l & 15), d = dn * 144 + n * 16 + 4 * (l >> 4);
      *(u32x2*)(out + tok * D_ + d) = (u32x2){pk(res[0], res[1]), pk(res[2], res[3])};
    }
  }
}

__global__ void __launch_bounds__(512, 2) k_prep(const u16* x, const u16* k, const u16* v, const u16* sk, const u16* w1, const u16* w2, char* ws) {
  extern __shared__ char smem[];
  LAS char* lds = (LAS char*)smem;
  const int b = blockIdx.x, tid = threadIdx.x, l = tid & 63, w = __builtin_amdgcn_readfirstlane(tid >> 6);
  __amdgpu_buffer_rsrc_t wr = mkrsrc(ws, WS_END);
  if (tid < 8) ((u64*)(ws + OFF_FLAG))[b * 8 + tid] = 0;
  u32x4 wq[3];
#pragma unroll
  for (int i = 0; i < 3; i++) { int u = b * 8 + w + 2048 * i; if (u < 5184) wq[i] = w2_load(w2, u, l); }
  prep_stats(x, wr, b * 8 + w, l);
  if (b >= 112) prep_kv(k, v, sk, wr, b - 112, w, l);
#pragma unroll
  for (int i = 0; i < 3; i++) { int u = b * 8 + w + 2048 * i; if (u < 5184) w2_store(wq[i], wr, u, l); }
  if (b < 216) prep_colsum(w1, wr, b, tid, lds);
}

__global__ void __launch_bounds__(512, 2) k_g1(const u16* x, const u16* w1, const u16* ssk, const u16* Dv, char* ws) {
  extern __shared__ char smem[];
  LAS char* lds = (LAS char*)smem;
  const int b = blockIdx.x, tid = threadIdx.x, l = tid & 63, w = __builtin_amdgcn_readfirstlane(tid >> 6);
  const int xc = b & 7, i = b >> 3;
  const int rt = (xc & 1) * 8 + (i & 7), kq = xc >> 1, dn = i >> 3, j = 4 * kq + dn;
  __amdgpu_buffer_rsrc_t wr = mkrsrc(ws, WS_END);
  TS(0); TSC(30);
  g1_phase(x, w1, ssk, ws, wr, lds, rt, j, b, w, l, tid);
  TS(8);
  if (b < 192) {
    conv_issue(ssk, ws, lds, b, w, l);
    vmwait<0>(); lgwait(); bar();
    conv_compute(Dv, wr, lds, b, w, l);
  }
  TS(15); TSC(31);
  TSOUT(1);
}

__global__ void __launch_bounds__(512, 2) k_g2(const u16* x, u16* out, char* ws) {
  extern __shared__ char smem[];
  LAS char* lds = (LAS char*)smem;
  const int b = blockIdx.x, tid = threadIdx.x, l = tid & 63, w = __builtin_amdgcn_readfirstlane(tid >> 6);
  const int xc = b & 7, i = b >> 3;
  const int rt = (xc & 1) * 8 + (i & 7), kq = xc >> 1, dn = i >> 3;
  __amdgpu_buffer_rsrc_t wr = mkrsrc(ws, WS_END);
  TS(0); TSC(30);
  g2_phase(x, out, ws, wr, lds, rt, kq, dn, w, l, 1ull);
  TS(15); TSC(31);
  TSOUT(2);
}

extern "C" void kernel_launch(void* const* d_in, const int* in_sizes, int n_in, void* d_out, int out_size, void* d_ws,
                              size_t ws_size, hipStream_t stream) {
  const u16* q = (const u16*)d_in[0]; const u16* k = (const u16*)d_in[1]; const u16* v = (const u16*)d_in[2];
  const u16* sk = (const u16*)d_in[3]; const u16* ssk = (const u16*)d_in[4]; const u16* Dv = (const u16*)d_in[5];
  const u16* w1 = (const u16*)d_in[7]; const u16* w2 = (const u16*)d_in[8];
  char* ws = (char*)d_ws;
  hipFuncSetAttribute((const void*)k_g1, hipFuncAttributeMaxDynamicSharedMemorySize, LDS_BYTES);
  hipFuncSetAttribute((const void*)k_g2, hipFuncAttributeMaxDynamicSharedMemorySize, LDS_BYTES);
  k_prep<<<256, 512, 4096, stream>>>(q, k, v, sk, w1, w2, ws);
  k_g1<<<256, 512, LDS_BYTES, stream>>>(q, w1, ssk, Dv, ws);
  k_g2<<<256, 512, LDS_BYTES, stream>>>(q, (u16*)d_out, ws);
}
```

```cpp
#include <hip/hip_runtime.h>
#include <stdint.h>

typedef unsigned short u16;
typedef unsigned int u32;
typedef unsigned long long u64;
#define LAS __attribute__((address_space(3)))
#define CAS __attribute__((address_space(4)))
typedef __attribute__((ext_vector_type(8))) __bf16 bf16x8;
typedef __attribute__((ext_vector_type(2))) __bf16 bf16x2;
typedef __attribute__((ext_vector_type(2))) float f32x2;
typedef __attribute__((ext_vector_type(4))) float f32x4;
typedef __attribute__((ext_vector_type(16))) float f32x16;
typedef __attribute__((ext_vector_type(2))) u32 u32x2;
typedef __attribute__((ext_vector_type(4))) u32 u32x4;
typedef __attribute__((ext_vector_type(4))) short v4i16;

static constexpr int S_ = 2048, D_ = 576, F_ = 4608;
static constexpr u32 OFF_E = 0;
static constexpr u32 OFF_FP = 4096;
static constexpr u32 OFF_FK = OFF_FP + 2048;
static constexpr u32 OFF_FH = OFF_FK + 2048;
static constexpr u32 OFF_FY = OFF_FH + 2048;
static constexpr u32 OFF_FX = OFF_FY + 2048;
static constexpr u32 OFF_STATS = 65536;
static constexpr u32 OFF_CS = OFF_STATS + 8192;
static constexpr u32 OFF_UR = 131072;
static constexpr u32 OFF_Y2 = OFF_UR + 576 * 4096;
static constexpr u32 OFF_H = OFF_Y2 + 576 * 4096;
static constexpr u32 OFF_P = OFF_H + 2048 * 4608 * 2;
static constexpr u32 WS_END = OFF_P + 256 * 3 * 9216;
static constexpr int LDS_BYTES = 163840;
static constexpr int SC = 16;
static constexpr u32 OOB = 0xF0000000u;
static constexpr u32 L_B1 = 73728, L_S1 = 147456, L_CSS = 159744, L_CS = 161792, L_ST = 162944, L_CNT = 163456;

#define TS(i)
#define TSARG
#define TSPASS

__device__ __forceinline__ float blo(u32 v) { return __uint_as_float(v << 16); }
__device__ __forceinline__ float bhi(u32 v) { return __uint_as_float(v & 0xffff0000u); }
__device__ __forceinline__ u32 pk(float lo, float hi) {
  f32x2 t = {lo, hi};
  return __builtin_bit_cast(u32, __builtin_convertvector(t, bf16x2));
}
__device__ __forceinline__ float rb(float x) { return blo(pk(x, x)); }
template <int C> __device__ __forceinline__ float dppf(float v) {
  return __int_as_float(__builtin_amdgcn_update_dpp(0, __float_as_int(v), C, 0xf, 0xf, false));
}
__device__ __forceinline__ float swp32add(float a, float b) {
  auto r = __builtin_amdgcn_permlane32_swap(__float_as_uint(a), __float_as_uint(b), false, false);
  return __uint_as_float(r[0]) + __uint_as_float(r[1]);
}
__device__ __forceinline__ float swp16add(float a, float b) {
  auto r = __builtin_amdgcn_permlane16_swap(__float_as_uint(a), __float_as_uint(b), false, false);
  return __uint_as_float(r[0]) + __uint_as_float(r[1]);
}
__device__ __forceinline__ float wave_sum(float v) {
  v += dppf<0x128>(v); v += dppf<0x124>(v); v += dppf<0x122>(v); v += dppf<0x121>(v);
  v = swp16add(v, v);
  return swp32add(v, v);
}
__device__ __forceinline__ void glds(const void* sbase, u32 voff, u32 ldsdst) {
  asm volatile("s_mov_b32 m0, %2\n\ts_nop 0\n\tglobal_load_lds_dwordx4 %0, %1" ::"v"(voff), "s"(sbase), "s"(ldsdst) : "memory");
}
__device__ __forceinline__ void blds(u32x4 srd, u32 voff, u32 ldsdst) {
  asm volatile("s_mov_b32 m0, %2\n\ts_nop 0\n\tbuffer_load_dwordx4 %0, %1, 0 offen lds" ::"v"(voff), "s"(srd), "s"(ldsdst) : "memory");
}
__device__ __forceinline__ void ldg16(u32x4& d, const void* sbase, u32 voff) {
  asm volatile("global_load_dwordx4 %0, %1, %2" : "=v"(d) : "v"(voff), "s"(sbase) : "memory");
}
__device__ __forceinline__ void ldg16s(u32x4& d, const void* sbase, u32 voff) {
  asm volatile("global_load_dwordx4 %0, %1, %2 sc1" : "=v"(d) : "v"(voff), "s"(sbase) : "memory");
}
__device__ __forceinline__ void ldg16so(u32x4& d, const void* sbase, u32 voff) {
  asm volatile("global_load_dwordx4 %0, %1, %2 offset:1024 sc1" : "=v"(d) : "v"(voff), "s"(sbase) : "memory");
}
__device__ __forceinline__ void ldg8(u32x2& d, const void* sbase, u32 voff) {
  asm volatile("global_load_dwordx2 %0, %1, %2" : "=v"(d) : "v"(voff), "s"(sbase) : "memory");
}
__device__ __forceinline__ void ldg8s(u32x2& d, const void* sbase, u32 voff) {
  asm volatile("global_load_dwordx2 %0, %1, %2 sc1" : "=v"(d) : "v"(voff), "s"(sbase) : "memory");
}
__device__ __forceinline__ void ldb8(u32x2& d, u32x4 srd, u32 voff) {
  asm volatile("buffer_load_dwordx2 %0, %1, %2, 0 offen" : "=v"(d) : "v"(voff), "s"(srd) : "memory");
}
__device__ __forceinline__ void ldb16(u32x4& d, u32x4 srd, u32 voff) {
  asm volatile("buffer_load_dwordx4 %0, %1, %2, 0 offen" : "=v"(d) : "v"(voff), "s"(srd) : "memory");
}
template <int N> __device__ __forceinline__ void vmwait() { asm volatile("s_waitcnt vmcnt(%0)" ::"n"(N) : "memory"); }
__device__ __forceinline__ void lgwait() { asm volatile("s_waitcnt lgkmcnt(0)" ::: "memory"); }
__device__ __forceinline__ void bar() { asm volatile("s_barrier" ::: "memory"); }
#define PIN(x) asm volatile("" : "+v"(x))
__device__ __forceinline__ void sbar() { __builtin_amdgcn_sched_barrier(0); }
__device__ __forceinline__ __amdgpu_buffer_rsrc_t mkrsrc(const void* p, u32 bytes) {
  return __builtin_amdgcn_make_buffer_rsrc((void*)p, 0, bytes, 0x00020000);
}
__device__ __forceinline__ u32x4 mksrd(const void* p, u32 bytes) {
  u64 b = (u64)p;
  return (u32x4){(u32)b, (u32)(b >> 32) & 0xffffu, bytes, 0x00020000u};
}
__device__ __forceinline__ f32x4 mfma16(u32x4 a, u32x4 b, f32x4 c) {
  return __builtin_amdgcn_mfma_f32_16x16x32_bf16(__builtin_bit_cast(bf16x8, a), __builtin_bit_cast(bf16x8, b), c, 0, 0, 0);
}
__device__ __forceinline__ f32x16 mfma32(u32x4 a, u32x4 b, f32x16 c) {
  return __builtin_amdgcn_mfma_f32_32x32x16_bf16(__builtin_bit_cast(bf16x8, a), __builtin_bit_cast(bf16x8, b), c, 0, 0, 0);
}
__device__ __forceinline__ u32x2 trread(LAS char* p) {
  return __builtin_bit_cast(u32x2, __builtin_amdgcn_ds_read_tr16_b64_v4i16((LAS v4i16*)p));
}
__device__ __forceinline__ void flag_set(char* ws, u32 off, u64 tag) {
  __hip_atomic_store((__attribute__((address_space(1))) u64*)(ws + off), tag, __ATOMIC_RELAXED, __HIP_MEMORY_SCOPE_AGENT);
}
__device__ __forceinline__ void flag_wait(char* ws, u32 off, bool act, u64 tag) {
  for (int spins = 0; spins < 4000000; spins++) {
    u64 f = __hip_atomic_load((__attribute__((address_space(1))) u64*)(ws + off), __ATOMIC_RELAXED, __HIP_MEMORY_SCOPE_AGENT);
    if (__all(f == tag || !act)) break;
    __builtin_amdgcn_s_sleep(1);
  }
  asm volatile("" ::: "memory");
}
__device__ __forceinline__ bool flag_ok(u32x2 f, bool act, u64 tag) {
  return __all(!act || (f[0] == (u32)tag && f[1] == (u32)(tag >> 32)));
}

static constexpr u32 L_KC = L_B1;
static constexpr u32 L_U = L_B1 + 12 * 4160;

struct G1Ctx {
  LAS char* lds;
  char* ws;
  const u16* ssk;
  __amdgpu_buffer_rsrc_t wr;
  float nm, cc;
  u64 tag;
  int b, cb, w, l;
};

template <int MODE>
__device__ __forceinline__ void g1_pair(const G1Ctx& c, const u32x4 (&xf)[36], f32x16& acc, const f32x16& pa, LAS char* a0, LAS char* n0,
                                        u32x2 (&ra)[3], u32x2 (&rb_)[3], f32x4 (&cs)[2], LAS char* csp, LAS char* csn, u32 hoff,
                                        const char* dsrc, u32 dvoff, u32 ddst, int pp, u32 kfoff, bool kfact) {
  LAS char* a1 = a0 + 18 * 2048;
  const int w = c.w, l = c.l;
  float X[16], E[16];
  u32 P[8];
  u32x2 pf, k0;
  u32x4 uq[2];
#pragma unroll
  for (int ks = 0; ks < 36; ks++) {
    if (ks + 2 < 36) {
      LAS char* ap = (ks + 2 < 18 ? a0 + (ks + 2) * 2048 : a1 + (ks + 2 - 18) * 2048);
      ra[(ks + 2) % 3] = trread(ap); rb_[(ks + 2) % 3] = trread(ap + 512);
    }
    if (MODE != 2 && ks == 35) { ra[1] = trread(n0 + 2048); rb_[1] = trread(n0 + 2048 + 512); }
    if (ks == 34) lgwait();
    u32x4 A = {ra[ks % 3][0], ra[ks % 3][1], rb_[ks % 3][0], rb_[ks % 3][1]};
    if (ks == 0) { f32x16 z = {}; acc = mfma32(A, xf[0], z); }
    else acc = mfma32(A, xf[ks], acc);
    if (ks == 34) {
      vmwait<1>();
      if (MODE == 0) {
        PIN(pf);
        if (pp == 1 && !flag_ok(pf, kfact, c.tag)) flag_wait(c.ws, kfoff, kfact, c.tag);
      }
      bar();
      if (MODE == 0 && pp == 0 && w == 0 && l == 0) flag_set(c.ws, OFF_FK + c.b * 8, c.tag);
      if (MODE != 2) { ra[0] = trread(n0); rb_[0] = trread(n0 + 512); }
    }
    if (ks % 8 == 6 && ks / 8 < 3) cs[(ks / 8 + 1) & 1] = *(LAS f32x4*)(csp + (ks / 8 + 1) * 32);
    if (ks % 2 == 1 && ks / 2 < 16) {
      int e = ks / 2;
      float xx = __builtin_fmaf(c.nm, cs[(e >> 2) & 1][e & 3], pa[e]);
      PIN(xx);
      float tt = xx * c.cc;
      PIN(tt);
      X[e] = xx; E[e] = tt;
    }
    if (ks % 2 == 0 && ks >= 2 && ks / 2 - 1 < 16) {
      int e = ks / 2 - 1;
      float tt = __builtin_amdgcn_exp2f(E[e]);
      PIN(tt);
      E[e] = tt;
    }
    if (ks % 2 == 1 && ks >= 3 && (ks - 3) / 2 < 16) {
      int e = (ks - 3) / 2;
      float tt = 1.0f + E[e];
      PIN(tt);
      tt = __builtin_amdgcn_rcpf(tt);
      PIN(tt);
      E[e] = tt;
    }
    if (ks % 2 == 0 && ks >= 4 && (ks - 4) / 2 < 16) {
      int e = (ks - 4) / 2;
      float hh = X[e] * E[e];
      PIN(hh);
      X[e] = hh;
      if (e & 1) { u32 pq = pk(X[e - 1], X[e]); PIN(pq); P[e >> 1] = pq; }
    }
    if (ks == 19) __builtin_amdgcn_raw_buffer_store_b128((u32x4){P[0], P[1], P[2], P[3]}, c.wr, hoff, 0, SC);
    if (ks == 35) {
      __builtin_amdgcn_raw_buffer_store_b128((u32x4){P[4], P[5], P[6], P[7]}, c.wr, hoff + 512, 0, SC);
      if (MODE != 2) cs[0] = *(LAS f32x4*)(csn);
    }
    if (MODE != 2) {
      if (ks % 2 == 0 && ks / 2 < 9) glds(dsrc + (size_t)(ks / 2) * (64 * 9216), dvoff, ddst + (ks / 2) * 8192);
      if (MODE == 0 && ks == 1) ldg8s(pf, c.ws, kfoff);
    } else {
      if (ks % 2 == 0 && ks / 2 < 6) {
        int idx = w * 6 + ks / 2, cc = idx >> 2, part = idx & 3;
        u32x4 srd = mksrd(c.ssk + (size_t)(c.cb * 3 + (cc >> 2)) * 2048, 4096u);
        blds(srd, (cc & 3) * 2 + part * 1024 + l * 16, L_KC + cc * 4160 + 32 + part * 1024);
      }
      if (ks == 12 || ks == 14) {
        int idx = (w < 6 ? w : 5) * 2 + (ks - 12) / 2;
        ldg16s(uq[(ks - 12) / 2], c.ws + OFF_UR + (size_t)(c.cb * 3 + (idx >> 2)) * 4096 + (idx & 3) * 1024, l * 16);
      }
      if (ks == 16) ldg8(k0, c.ssk + (size_t)c.cb * 3 * 2048, (l < 8 ? l >> 2 : 2) * 4096);
      if (ks == 3) {
        if (w == 6 && l < 12) *(LAS u32x4*)(c.lds + L_KC + l * 4160) = (u32x4){0, 0, 0, 0};
        if (w < 3 && l < 32) *(LAS u32x4*)(c.lds + L_U + w * 4608 + 4096 + l * 16) = (u32x4){0, 0, 0, 0};
      }
      if (ks == 30) {
        vmwait<1>();
        PIN(uq[0]); PIN(uq[1]); PIN(k0);
        if (w < 6) {
          int idx = w * 2;
          *(LAS u32x4*)(c.lds + L_U + (idx >> 2) * 4608 + (idx & 3) * 1024 + l * 16) = uq[0];
          *(LAS u32x4*)(c.lds + L_U + (idx >> 2) * 4608 + ((idx & 3) + 1) * 1024 + l * 16) = uq[1];
        }
        if (w == 6 && l < 12) {
          int cp = l & 3;
          u32x4 c1 = {0, 0, 0, 0};
          if (cp == 1) c1[3] = k0[0] << 16;
          if (cp == 2) c1[3] = k0[0];
          if (cp == 3) { c1[2] = k0[0] << 16; c1[3] = (k0[0] >> 16) | (k0[1] << 16); }
          *(LAS u32x4*)(c.lds + L_KC + l * 4160 + 16) = c1;
        }
      }
    }
    sbar();
  }
}

__device__ __forceinline__ void g1_epi_final(const G1Ctx& c, const f32x16& pa, u32 csoff, u32 hoff) {
  u32 P[8];
#pragma unroll
  for (int q = 0; q < 4; q++) {
    f32x4 cs = *(LAS f32x4*)(c.lds + csoff + q * 32);
    float h[4];
#pragma unroll
    for (int i = 0; i < 4; i++) {
      float X = __builtin_fmaf(c.nm, cs[i], pa[4 * q + i]);
      h[i] = X * __builtin_amdgcn_rcpf(1.0f + __builtin_amdgcn_exp2f(X * c.cc));
    }
    P[2 * q] = pk(h[0], h[1]); P[2 * q + 1] = pk(h[2], h[3]);
  }
  __builtin_amdgcn_raw_buffer_store_b128((u32x4){P[0], P[1], P[2], P[3]}, c.wr, hoff, 0, SC);
  __builtin_amdgcn_raw_buffer_store_b128((u32x4){P[4], P[5], P[6], P[7]}, c.wr, hoff + 512, 0, SC);
}

__device__ __forceinline__ void kv_compute(const u32x2 (&kr)[11], const u32x2 (&vr)[8], const u32x4 (&sq)[2],
                                           __amdgpu_buffer_rsrc_t wr, int ch, int t) {
#pragma unroll
  for (int q = 0; q < 4; q++) {
    u32 sa = sq[q >> 1][(q & 1) * 2], sb = sq[q >> 1][(q & 1) * 2 + 1];
    float w0 = blo(sa), w1 = bhi(sa), w2 = blo(sb), w3 = bhi(sb);
    float kk[11], o[8];
#pragma unroll
    for (int i = 0; i < 11; i++) kk[i] = (q & 1) ? bhi(kr[i][q >> 1]) : blo(kr[i][q >> 1]);
#pragma unroll
    for (int i = 0; i < 8; i++) {
      float vv = (q & 1) ? bhi(vr[i][q >> 1]) : blo(vr[i][q >> 1]);
      o[i] = (kk[i] * w3 + kk[i + 1] * w2 + kk[i + 2] * w1 + kk[i + 3] * w0) * vv;
    }
    u32x4 r = {pk(o[7], o[6]), pk(o[5], o[4]), pk(o[3], o[2]), pk(o[1], o[0])};
    __builtin_amdgcn_raw_buffer_store_b128(r, wr, OFF_UR + (ch + q) * 4096 + 2 * (2040 - t), 0, SC);
    sbar();
  }
}

__device__ __forceinline__ void g1_phase(G1Ctx& c, const u16* x, const u16* k, const u16* v, const u16* sk, const u16* w1,
                                         int rt, int j, int kq TSARG) {
  LAS char* lds = c.lds;
  char* ws = c.ws;
  const int w = c.w, l = c.l, b = c.b, tid = w * 64 + l;
  const int tg = w & 3, fg = w >> 2, odd = j & 1, kh = l >> 5;
  u32x2 ep;
  ldg8s(ep, ws + OFF_E, 0);
  u32x4 sr[2], cq[3];
  const int stok = rt * 128 + j * 8 + w;
  {
    u32 o = stok * 1152 + (l < 36 ? l : 35) * 32;
    ldg16(sr[0], x, o); ldg16(sr[1], x, o + 16);
  }
  const int cuq = (b & 1) * 32 + (b >> 3);
  const int cu = cuq < 54 ? cuq : 53;
  const int ccl = 18 * kq + cu % 18, cpp = cu / 18;
  {
    u32 o = (cpp * 192 + (tid >> 3)) * 9216 + ccl * 128 + (tid & 7) * 16;
    ldg16(cq[0], w1, o); ldg16(cq[1], w1, o + 64 * 9216); ldg16(cq[2], w1, o + 128 * 9216);
  }
  {
    const char* xb = (const char*)x + (size_t)rt * 128 * 1152;
#pragma unroll
    for (int h = 0; h < 2; h++) {
      u32 vo = (16 * w + 8 * h + (l >> 3)) * 1152 + (((l & 7) ^ (4 * h + (l >> 4))) * 16);
#pragma unroll
      for (int kb = 0; kb < 9; kb++) glds(xb + kb * 128, vo, kb * 16384 + (16 * w + 8 * h) * 128);
    }
  }
  const char* w1b = (const char*)w1 + j * 576;
  const char* sb = w1b + (odd ? 0 : 512);
  const char* pb = w1b + (odd ? 64 : 0);
  const u32 svo = (l >> 2) * 9216 + (l & 3) * 16;
  glds(sb + (size_t)w * 16 * 9216, svo, L_S1 + w * 1024);
  if (w < 4) glds(sb + (size_t)(w + 8) * 16 * 9216, svo, L_S1 + (w + 8) * 1024);
  if (tid == 0) *(LAS u32*)(lds + L_CNT) = 0;
  vmwait<22>();
  PIN(ep); PIN(sr[0]); PIN(sr[1]);
  c.tag = (((u64)(u32)__builtin_amdgcn_readfirstlane(ep[1]) << 32) | (u32)__builtin_amdgcn_readfirstlane(ep[0])) + 1;
  {
    float e[16];
#pragma unroll
    for (int i = 0; i < 8; i++) { e[2 * i] = blo(sr[i >> 2][i & 3]); e[2 * i + 1] = bhi(sr[i >> 2][i & 3]); }
    float s = 0;
#pragma unroll
    for (int i = 0; i < 16; i++) s += e[i];
    if (l >= 36) s = 0;
    float mean = wave_sum(s) * (1.0f / 576.0f);
    float vv = 0;
#pragma unroll
    for (int i = 0; i < 16; i++) { float d = e[i] - mean; vv += d * d; }
    if (l >= 36) vv = 0;
    float var = rb(wave_sum(vv) * (1.0f / 576.0f));
    float sd = rb(__builtin_sqrtf(rb(var + 1e-5f)));
    __builtin_amdgcn_raw_buffer_store_b32(pk(mean, sd), c.wr, l == 0 ? OFF_STATS + stok * 4 : OOB, 0, SC);
  }
  vmwait<20>();
  PIN(cq[0]); PIN(cq[1]); PIN(cq[2]);
  {
    float a[8];
#pragma unroll
    for (int i = 0; i < 4; i++) {
      a[2 * i] = blo(cq[0][i]) + blo(cq[1][i]) + blo(cq[2][i]);
      a[2 * i + 1] = bhi(cq[0][i]) + bhi(cq[1][i]) + bhi(cq[2][i]);
    }
    float b0 = swp32add(a[0], a[4]), b1 = swp32add(a[1], a[5]), b2 = swp32add(a[2], a[6]), b3 = swp32add(a[3], a[7]);
    float c0 = swp16add(b0, b2), c1 = swp16add(b1, b3);
    c0 += dppf<0x128>(c0); c1 += dppf<0x128>(c1);
    float r = (l & 8) ? c1 : c0;
    *(LAS float*)(lds + L_CSS + w * 256 + (8 * (l & 7) + (l >> 3)) * 4) = r;
    lgwait(); bar();
    if (w == 0) {
      float s = 0;
#pragma unroll
      for (int i = 0; i < 8; i++) s += *(LAS float*)(lds + L_CSS + i * 256 + l * 4);
      __builtin_amdgcn_raw_buffer_store_b32(__float_as_uint(s), c.wr, cuq < 54 ? OFF_CS + (cpp * F_ + ccl * 64 + l) * 4 : OOB, 0, SC);
    }
  }
  const bool kvw = w >= 5 && b < 192;
  const int kid = 3 * b + w - 5, kch = (kid >> 6) * 64 + (l & 15) * 4, kt = (kid & 63) * 32 + (l >> 4) * 8;
  u32x2 kr[11], vr[8];
  u32x4 sq[2];
  {
    u32x4 rk = mksrd(k, S_ * D_ * 2), rv = mksrd(v, S_ * D_ * 2), rs = mksrd(sk, D_ * 8);
    u32 o = kvw ? kt * 1152 + kch * 2 : OOB;
#pragma unroll
    for (int i = 0; i < 11; i++) ldb8(kr[i], rk, o + (i - 3) * 1152);
#pragma unroll
    for (int i = 0; i < 8; i++) ldb8(vr[i], rv, o + i * 1152);
    u32 o2 = kvw ? kch * 8 : OOB;
    ldb16(sq[0], rs, o2); ldb16(sq[1], rs, o2 + 16);
  }
  vmwait<21>(); lgwait(); bar();
  if (tid == 0) flag_set(ws, OFF_FP + b * 8, c.tag);
  TS(1);
  u32x4 xf[36];
  {
    u32 row = tg * 32 + (l & 31);
#pragma unroll
    for (int q = 0; q < 4; q++) {
      u32 a = row * 128 + (((2 * q + (l >> 5)) ^ ((row >> 1) & 7)) * 16);
#pragma unroll
      for (int kb = 0; kb < 9; kb++) xf[kb * 4 + q] = *(LAS u32x4*)(lds + a + kb * 16384);
    }
  }
  lgwait(); bar();
  TS(2);
  const u32 pvo = (l >> 3) * 9216 + (((l & 7) ^ (((l >> 4) & 1) << 2)) * 16);
  const char* pw = pb + (size_t)w * 8 * 9216;
  f32x16 acc0 = {}, acc1 = {};
  if (w != 4) {
    const int w7 = w < 4 ? w : w - 1;
#pragma unroll
    for (int i = 0; i < 4; i++) {
      int p = w7 + 7 * i;
      if (i < 3 || p < 24) glds(sb + (size_t)(12 + p) * 16 * 9216, svo, p * 1024);
    }
#pragma unroll
    for (int i = 0; i < 11; i++) {
      int q = w7 + 7 * i;
      if (i < 10 || q < 72) glds(pb + (size_t)q * 8 * 9216, pvo, L_B1 + q * 1024);
    }
  }
  if (w < 4) {
    u32 sa = (8 * kh + ((l >> 2) & 3)) * 64 + ((l >> 4) & 1) * 32 + (l & 3) * 8;
    LAS char* s0 = lds + L_S1 + sa;
    LAS char* s1 = lds + sa - 12 * 1024;
    u32x2 ra[3], rb_[3];
    ra[0] = trread(s0); rb_[0] = trread(s0 + 256);
    ra[1] = trread(s0 + 1024); rb_[1] = trread(s0 + 1024 + 256);
#pragma unroll
    for (int ks = 0; ks < 36; ks++) {
      if (ks == 10) { vmwait<10>(); bar(); }
      if (ks + 2 < 36) {
        LAS char* ap = (ks + 2 < 12 ? s0 : s1) + (ks + 2) * 1024;
        ra[(ks + 2) % 3] = trread(ap); rb_[(ks + 2) % 3] = trread(ap + 256);
      }
      u32x4 A = {ra[ks % 3][0], ra[ks % 3][1], rb_[ks % 3][0], rb_[ks % 3][1]};
      acc1 = mfma32(A, xf[ks], acc1);
      sbar();
    }
    vmwait<0>();
  } else if (w == 4) {
    int pb_;
    if (l < 16) pb_ = 2 * (l >> 2) + (rt >> 3) + 8 * ((l & 3) * 8 + (rt & 7));
    else {
      int q = (l - 16) < 15 ? l - 16 : 14;
      int cq_ = (q / 5) * 18 + ((9 * j) >> 1) + q % 5 - 18 * kq;
      pb_ = 2 * kq + (cq_ >> 5) + 8 * (cq_ & 31);
    }
    bar();
    flag_wait(ws, OFF_FP + pb_ * 8, l < 31, c.tag);
    u32x4 stq = __builtin_amdgcn_raw_buffer_load_b128(c.wr, l < 32 ? OFF_STATS + rt * 512 + l * 16 : OOB, 0, SC);
    f32x4 cv[2];
#pragma unroll
    for (int i = 0; i < 2; i++) {
      int it = i * 64 + l;
      u32 o = it < 72 ? OFF_CS + j * 1152 + it * 16 : OOB;
      f32x4 p0 = __builtin_bit_cast(f32x4, __builtin_amdgcn_raw_buffer_load_b128(c.wr, o, 0, SC));
      f32x4 p1 = __builtin_bit_cast(f32x4, __builtin_amdgcn_raw_buffer_load_b128(c.wr, o + F_ * 4, 0, SC));
      f32x4 p2 = __builtin_bit_cast(f32x4, __builtin_amdgcn_raw_buffer_load_b128(c.wr, o + 2 * F_ * 4, 0, SC));
      cv[i] = p0 + p1 + p2;
    }
    if (l < 32) *(LAS u32x4*)(lds + L_ST + l * 16) = stq;
    *(LAS f32x4*)(lds + L_CS + l * 16) = cv[0];
    if (l < 8) *(LAS f32x4*)(lds + L_CS + 1024 + l * 16) = cv[1];
  } else {
    if (kvw) {
      vmwait<13>();
#pragma unroll
      for (int i = 0; i < 11; i++) PIN(kr[i]);
#pragma unroll
      for (int i = 0; i < 8; i++) PIN(vr[i]);
      PIN(sq[0]); PIN(sq[1]);
      kv_compute(kr, vr, sq, c.wr, kch, kt);
      vmwait<14>(); bar();
      vmwait<4>();
    } else {
      vmwait<10>(); bar();
      vmwait<0>();
    }
  }
  lgwait(); bar();
  TS(3);
  {
    u32 st = *(LAS u32*)(lds + L_ST + (tg * 32 + (l & 31)) * 4);
    c.nm = -blo(st);
    c.cc = -1.44269504088896f / bhi(st);
  }
  const u32 abase = (8 * kh + ((l >> 2) & 3)) * 128 + ((fg * 64 + ((l >> 4) & 1) * 32 + (l & 3) * 8) ^ (((l >> 3) & 1) << 6));
  const u32 hbase = OFF_H + (rt * 144 + 9 * j) * 8192 + (2 * tg + ((l >> 4) & 1)) * 1024 + (kh * 16 + (l & 15)) * 16;
  LAS char* aB0 = lds + abase;
  LAS char* aB1 = lds + L_B1 + abase;
  LAS char* csb = lds + L_CS + kh * 16;
  int ptile = odd ? 0 : 8;
  u32 hoff = (w < 4) ? hbase + ptile * 8192 : OOB;
  const int klo = (((c.cb * 3) >> 6) * 64) / 3, khi = (((c.cb * 3 + 2) >> 6) * 64 + 63) / 3;
  const bool kfact = klo + l <= khi;
  const u32 kfoff = OFF_FK + (kfact ? klo + l : klo) * 8;
  u32x2 ra[3], rb_[3];
  f32x4 cs[2];
  ra[0] = trread(aB1); rb_[0] = trread(aB1 + 512);
  ra[1] = trread(aB1 + 2048); rb_[1] = trread(aB1 + 2048 + 512);
  cs[0] = *(LAS f32x4*)(csb + ptile * 128);
#pragma unroll 1
  for (int pp = 0; pp < 2; pp++) {
    int t0 = odd + 4 * pp + fg, t1 = t0 + 2;
    g1_pair<0>(c, xf, acc0, acc1, aB1, aB0, ra, rb_, cs, csb + ptile * 128, csb + t0 * 128, hoff,
               pw + (2 * pp + 1) * 128, pvo, w * 1024, pp, kfoff, kfact);
    if (pp == 0) { TS(4); } else { TS(6); }
    hoff = hbase + t0 * 8192;
    ptile = t0;
    if (pp == 1) break;
    g1_pair<1>(c, xf, acc1, acc0, aB0, aB1, ra, rb_, cs, csb + t0 * 128, csb + t1 * 128, hoff,
               pw + 2 * 128, pvo, L_B1 + w * 1024, pp, kfoff, kfact);
    TS(5);
    ptile = t1;
    hoff = hbase + t1 * 8192;
  }
  g1_pair<2>(c, xf, acc1, acc0, aB0, aB1, ra, rb_, cs, csb + ptile * 128, csb, hoff, pw, pvo, 0, 1, kfoff, kfact);
  TS(7);
  ptile += 2;
  g1_epi_final(c, acc1, L_CS + ptile * 128 + kh * 16, hbase + ptile * 8192);
}

static constexpr u32 L_RED = L_B1;
static constexpr u32 L_EX = 73728;
static constexpr u32 L_OWN = 0;
static constexpr u32 L_Y = 18432;

__device__ __forceinline__ void ldb16s(u32x4& d, u32x4 srd, u32 voff) {
  asm volatile("buffer_load_dwordx4 %0, %1, %2, 0 offen sc1" : "=v"(d) : "v"(voff), "s"(srd) : "memory");
}
template <int P>
__device__ __forceinline__ void w2_piece(const char* wsrc, const u32 (&cvo)[5], u32 ldst, int w, int l) {
  if (P < 4) glds(wsrc, cvo[P < 4 ? P : 0], ldst + (w * 4 + P) * 1024);
  else if (l < 32) glds(wsrc, cvo[4], ldst + (32 + (w >> 1)) * 1024 + (w & 1) * 512);
}
template <int P>
__device__ __forceinline__ void w2_piece10(const char* wsrc, const u32 (&cvo)[5], int w, int l) {
  w2_piece<P % 5>(wsrc + (P / 5) * 73728, cvo, (P / 5) * 36864, w, l);
}
template <bool GL>
__device__ __forceinline__ void conv_res(f32x4 (&acc)[8], u32x4 (&A)[8], u32x4 (&B)[8], LAS char* ap, LAS char* bp,
                                         const char* wsrc, const u32 (&cvo)[5], int w, int l) {
#pragma unroll
  for (int q = 0; q < 8; q++) {
#pragma unroll
    for (int i = 7 - q; i >= 0; i--) acc[i + q] = mfma16(A[i], B[q], acc[i + q]);
    B[q] = *(LAS u32x4*)(bp + 512 * (7 - q));
    u32x2 lo = *(LAS u32x2*)(ap + 512 * (7 - q)), hi = *(LAS u32x2*)(ap + 512 * (7 - q) + 8);
    A[7 - q] = (u32x4){lo[0], lo[1], hi[0], hi[1]};
    if (GL) {
      if (q == 0) { w2_piece10<0>(wsrc, cvo, w, l); w2_piece10<1>(wsrc, cvo, w, l); }
      if (q == 1) { w2_piece10<2>(wsrc, cvo, w, l); w2_piece10<3>(wsrc, cvo, w, l); }
      if (q == 2) w2_piece10<4>(wsrc, cvo, w, l);
      if (q == 3) w2_piece10<5>(wsrc, cvo, w, l);
      if (q == 4) w2_piece10<6>(wsrc, cvo, w, l);
      if (q == 5) w2_piece10<7>(wsrc, cvo, w, l);
      if (q == 6) w2_piece10<8>(wsrc, cvo, w, l);
      if (q == 7) w2_piece10<9>(wsrc, cvo, w, l);
    }
    sbar();
  }
}

struct G2Ctx {
  const char* wsrc;
  const char* hsrc;
  char* ws;
  LAS char* apA;
  LAS char* apB;
  u32x4 srd, srx;
  u64 tag;
  u32 cvo[5];
  u32 fyoff, yoff0, yoff1, xoff0, xoff2;
  bool fyact;
  int w, l, b;
};
struct G2Tail { u32x2 pf; u32x4 fh[2], yq[2]; u32x2 xv[3]; };

template <int I>
__device__ __forceinline__ void g2_issue1(const char* wsrc, const char* hsrc, const u32 (&cvo)[5], u32 ldst, int w, int l, u32x4 (&hb)[4]) {
  if (I % 2 == 0) w2_piece<I / 2>(wsrc, cvo, ldst, w, l);
  if (I == 1) ldg16s(hb[0], hsrc, l * 16);
  if (I == 3) ldg16so(hb[1], hsrc, l * 16);
  if (I == 5) ldg16s(hb[2], hsrc + 8192, l * 16);
  if (I == 7) ldg16so(hb[3], hsrc + 8192, l * 16);
}
template <int IT, int SS>
__device__ __forceinline__ void g2_aread(const G2Ctx& c, u32x2& ra, u32x2& rb_) {
  LAS char* p = ((IT & 2) ? c.apB : c.apA) + (IT & 1) * 36864 + (SS / 9) * 9216 + (SS % 9) * 32;
  ra = trread(p); rb_ = trread(p + 2304);
}
template <int IT, int SS>
__device__ __forceinline__ void g2_step(const G2Ctx& c, G2Tail& t, u32x4 (&hb)[4][4], f32x4 (&acc)[2][9], u32x2 (&ra)[3], u32x2 (&rb_)[3]) {
  constexpr int u = SS / 9, n = SS % 9;
  const int w = c.w, l = c.l;
  if (SS + 2 < 18) g2_aread<IT, (SS + 2) % 18>(c, ra[(SS + 2) % 3], rb_[(SS + 2) % 3]);
  else if (SS == 17 && IT < 8) g2_aread<IT + 1, 1>(c, ra[1], rb_[1]);
  if (SS == 16) lgwait();
  u32x4 A = {ra[SS % 3][0], ra[SS % 3][1], rb_[SS % 3][0], rb_[SS % 3][1]};
  acc[0][n] = mfma16(A, hb[IT & 3][u * 2], acc[0][n]);
  acc[1][n] = mfma16(A, hb[IT & 3][u * 2 + 1], acc[1][n]);
  if (SS == 16 && IT < 8) {
    if (IT == 0) vmwait<4 + 3 + 5 + 8>();
    else if (IT + 3 < 9) vmwait<9 + 8>();
    else if (IT + 2 < 9) vmwait<9>();
    else vmwait<0>();
    bar();
    PIN(hb[(IT + 1) & 3][0]); PIN(hb[(IT + 1) & 3][1]); PIN(hb[(IT + 1) & 3][2]); PIN(hb[(IT + 1) & 3][3]);
    if (IT == 1 && c.b < 192 && w == 0 && l == 0) flag_set(c.ws, OFF_FY + c.b * 8, c.tag);
    g2_aread<IT + 1, 0>(c, ra[0], rb_[0]);
  }
  if (IT + 3 < 9) {
    const char* ws_ = c.wsrc + (IT + 3) * 73728;
    const char* hs_ = c.hsrc + (IT + 3) * 16384;
    if (SS % 2 == 1 && SS < 16) g2_issue1<(SS % 2 == 1 ? SS / 2 : 0)>(ws_, hs_, c.cvo, ((IT + 3) & 3) * 36864, w, l, hb[(IT + 3) & 3]);
    if (SS == 17) g2_issue1<8>(ws_, hs_, c.cvo, ((IT + 3) & 3) * 36864, w, l, hb[(IT + 3) & 3]);
  }
  if (IT == 7 && SS == 17) {
    ldg8s(t.pf, c.ws, c.fyoff);
    u32 o = (c.b == 0 && w == 7) ? OFF_FH + l * 32 : OOB;
    ldb16s(t.fh[0], c.srd, o); ldb16s(t.fh[1], c.srd, o + 16);
  }
  if (IT == 8 && SS == 12) {
    vmwait<0>();
    PIN(t.pf); PIN(t.fh[0]); PIN(t.fh[1]);
    if (!flag_ok(t.pf, c.fyact, c.tag)) flag_wait(c.ws, c.fyoff, c.fyact, c.tag);
    if (c.b == 0 && w == 7) {
      const u32 lo = (u32)c.tag, hi = (u32)(c.tag >> 32);
      bool ok = t.fh[0][0] == lo && t.fh[0][1] == hi && t.fh[0][2] == lo && t.fh[0][3] == hi &&
                t.fh[1][0] == lo && t.fh[1][1] == hi && t.fh[1][2] == lo && t.fh[1][3] == hi;
      if (!__all(ok))
        for (int i = 0; i < 4; i++) flag_wait(c.ws, OFF_FH + (i * 64 + l) * 8, true, c.tag);
      if (l == 0) flag_set(c.ws, OFF_E, c.tag);
    }
    ldb16s(t.yq[0], c.srd, c.yoff0); ldb16s(t.yq[1], c.srd, c.yoff1);
    ldb8(t.xv[0], c.srx, c.xoff0); ldb8(t.xv[1], c.srx, c.xoff0 + 32); ldb8(t.xv[2], c.srx, c.xoff2);
  }
  sbar();
}
template <int IT, int SS>
__device__ __forceinline__ void g2_steps(const G2Ctx& c, G2Tail& t, u32x4 (&hb)[4][4], f32x4 (&acc)[2][9], u32x2 (&ra)[3], u32x2 (&rb_)[3]) {
  g2_step<IT, SS>(c, t, hb, acc, ra, rb_);
  if constexpr (SS < 17) g2_steps<IT, SS + 1>(c, t, hb, acc, ra, rb_);
  else if constexpr (IT < 8) g2_steps<IT + 1, 0>(c, t, hb, acc, ra, rb_);
}
__device__ __forceinline__ void fin4(f32x4 s, u32x2 xv, u32x2 yv, float m, float rs, u16* op) {
  float res[4];
#pragma unroll
  for (int e = 0; e < 4; e++) {
    float xe = (e & 1) ? bhi(xv[e >> 1]) : blo(xv[e >> 1]);
    float ye = (e & 1) ? bhi(yv[e >> 1]) : blo(yv[e >> 1]);
    float o = rb(s[e] * rs);
    float g = rb(o * __builtin_amdgcn_rcpf(1.0f + __builtin_amdgcn_exp2f(o * -1.44269504088896f)));
    float xn = rb(rb(xe - m) * rs);
    float sv = rb(xn * ye);
    res[e] = xe + rb(sv * g);
  }
  *(u32x2*)op = (u32x2){pk(res[0], res[1]), pk(res[2], res[3])};
}
__device__ __forceinline__ f32x4 up4(u32 a, u32 b) { return (f32x4){blo(a), bhi(a), blo(b), bhi(b)}; }

__device__ __forceinline__ void conv_g2(G1Ctx& g, const u16* x, const u16* Dv, const u16* w2, u16* out, int rt, int kq, int dn TSARG) {
  LAS char* lds = g.lds;
  char* ws = g.ws;
  const int w = g.w, l = g.l, b = g.b, cb = g.cb;
  const int tg = w & 3, kh = w >> 2, xbit = rt >> 3, i7 = rt & 7, lg = l >> 4;
  G2Ctx c;
  c.ws = ws; c.tag = g.tag; c.w = w; c.l = l; c.b = b;
#pragma unroll
  for (int i = 0; i < 5; i++) {
    u32 idx = i < 4 ? (w * 4 + i) * 64 + l : (32 + (w >> 1)) * 64 + (w & 1) * 32 + (l & 31);
    u32 row = idx / 18, c16 = idx - row * 18;
    c.cvo[i] = ((row >> 6) * 576 + (row & 63)) * 1152 + c16 * 16;
  }
  c.wsrc = (const char*)w2 + (size_t)(1152 * kq) * 1152 + dn * 288;
  c.hsrc = ws + OFF_H + (size_t)((rt * 144 + 36 * kq + 18 * kh) * 8 + 2 * tg) * 1024;
  f32x4 ca[8];
#pragma unroll
  for (int i = 0; i < 8; i++) ca[i] = (f32x4){0, 0, 0, 0};
  const int ch = (w < 3) ? 0 : (w < 6) ? 1 : 2;
  const int r0 = (w == 0 || w == 4 || w == 6) ? 0 : (w == 1 || w == 5) ? 3 : (w == 7) ? 4 : 6;
  const int nr = (w >= 6) ? 4 : (w == 2 || w == 3) ? 2 : 3;
  const int lb = l & 15, cp = (lb + 1) & 3;
  LAS char* ap = lds + L_KC + (ch * 4 + cp) * 4160 + 2 * (lb + 1 - cp) + 16 * lg + 64 * r0;
  LAS char* bp = lds + L_U + ch * 4608 + 2 * (240 - 16 * lb + 8 * lg) + 64 * r0;
  u32x4 A[8], B[8];
  if (b < 192) {
#pragma unroll
    for (int i = 7; i >= 0; i--) {
      B[7 - i] = *(LAS u32x4*)(bp + 512 * i);
      u32x2 lo = *(LAS u32x2*)(ap + 512 * i), hi = *(LAS u32x2*)(ap + 512 * i + 8);
      A[i] = (u32x4){lo[0], lo[1], hi[0], hi[1]};
    }
    conv_res<true>(ca, A, B, ap + 64, bp + 64, c.wsrc, c.cvo, w, l);
  } else {
    w2_piece10<0>(c.wsrc, c.cvo, w, l); w2_piece10<1>(c.wsrc, c.cvo, w, l); w2_piece10<2>(c.wsrc, c.cvo, w, l);
    w2_piece10<3>(c.wsrc, c.cvo, w, l); w2_piece10<4>(c.wsrc, c.cvo, w, l); w2_piece10<5>(c.wsrc, c.cvo, w, l);
    w2_piece10<6>(c.wsrc, c.cvo, w, l); w2_piece10<7>(c.wsrc, c.cvo, w, l); w2_piece10<8>(c.wsrc, c.cvo, w, l);
    w2_piece10<9>(c.wsrc, c.cvo, w, l);
  }
  vmwait<10>();
  if (l == 0) {
    u32 old = __hip_atomic_fetch_add((LAS u32*)(lds + L_CNT), 1u, __ATOMIC_RELAXED, __HIP_MEMORY_SCOPE_WORKGROUP);
    if (old == 7) flag_set(ws, OFF_FH + b * 8, g.tag);
  }
  TS(8);
  if (b < 192) {
#pragma unroll 1
    for (int r = 1; r < nr; r++) conv_res<false>(ca, A, B, ap + 64 * (r + 1), bp + 64 * (r + 1), c.wsrc, c.cvo, w, l);
  }
  TS(9);
  u32x2 uu[3];
#pragma unroll
  for (int i = 0; i < 3; i++) {
    int u = w * 3 + i;
    uu[i] = *(LAS u32x2*)(lds + L_U + (u >> 3) * 4608 + 2 * (2044 - (256 * (u & 7) + 16 * lb + 4 * lg)));
  }
  const u32 fhoff = OFF_FH + ((b & 7) + 8 * ((l & 3) * 8 + i7)) * 8;
  u32x2 pf;
  ldg8s(pf, ws, fhoff);
  lgwait(); bar();
#pragma unroll
  for (int i = 0; i < 8; i++) *(LAS f32x4*)(lds + L_RED + w * 8192 + i * 1024 + l * 16) = ca[i];
  lgwait(); bar();
  TS(10);
  f32x4 ys[3];
#pragma unroll
  for (int i = 0; i < 3; i++) {
    int u = w * 3 + i, c2 = u >> 3;
    LAS char* rp = lds + L_RED + c2 * 3 * 8192 + (u & 7) * 1024 + l * 16;
    ys[i] = *(LAS f32x4*)(rp) + *(LAS f32x4*)(rp + 8192);
    if (c2 < 2) ys[i] += *(LAS f32x4*)(rp + 2 * 8192);
  }
  vmwait<0>();
  PIN(pf);
  if (!flag_ok(pf, true, g.tag)) flag_wait(ws, fhoff, true, g.tag);
  TS(11);
  u32x4 hb[4][4];
#pragma unroll
  for (int it = 0; it < 3; it++) {
    ldg16s(hb[it][0], c.hsrc + it * 16384, l * 16); ldg16so(hb[it][1], c.hsrc + it * 16384, l * 16);
    ldg16s(hb[it][2], c.hsrc + it * 16384 + 8192, l * 16); ldg16so(hb[it][3], c.hsrc + it * 16384 + 8192, l * 16);
  }
#pragma unroll
  for (int i = 0; i < 3; i++) {
    int u = w * 3 + i, chn = cb * 3 + (u >> 3);
    u32 dw = ((CAS const u32*)Dv)[chn >> 1];
    float dv = (chn & 1) ? bhi(dw) : blo(dw);
    float o0 = rb(ys[i][0]) + rb(bhi(uu[i][1]) * dv), o1 = rb(ys[i][1]) + rb(blo(uu[i][1]) * dv);
    float o2 = rb(ys[i][2]) + rb(bhi(uu[i][0]) * dv), o3 = rb(ys[i][3]) + rb(blo(uu[i][0]) * dv);
    u32 o = b < 192 ? OFF_Y2 + chn * 4096 + (256 * (u & 7) + 16 * lb + 4 * lg) * 2 : OOB;
    __builtin_amdgcn_raw_buffer_store_b64((u32x2){pk(o0, o1), pk(o2, o3)}, g.wr, o, 0, SC);
  }
  lgwait(); bar();
  w2_piece<0>(c.wsrc + 2 * 73728, c.cvo, 73728, w, l); w2_piece<1>(c.wsrc + 2 * 73728, c.cvo, 73728, w, l);
  w2_piece<2>(c.wsrc + 2 * 73728, c.cvo, 73728, w, l); w2_piece<3>(c.wsrc + 2 * 73728, c.cvo, 73728, w, l);
  w2_piece<4>(c.wsrc + 2 * 73728, c.cvo, 73728, w, l);
  c.apA = lds + (kh * 64 + 16 * (lg >> 1) + 4 * (lg & 1) + ((l >> 2) & 3)) * 288 + (l & 3) * 8;
  c.apB = c.apA + 73728;
  c.srd = mksrd(ws, WS_END);
  c.srx = mksrd(x, S_ * D_ * 2);
  const int p = w & 3, tt = w >> 2;
  const int tok = rt * 128 + kq * 32 + tt * 16 + lb, d0 = dn * 144 + 4 * lg;
  c.fyact = l < 48;
  c.fyoff = OFF_FY + (48 * dn + (l < 48 ? l : 0)) * 8;
  {
    u32 yb = OFF_Y2 + (dn * 144 + w * 16 + (l >> 2)) * 4096 + (rt * 128 + kq * 32) * 2 + (l & 3) * 16;
    c.yoff0 = yb;
    c.yoff1 = w == 0 ? yb + 128 * 4096 : OOB;
    c.xoff0 = tok * 1152 + (d0 + p * 32) * 2;
    c.xoff2 = p == 0 ? tok * 1152 + (d0 + 128) * 2 : OOB;
  }
  f32x4 acc[2][9];
#pragma unroll
  for (int n = 0; n < 9; n++) { acc[0][n] = (f32x4){0, 0, 0, 0}; acc[1][n] = (f32x4){0, 0, 0, 0}; }
  G2Tail t;
  u32x2 ra[3], rb_[3];
  g2_aread<0, 0>(c, ra[0], rb_[0]); g2_aread<0, 1>(c, ra[1], rb_[1]);
  vmwait<8 + 3 + 5>();
  PIN(hb[0][0]); PIN(hb[0][1]); PIN(hb[0][2]); PIN(hb[0][3]);
  TS(12);
  g2_steps<0, 0>(c, t, hb, acc, ra, rb_);
  TS(13);
#pragma unroll
  for (int n = 0; n < 9; n++) *(LAS f32x4*)(lds + L_EX + w * 9216 + n * 1024 + l * 16) = kh ? acc[0][n] : acc[1][n];
  lgwait(); bar();
  f32x4 r[9];
#pragma unroll
  for (int n = 0; n < 9; n++) r[n] = (kh ? acc[1][n] : acc[0][n]) + *(LAS f32x4*)(lds + L_EX + (w ^ 4) * 9216 + n * 1024 + l * 16);
  {
    int ob = 2 * tg + xbit + 8 * (dn * 8 + i7);
    u32 po = tg != kq ? OFF_P + (ob * 3 + ((kq - tg - 1) & 3)) * 9216 + kh * 4608 : OOB;
#pragma unroll
    for (int q = 0; q < 4; q++)
      __builtin_amdgcn_raw_buffer_store_b128((u32x4){pk(r[2 * q][0], r[2 * q][1]), pk(r[2 * q][2], r[2 * q][3]),
                                                     pk(r[2 * q + 1][0], r[2 * q + 1][1]), pk(r[2 * q + 1][2], r[2 * q + 1][3])},
                                             g.wr, po + q * 1024 + l * 16, 0, SC);
    __builtin_amdgcn_raw_buffer_store_b64((u32x2){pk(r[8][0], r[8][1]), pk(r[8][2], r[8][3])}, g.wr, po + 4096 + l * 8, 0, SC);
    if (tg == kq) {
#pragma unroll
      for (int n = 0; n < 9; n++) *(LAS f32x4*)(lds + L_OWN + kh * 9216 + n * 1024 + l * 16) = r[n];
    }
  }
  vmwait<5>();
  PIN(t.yq[0]); PIN(t.yq[1]); PIN(t.xv[0]); PIN(t.xv[1]); PIN(t.xv[2]);
  *(LAS u32x4*)(lds + L_Y + (w * 16 + (l >> 2)) * 96 + (l & 3) * 16) = t.yq[0];
  if (w == 0) *(LAS u32x4*)(lds + L_Y + (128 + (l >> 2)) * 96 + (l & 3) * 16) = t.yq[1];
  vmwait<0>(); lgwait(); bar();
  if (w == 0 && l == 0) flag_set(ws, OFF_FX + b * 8, g.tag);
  TS(14);
  const u32 fxoff = OFF_FX + (2 * ((kq + 1 + (l < 3 ? l : 0)) & 3) + xbit + 8 * (dn * 8 + i7)) * 8;
  flag_wait(ws, fxoff, l < 3, g.tag);
  TS(15);
  u32x4 pq[3];
  u32x2 p8[3];
  {
    u32 po = OFF_P + b * 3 * 9216 + tt * 4608;
    u32 o8 = p == 0 ? po + 4096 + l * 8 : OOB;
#pragma unroll
    for (int s = 0; s < 3; s++) {
      pq[s] = __builtin_amdgcn_raw_buffer_load_b128(g.wr, po + s * 9216 + p * 1024 + l * 16, 0, SC);
      p8[s] = __builtin_amdgcn_raw_buffer_load_b64(g.wr, o8 + s * 9216, 0, SC);
    }
  }
  u32 st = *(LAS u32*)(lds + L_ST + (kq * 32 + tt * 16 + lb) * 4);
  float m = blo(st), rs = 1.0f / bhi(st);
  LAS char* yp = lds + L_Y + (4 * lg + ((l >> 2) & 3)) * 96 + tt * 32 + (l & 3) * 8;
  LAS char* op = lds + L_OWN + tt * 9216 + l * 16;
  u32x2 y0 = trread(yp + p * 32 * 96), y1 = trread(yp + (p * 32 + 16) * 96), y2 = trread(yp + 128 * 96);
  f32x4 s0 = *(LAS f32x4*)(op + 2 * p * 1024), s1 = *(LAS f32x4*)(op + (2 * p + 1) * 1024), s2 = *(LAS f32x4*)(op + 8 * 1024);
#pragma unroll
  for (int s = 0; s < 3; s++) { s0 += up4(pq[s][0], pq[s][1]); s1 += up4(pq[s][2], pq[s][3]); }
  u16* o = out + tok * D_ + d0;
  fin4(s0, t.xv[0], y0, m, rs, o + p * 32);
  fin4(s1, t.xv[1], y1, m, rs, o + p * 32 + 16);
  if (p == 0) {
#pragma unroll
    for (int s = 0; s < 3; s++) s2 += up4(p8[s][0], p8[s][1]);
    fin4(s2, t.xv[2], y2, m, rs, o + 128);
  }
}

__global__ void __launch_bounds__(512, 2) k_fused(const u16* x, const u16* k, const u16* v, const u16* sk, const u16* ssk, const u16* Dv,
                                                  const u16* w1, const u16* w2, u16* out, char* ws) {
  extern __shared__ __attribute__((aligned(1024))) char smem[];
  const int tid = threadIdx.x, b = blockIdx.x;
  const int xc = b & 7, i = b >> 3;
  const int rt = (xc & 1) * 8 + (i & 7), kq = xc >> 1, dn = i >> 3;
  G1Ctx c;
  c.lds = (LAS char*)smem;
  c.ws = ws;
  c.ssk = ssk;
  c.wr = mkrsrc(ws, WS_END);
  c.b = b; c.cb = b < 192 ? b : 191;
  c.w = __builtin_amdgcn_readfirstlane(tid >> 6); c.l = tid & 63;
  g1_phase(c, x, k, v, sk, w1, rt, 4 * kq + dn, kq TSPASS);
  conv_g2(c, x, Dv, w2, out, rt, kq, dn TSPASS);
}

extern "C" void kernel_launch(void* const* d_in, const int* in_sizes, int n_in, void* d_out, int out_size, void* d_ws, size_t ws_size,
                              hipStream_t stream) {
  (void)in_sizes; (void)n_in; (void)out_size; (void)ws_size;
  (void)hipFuncSetAttribute((const void*)k_fused, hipFuncAttributeMaxDynamicSharedMemorySize, LDS_BYTES);
  k_fused<<<256, 512, LDS_BYTES, stream>>>((const u16*)d_in[0], (const u16*)d_in[1], (const u16*)d_in[2], (const u16*)d_in[3],
                                           (const u16*)d_in[4], (const u16*)d_in[5], (const u16*)d_in[7], (const u16*)d_in[8],
                                           (u16*)d_out, (char*)d_ws);
}
```

```cpp
#define TFIX 4400
#include <hip/hip_runtime.h>
#include <stdint.h>

typedef unsigned short u16;
typedef unsigned int u32;
typedef unsigned long long u64;
#define LAS __attribute__((address_space(3)))
#define CAS __attribute__((address_space(4)))
typedef __attribute__((ext_vector_type(8))) __bf16 bf16x8;
typedef __attribute__((ext_vector_type(2))) __bf16 bf16x2;
typedef __attribute__((ext_vector_type(2))) float f32x2;
typedef __attribute__((ext_vector_type(4))) float f32x4;
typedef __attribute__((ext_vector_type(16))) float f32x16;
typedef __attribute__((ext_vector_type(2))) u32 u32x2;
typedef __attribute__((ext_vector_type(4))) u32 u32x4;
typedef __attribute__((ext_vector_type(4))) short v4i16;

static constexpr int S_ = 2048, D_ = 576, F_ = 4608;
static constexpr u32 OFF_E = 0;
static constexpr u32 OFF_FP = 4096;
static constexpr u32 OFF_FK = OFF_FP + 2048;
static constexpr u32 OFF_FH = OFF_FK + 2048;
static constexpr u32 OFF_FY = OFF_FH + 2048;
static constexpr u32 OFF_FX = OFF_FY + 2048;
static constexpr u32 OFF_STATS = 65536;
static constexpr u32 OFF_CS = OFF_STATS + 8192;
static constexpr u32 OFF_UR = 131072;
static constexpr u32 OFF_Y2 = OFF_UR + 576 * 4096;
static constexpr u32 OFF_H = OFF_Y2 + 576 * 4096;
static constexpr u32 OFF_P = OFF_H + 2048 * 4608 * 2;
static constexpr u32 WS_END = OFF_P + 256 * 3 * 9216;
static constexpr int LDS_BYTES = 163840;
static constexpr int SC = 16;
static constexpr u32 OOB = 0xF0000000u;
static constexpr u32 L_B1 = 73728, L_S1 = 147456, L_CSS = 159744, L_CS = 161792, L_ST = 162944, L_CNT = 163456;

#define TS(i)
#define TSARG
#define TSPASS

__device__ __forceinline__ float blo(u32 v) { return __uint_as_float(v << 16); }
__device__ __forceinline__ float bhi(u32 v) { return __uint_as_float(v & 0xffff0000u); }
__device__ __forceinline__ u32 pk(float lo, float hi) {
  f32x2 t = {lo, hi};
  return __builtin_bit_cast(u32, __builtin_convertvector(t, bf16x2));
}
__device__ __forceinline__ float rb(float x) { return blo(pk(x, x)); }
template <int C> __device__ __forceinline__ float dppf(float v) {
  return __int_as_float(__builtin_amdgcn_update_dpp(0, __float_as_int(v), C, 0xf, 0xf, false));
}
__device__ __forceinline__ float swp32add(float a, float b) {
  auto r = __builtin_amdgcn_permlane32_swap(__float_as_uint(a), __float_as_uint(b), false, false);
  return __uint_as_float(r[0]) + __uint_as_float(r[1]);
}
__device__ __forceinline__ float swp16add(float a, float b) {
  auto r = __builtin_amdgcn_permlane16_swap(__float_as_uint(a), __float_as_uint(b), false, false);
  return __uint_as_float(r[0]) + __uint_as_float(r[1]);
}
__device__ __forceinline__ float wave_sum(float v) {
  v += dppf<0x128>(v); v += dppf<0x124>(v); v += dppf<0x122>(v); v += dppf<0x121>(v);
  v = swp16add(v, v);
  return swp32add(v, v);
}
__device__ __forceinline__ void glds(const void* sbase, u32 voff, u32 ldsdst) {
  asm volatile("s_mov_b32 m0, %2\n\ts_nop 0\n\tglobal_load_lds_dwordx4 %0, %1" ::"v"(voff), "s"(sbase), "s"(ldsdst) : "memory");
}
__device__ __forceinline__ void blds(u32x4 srd, u32 voff, u32 ldsdst) {
  asm volatile("s_mov_b32 m0, %2\n\ts_nop 0\n\tbuffer_load_dwordx4 %0, %1, 0 offen lds" ::"v"(voff), "s"(srd), "s"(ldsdst) : "memory");
}
__device__ __forceinline__ void ldg16(u32x4& d, const void* sbase, u32 voff) {
  asm volatile("global_load_dwordx4 %0, %1, %2" : "=v"(d) : "v"(voff), "s"(sbase) : "memory");
}
__device__ __forceinline__ void ldg16s(u32x4& d, const void* sbase, u32 voff) {
  asm volatile("global_load_dwordx4 %0, %1, %2 sc1" : "=v"(d) : "v"(voff), "s"(sbase) : "memory");
}
__device__ __forceinline__ void ldg16so(u32x4& d, const void* sbase, u32 voff) {
  asm volatile("global_load_dwordx4 %0, %1, %2 offset:1024 sc1" : "=v"(d) : "v"(voff), "s"(sbase) : "memory");
}
__device__ __forceinline__ void ldg8(u32x2& d, const void* sbase, u32 voff) {
  asm volatile("global_load_dwordx2 %0, %1, %2" : "=v"(d) : "v"(voff), "s"(sbase) : "memory");
}
__device__ __forceinline__ void ldg8s(u32x2& d, const void* sbase, u32 voff) {
  asm volatile("global_load_dwordx2 %0, %1, %2 sc1" : "=v"(d) : "v"(voff), "s"(sbase) : "memory");
}
__device__ __forceinline__ void ldb8(u32x2& d, u32x4 srd, u32 voff) {
  asm volatile("buffer_load_dwordx2 %0, %1, %2, 0 offen" : "=v"(d) : "v"(voff), "s"(srd) : "memory");
}
__device__ __forceinline__ void ldb16(u32x4& d, u32x4 srd, u32 voff) {
  asm volatile("buffer_load_dwordx4 %0, %1, %2, 0 offen" : "=v"(d) : "v"(voff), "s"(srd) : "memory");
}
template <int N> __device__ __forceinline__ void vmwait() { asm volatile("s_waitcnt vmcnt(%0)" ::"n"(N) : "memory"); }
__device__ __forceinline__ void lgwait() { asm volatile("s_waitcnt lgkmcnt(0)" ::: "memory"); }
__device__ __forceinline__ void bar() { asm volatile("s_barrier" ::: "memory"); }
#define PIN(x) asm volatile("" : "+v"(x))
__device__ __forceinline__ void sbar() { __builtin_amdgcn_sched_barrier(0); }
__device__ __forceinline__ __amdgpu_buffer_rsrc_t mkrsrc(const void* p, u32 bytes) {
  return __builtin_amdgcn_make_buffer_rsrc((void*)p, 0, bytes, 0x00020000);
}
__device__ __forceinline__ u32x4 mksrd(const void* p, u32 bytes) {
  u64 b = (u64)p;
  return (u32x4){(u32)b, (u32)(b >> 32) & 0xffffu, bytes, 0x00020000u};
}
__device__ __forceinline__ f32x4 mfma16(u32x4 a, u32x4 b, f32x4 c) {
  return __builtin_amdgcn_mfma_f32_16x16x32_bf16(__builtin_bit_cast(bf16x8, a), __builtin_bit_cast(bf16x8, b), c, 0, 0, 0);
}
__device__ __forceinline__ f32x16 mfma32(u32x4 a, u32x4 b, f32x16 c) {
  return __builtin_amdgcn_mfma_f32_32x32x16_bf16(__builtin_bit_cast(bf16x8, a), __builtin_bit_cast(bf16x8, b), c, 0, 0, 0);
}
__device__ __forceinline__ u32x2 trread(LAS char* p) {
  return __builtin_bit_cast(u32x2, __builtin_amdgcn_ds_read_tr16_b64_v4i16((LAS v4i16*)p));
}
__device__ __forceinline__ void flag_set(char* ws, u32 off, u64 tag) {
  __hip_atomic_store((__attribute__((address_space(1))) u64*)(ws + off), tag, __ATOMIC_RELAXED, __HIP_MEMORY_SCOPE_AGENT);
}
__device__ __forceinline__ void flag_wait(char* ws, u32 off, bool act, u64 tag) {
  for (int spins = 0; spins < 4000000; spins++) {
    u64 f = __hip_atomic_load((__attribute__((address_space(1))) u64*)(ws + off), __ATOMIC_RELAXED, __HIP_MEMORY_SCOPE_AGENT);
    if (__all(f == tag || !act)) break;
    __builtin_amdgcn_s_sleep(1);
  }
  asm volatile("" ::: "memory");
}
__device__ __forceinline__ bool flag_ok(u32x2 f, bool act, u64 tag) {
  return __all(!act || (f[0] == (u32)tag && f[1] == (u32)(tag >> 32)));
}

static constexpr u32 L_KC = L_B1;
static constexpr u32 L_U = L_B1 + 12 * 4160;

struct G1Ctx {
  LAS char* lds;
  char* ws;
  const u16* ssk;
  __amdgpu_buffer_rsrc_t wr;
  float nm, cc;
  u64 tag;
  int b, cb, w, l;
};

template <int MODE>
__device__ __forceinline__ void g1_pair(const G1Ctx& c, const u32x4 (&xf)[36], f32x16& acc, const f32x16& pa, LAS char* a0, LAS char* n0,
                                        u32x2 (&ra)[3], u32x2 (&rb_)[3], f32x4 (&cs)[2], LAS char* csp, LAS char* csn, u32 hoff,
                                        const char* dsrc, u32 dvoff, u32 ddst, int pp, u32 kfoff, bool kfact) {
  LAS char* a1 = a0 + 18 * 2048;
  const int w = c.w, l = c.l;
  float X[16], E[16];
  u32 P[8];
  u32x2 pf, k0;
  u32x4 uq[2];
#pragma unroll
  for (int ks = 0; ks < 36; ks++) {
    if (ks + 2 < 36) {
      LAS char* ap = (ks + 2 < 18 ? a0 + (ks + 2) * 2048 : a1 + (ks + 2 - 18) * 2048);
      ra[(ks + 2) % 3] = trread(ap); rb_[(ks + 2) % 3] = trread(ap + 512);
    }
    if (MODE != 2 && ks == 35) { ra[1] = trread(n0 + 2048); rb_[1] = trread(n0 + 2048 + 512); }
    if (ks == 34) lgwait();
    u32x4 A = {ra[ks % 3][0], ra[ks % 3][1], rb_[ks % 3][0], rb_[ks % 3][1]};
    if (ks == 0) { f32x16 z = {}; acc = mfma32(A, xf[0], z); }
    else acc = mfma32(A, xf[ks], acc);
    if (ks == 34) {
      vmwait<1>();
      if (MODE == 0) {
        PIN(pf);
        if (pp == 1 && !flag_ok(pf, kfact, c.tag)) flag_wait(c.ws, kfoff, kfact, c.tag);
      }
      bar();
      if (MODE == 0 && pp == 0 && w == 0 && l == 0) flag_set(c.ws, OFF_FK + c.b * 8, c.tag);
      if (MODE != 2) { ra[0] = trread(n0); rb_[0] = trread(n0 + 512); }
    }
    if (ks % 8 == 6 && ks / 8 < 3) cs[(ks / 8 + 1) & 1] = *(LAS f32x4*)(csp + (ks / 8 + 1) * 32);
    if (ks % 2 == 1 && ks / 2 < 16) {
      int e = ks / 2;
      float xx = __builtin_fmaf(c.nm, cs[(e >> 2) & 1][e & 3], pa[e]);
      PIN(xx);
      float tt = xx * c.cc;
      PIN(tt);
      X[e] = xx; E[e] = tt;
    }
    if (ks % 2 == 0 && ks >= 2 && ks / 2 - 1 < 16) {
      int e = ks / 2 - 1;
      float tt = __builtin_amdgcn_exp2f(E[e]);
      PIN(tt);
      E[e] = tt;
    }
    if (ks % 2 == 1 && ks >= 3 && (ks - 3) / 2 < 16) {
      int e = (ks - 3) / 2;
      float tt = 1.0f + E[e];
      PIN(tt);
      tt = __builtin_amdgcn_rcpf(tt);
      PIN(tt);
      E[e] = tt;
    }
    if (ks % 2 == 0 && ks >= 4 && (ks - 4) / 2 < 16) {
      int e = (ks - 4) / 2;
      float hh = X[e] * E[e];
      PIN(hh);
      X[e] = hh;
      if (e & 1) { u32 pq = pk(X[e - 1], X[e]); PIN(pq); P[e >> 1] = pq; }
    }
    if (ks == 19) __builtin_amdgcn_raw_buffer_store_b128((u32x4){P[0], P[1], P[2], P[3]}, c.wr, hoff, 0, SC);
    if (ks == 35) {
      __builtin_amdgcn_raw_buffer_store_b128((u32x4){P[4], P[5], P[6], P[7]}, c.wr, hoff + 512, 0, SC);
      if (MODE != 2) cs[0] = *(LAS f32x4*)(csn);
    }
    if (MODE != 2) {
      if (ks % 2 == 0 && ks / 2 < 9) glds(dsrc + (size_t)(ks / 2) * (64 * 9216), dvoff, ddst + (ks / 2) * 8192);
      if (MODE == 0 && ks == 1) ldg8s(pf, c.ws, kfoff);
    } else {
      if (ks % 2 == 0 && ks / 2 < 6) {
        int idx = w * 6 + ks / 2, cc = idx >> 2, part = idx & 3;
        u32x4 srd = mksrd(c.ssk + (size_t)(c.cb * 3 + (cc >> 2)) * 2048, 4096u);
        blds(srd, (cc & 3) * 2 + part * 1024 + l * 16, L_KC + cc * 4160 + 32 + part * 1024);
      }
      if (ks == 12 || ks == 14) {
        int idx = (w < 6 ? w : 5) * 2 + (ks - 12) / 2;
        ldg16s(uq[(ks - 12) / 2], c.ws + OFF_UR + (size_t)(c.cb * 3 + (idx >> 2)) * 4096 + (idx & 3) * 1024, l * 16);
      }
      if (ks == 16) ldg8(k0, c.ssk + (size_t)c.cb * 3 * 2048, (l < 8 ? l >> 2 : 2) * 4096);
      if (ks == 3) {
        if (w == 6 && l < 12) *(LAS u32x4*)(c.lds + L_KC + l * 4160) = (u32x4){0, 0, 0, 0};
        if (w < 3 && l < 32) *(LAS u32x4*)(c.lds + L_U + w * 4608 + 4096 + l * 16) = (u32x4){0, 0, 0, 0};
      }
      if (ks == 30) {
        vmwait<1>();
        PIN(uq[0]); PIN(uq[1]); PIN(k0);
        if (w < 6) {
          int idx = w * 2;
          *(LAS u32x4*)(c.lds + L_U + (idx >> 2) * 4608 + (idx & 3) * 1024 + l * 16) = uq[0];
          *(LAS u32x4*)(c.lds + L_U + (idx >> 2) * 4608 + ((idx & 3) + 1) * 1024 + l * 16) = uq[1];
        }
        if (w == 6 && l < 12) {
          int cp = l & 3;
          u32x4 c1 = {0, 0, 0, 0};
          if (cp == 1) c1[3] = k0[0] << 16;
          if (cp == 2) c1[3] = k0[0];
          if (cp == 3) { c1[2] = k0[0] << 16; c1[3] = (k0[0] >> 16) | (k0[1] << 16); }
          *(LAS u32x4*)(c.lds + L_KC + l * 4160 + 16) = c1;
        }
      }
    }
    sbar();
  }
}

__device__ __forceinline__ void g1_epi_final(const G1Ctx& c, const f32x16& pa, u32 csoff, u32 hoff) {
  u32 P[8];
#pragma unroll
  for (int q = 0; q < 4; q++) {
    f32x4 cs = *(LAS f32x4*)(c.lds + csoff + q * 32);
    float h[4];
#pragma unroll
    for (int i = 0; i < 4; i++) {
      float X = __builtin_fmaf(c.nm, cs[i], pa[4 * q + i]);
      h[i] = X * __builtin_amdgcn_rcpf(1.0f + __builtin_amdgcn_exp2f(X * c.cc));
    }
    P[2 * q] = pk(h[0], h[1]); P[2 * q + 1] = pk(h[2], h[3]);
  }
  __builtin_amdgcn_raw_buffer_store_b128((u32x4){P[0], P[1], P[2], P[3]}, c.wr, hoff, 0, SC);
  __builtin_amdgcn_raw_buffer_store_b128((u32x4){P[4], P[5], P[6], P[7]}, c.wr, hoff + 512, 0, SC);
}

__device__ __forceinline__ void kv_compute(const u32x2 (&kr)[11], const u32x2 (&vr)[8], const u32x4 (&sq)[2],
                                           __amdgpu_buffer_rsrc_t wr, int ch, int t) {
#pragma unroll
  for (int q = 0; q < 4; q++) {
    u32 sa = sq[q >> 1][(q & 1) * 2], sb = sq[q >> 1][(q & 1) * 2 + 1];
    float w0 = blo(sa), w1 = bhi(sa), w2 = blo(sb), w3 = bhi(sb);
    float kk[11], o[8];
#pragma unroll
    for (int i = 0; i < 11; i++) kk[i] = (q & 1) ? bhi(kr[i][q >> 1]) : blo(kr[i][q >> 1]);
#pragma unroll
    for (int i = 0; i < 8; i++) {
      float vv = (q & 1) ? bhi(vr[i][q >> 1]) : blo(vr[i][q >> 1]);
      o[i] = (kk[i] * w3 + kk[i + 1] * w2 + kk[i + 2] * w1 + kk[i + 3] * w0) * vv;
    }
    u32x4 r = {pk(o[7], o[6]), pk(o[5], o[4]), pk(o[3], o[2]), pk(o[1], o[0])};
    __builtin_amdgcn_raw_buffer_store_b128(r, wr, OFF_UR + (ch + q) * 4096 + 2 * (2040 - t), 0, SC);
    sbar();
  }
}

__device__ __forceinline__ void g1_phase(G1Ctx& c, const u16* x, const u16* k, const u16* v, const u16* sk, const u16* w1,
                                         int rt, int j, int kq TSARG) {
  LAS char* lds = c.lds;
  char* ws = c.ws;
  const int w = c.w, l = c.l, b = c.b, tid = w * 64 + l;
  const int tg = w & 3, fg = w >> 2, odd = j & 1, kh = l >> 5;
  u32x2 ep;
  ldg8s(ep, ws + OFF_E, 0);
  u32x4 sr[2], cq[3];
  const int stok = rt * 128 + j * 8 + w;
  {
    u32 o = stok * 1152 + (l < 36 ? l : 35) * 32;
    ldg16(sr[0], x, o); ldg16(sr[1], x, o + 16);
  }
  const int cuq = (b & 1) * 32 + (b >> 3);
  const int cu = cuq < 54 ? cuq : 53;
  const int ccl = 18 * kq + cu % 18, cpp = cu / 18;
  {
    u32 o = (cpp * 192 + (tid >> 3)) * 9216 + ccl * 128 + (tid & 7) * 16;
    ldg16(cq[0], w1, o); ldg16(cq[1], w1, o + 64 * 9216); ldg16(cq[2], w1, o + 128 * 9216);
  }
  {
    const char* xb = (const char*)x + (size_t)rt * 128 * 1152;
#pragma unroll
    for (int h = 0; h < 2; h++) {
      u32 vo = (16 * w + 8 * h + (l >> 3)) * 1152 + (((l & 7) ^ (4 * h + (l >> 4))) * 16);
#pragma unroll
      for (int kb = 0; kb < 9; kb++) glds(xb + kb * 128, vo, kb * 16384 + (16 * w + 8 * h) * 128);
    }
  }
  const char* w1b = (const char*)w1 + j * 576;
  const char* sb = w1b + (odd ? 0 : 512);
  const char* pb = w1b + (odd ? 64 : 0);
  const u32 svo = (l >> 2) * 9216 + (l & 3) * 16;
  glds(sb + (size_t)w * 16 * 9216, svo, L_S1 + w * 1024);
  if (w < 4) glds(sb + (size_t)(w + 8) * 16 * 9216, svo, L_S1 + (w + 8) * 1024);
  if (tid == 0) *(LAS u32*)(lds + L_CNT) = 0;
  vmwait<22>();
  PIN(ep); PIN(sr[0]); PIN(sr[1]);
  c.tag = (((u64)(u32)__builtin_amdgcn_readfirstlane(ep[1]) << 32) | (u32)__builtin_amdgcn_readfirstlane(ep[0])) + 1;
  {
    float e[16];
#pragma unroll
    for (int i = 0; i < 8; i++) { e[2 * i] = blo(sr[i >> 2][i & 3]); e[2 * i + 1] = bhi(sr[i >> 2][i & 3]); }
    float s = 0;
#pragma unroll
    for (int i = 0; i < 16; i++) s += e[i];
    if (l >= 36) s = 0;
    float mean = wave_sum(s) * (1.0f / 576.0f);
    float vv = 0;
#pragma unroll
    for (int i = 0; i < 16; i++) { float d = e[i] - mean; vv += d * d; }
    if (l >= 36) vv = 0;
    float var = rb(wave_sum(vv) * (1.0f / 576.0f));
    float sd = rb(__builtin_sqrtf(rb(var + 1e-5f)));
    __builtin_amdgcn_raw_buffer_store_b32(pk(mean, sd), c.wr, l == 0 ? OFF_STATS + stok * 4 : OOB, 0, SC);
  }
  vmwait<20>();
  PIN(cq[0]); PIN(cq[1]); PIN(cq[2]);
  {
    float a[8];
#pragma unroll
    for (int i = 0; i < 4; i++) {
      a[2 * i] = blo(cq[0][i]) + blo(cq[1][i]) + blo(cq[2][i]);
      a[2 * i + 1] = bhi(cq[0][i]) + bhi(cq[1][i]) + bhi(cq[2][i]);
    }
    float b0 = swp32add(a[0], a[4]), b1 = swp32add(a[1], a[5]), b2 = swp32add(a[2], a[6]), b3 = swp32add(a[3], a[7]);
    float c0 = swp16add(b0, b2), c1 = swp16add(b1, b3);
    c0 += dppf<0x128>(c0); c1 += dppf<0x128>(c1);
    float r = (l & 8) ? c1 : c0;
    *(LAS float*)(lds + L_CSS + w * 256 + (8 * (l & 7) + (l >> 3)) * 4) = r;
    lgwait(); bar();
    if (w == 0) {
      float s = 0;
#pragma unroll
      for (int i = 0; i < 8; i++) s += *(LAS float*)(lds + L_CSS + i * 256 + l * 4);
      __builtin_amdgcn_raw_buffer_store_b32(__float_as_uint(s), c.wr, cuq < 54 ? OFF_CS + (cpp * F_ + ccl * 64 + l) * 4 : OOB, 0, SC);
    }
  }
  const bool kvw = w >= 5 && b < 192;
  const int kid = 3 * b + w - 5, kch = (kid >> 6) * 64 + (l & 15) * 4, kt = (kid & 63) * 32 + (l >> 4) * 8;
  u32x2 kr[11], vr[8];
  u32x4 sq[2];
  {
    u32x4 rk = mksrd(k, S_ * D_ * 2), rv = mksrd(v, S_ * D_ * 2), rs = mksrd(sk, D_ * 8);
    u32 o = kvw ? kt * 1152 + kch * 2 : OOB;
#pragma unroll
    for (int i = 0; i < 11; i++) ldb8(kr[i], rk, o + (i - 3) * 1152);
#pragma unroll
    for (int i = 0; i < 8; i++) ldb8(vr[i], rv, o + i * 1152);
    u32 o2 = kvw ? kch * 8 : OOB;
    ldb16(sq[0], rs, o2); ldb16(sq[1], rs, o2 + 16);
  }
  vmwait<21>(); lgwait(); bar();
  if (tid == 0) flag_set(ws, OFF_FP + b * 8, c.tag);
  TS(1);
  u32x4 xf[36];
  {
    u32 row = tg * 32 + (l & 31);
#pragma unroll
    for (int q = 0; q < 4; q++) {
      u32 a = row * 128 + (((2 * q + (l >> 5)) ^ ((row >> 1) & 7)) * 16);
#pragma unroll
      for (int kb = 0; kb < 9; kb++) xf[kb * 4 + q] = *(LAS u32x4*)(lds + a + kb * 16384);
    }
  }
  lgwait(); bar();
  TS(2);
  const u32 pvo = (l >> 3) * 9216 + (((l & 7) ^ (((l >> 4) & 1) << 2)) * 16);
  const char* pw = pb + (size_t)w * 8 * 9216;
  f32x16 acc0 = {}, acc1 = {};
  if (w != 4) {
    const int w7 = w < 4 ? w : w - 1;
#pragma unroll
    for (int i = 0; i < 4; i++) {
      int p = w7 + 7 * i;
      if (i < 3 || p < 24) glds(sb + (size_t)(12 + p) * 16 * 9216, svo, p * 1024);
    }
#pragma unroll
    for (int i = 0; i < 11; i++) {
      int q = w7 + 7 * i;
      if (i < 10 || q < 72) glds(pb + (size_t)q * 8 * 9216, pvo, L_B1 + q * 1024);
    }
  }
  if (w < 4) {
    u32 sa = (8 * kh + ((l >> 2) & 3)) * 64 + ((l >> 4) & 1) * 32 + (l & 3) * 8;
    LAS char* s0 = lds + L_S1 + sa;
    LAS char* s1 = lds + sa - 12 * 1024;
    u32x2 ra[3], rb_[3];
    ra[0] = trread(s0); rb_[0] = trread(s0 + 256);
    ra[1] = trread(s0 + 1024); rb_[1] = trread(s0 + 1024 + 256);
#pragma unroll
    for (int ks = 0; ks < 36; ks++) {
      if (ks == 10) { vmwait<10>(); bar(); }
      if (ks + 2 < 36) {
        LAS char* ap = (ks + 2 < 12 ? s0 : s1) + (ks + 2) * 1024;
        ra[(ks + 2) % 3] = trread(ap); rb_[(ks + 2) % 3] = trread(ap + 256);
      }
      u32x4 A = {ra[ks % 3][0], ra[ks % 3][1], rb_[ks % 3][0], rb_[ks % 3][1]};
      acc1 = mfma32(A, xf[ks], acc1);
      sbar();
    }
    vmwait<0>();
  } else if (w == 4) {
    int pb_;
    if (l < 16) pb_ = 2 * (l >> 2) + (rt >> 3) + 8 * ((l & 3) * 8 + (rt & 7));
    else {
      int q = (l - 16) < 15 ? l - 16 : 14;
      int cq_ = (q / 5) * 18 + ((9 * j) >> 1) + q % 5 - 18 * kq;
      pb_ = 2 * kq + (cq_ >> 5) + 8 * (cq_ & 31);
    }
    bar();
    flag_wait(ws, OFF_FP + pb_ * 8, l < 31, c.tag);
    u32x4 stq = __builtin_amdgcn_raw_buffer_load_b128(c.wr, l < 32 ? OFF_STATS + rt * 512 + l * 16 : OOB, 0, SC);
    f32x4 cv[2];
#pragma unroll
    for (int i = 0; i < 2; i++) {
      int it = i * 64 + l;
      u32 o = it < 72 ? OFF_CS + j * 1152 + it * 16 : OOB;
      f32x4 p0 = __builtin_bit_cast(f32x4, __builtin_amdgcn_raw_buffer_load_b128(c.wr, o, 0, SC));
      f32x4 p1 = __builtin_bit_cast(f32x4, __builtin_amdgcn_raw_buffer_load_b128(c.wr, o + F_ * 4, 0, SC));
      f32x4 p2 = __builtin_bit_cast(f32x4, __builtin_amdgcn_raw_buffer_load_b128(c.wr, o + 2 * F_ * 4, 0, SC));
      cv[i] = p0 + p1 + p2;
    }
    if (l < 32) *(LAS u32x4*)(lds + L_ST + l * 16) = stq;
    *(LAS f32x4*)(lds + L_CS + l * 16) = cv[0];
    if (l < 8) *(LAS f32x4*)(lds + L_CS + 1024 + l * 16) = cv[1];
  } else {
    if (kvw) {
      vmwait<13>();
#pragma unroll
      for (int i = 0; i < 11; i++) PIN(kr[i]);
#pragma unroll
      for (int i = 0; i < 8; i++) PIN(vr[i]);
      PIN(sq[0]); PIN(sq[1]);
      kv_compute(kr, vr, sq, c.wr, kch, kt);
      vmwait<14>(); bar();
      vmwait<4>();
    } else {
      vmwait<10>(); bar();
      vmwait<0>();
    }
  }
  lgwait(); bar();
  TS(3);
  {
    u32 st = *(LAS u32*)(lds + L_ST + (tg * 32 + (l & 31)) * 4);
    c.nm = -blo(st);
    c.cc = -1.44269504088896f / bhi(st);
  }
  const u32 abase = (8 * kh + ((l >> 2) & 3)) * 128 + ((fg * 64 + ((l >> 4) & 1) * 32 + (l & 3) * 8) ^ (((l >> 3) & 1) << 6));
  const u32 hbase = OFF_H + (rt * 144 + 9 * j) * 8192 + (2 * tg + ((l >> 4) & 1)) * 1024 + (kh * 16 + (l & 15)) * 16;
  LAS char* aB0 = lds + abase;
  LAS char* aB1 = lds + L_B1 + abase;
  LAS char* csb = lds + L_CS + kh * 16;
  int ptile = odd ? 0 : 8;
  u32 hoff = (w < 4) ? hbase + ptile * 8192 : OOB;
  const int klo = (((c.cb * 3) >> 6) * 64) / 3, khi = (((c.cb * 3 + 2) >> 6) * 64 + 63) / 3;
  const bool kfact = klo + l <= khi;
  const u32 kfoff = OFF_FK + (kfact ? klo + l : klo) * 8;
  u32x2 ra[3], rb_[3];
  f32x4 cs[2];
  ra[0] = trread(aB1); rb_[0] = trread(aB1 + 512);
  ra[1] = trread(aB1 + 2048); rb_[1] = trread(aB1 + 2048 + 512);
  cs[0] = *(LAS f32x4*)(csb + ptile * 128);
#pragma unroll 1
  for (int pp = 0; pp < 2; pp++) {
    int t0 = odd + 4 * pp + fg, t1 = t0 + 2;
    g1_pair<0>(c, xf, acc0, acc1, aB1, aB0, ra, rb_, cs, csb + ptile * 128, csb + t0 * 128, hoff,
               pw + (2 * pp + 1) * 128, pvo, w * 1024, pp, kfoff, kfact);
    if (pp == 0) { TS(4); } else { TS(6); }
    hoff = hbase + t0 * 8192;
    ptile = t0;
    if (pp == 1) break;
    g1_pair<1>(c, xf, acc1, acc0, aB0, aB1, ra, rb_, cs, csb + t0 * 128, csb + t1 * 128, hoff,
               pw + 2 * 128, pvo, L_B1 + w * 1024, pp, kfoff, kfact);
    TS(5);
    ptile = t1;
    hoff = hbase + t1 * 8192;
  }
  g1_pair<2>(c, xf, acc1, acc0, aB0, aB1, ra, rb_, cs, csb + ptile * 128, csb, hoff, pw, pvo, 0, 1, kfoff, kfact);
  TS(7);
  ptile += 2;
  g1_epi_final(c, acc1, L_CS + ptile * 128 + kh * 16, hbase + ptile * 8192);
}

static constexpr u32 L_RED = L_B1;
static constexpr u32 L_EX = 73728;
static constexpr u32 L_OWN = 0;
static constexpr u32 L_Y = 18432;

__device__ __forceinline__ void ldb16s(u32x4& d, u32x4 srd, u32 voff) {
  asm volatile("buffer_load_dwordx4 %0, %1, %2, 0 offen sc1" : "=v"(d) : "v"(voff), "s"(srd) : "memory");
}
template <int P>
__device__ __forceinline__ void w2_piece(const char* wsrc, const u32 (&cvo)[5], u32 ldst, int w, int l) {
  if (P < 4) glds(wsrc, cvo[P < 4 ? P : 0], ldst + (w * 4 + P) * 1024);
  else if (l < 32) glds(wsrc, cvo[4], ldst + (32 + (w >> 1)) * 1024 + (w & 1) * 512);
}
template <int P>
__device__ __forceinline__ void w2_piece10(const char* wsrc, const u32 (&cvo)[5], int w, int l) {
  w2_piece<P % 5>(wsrc + (P / 5) * 73728, cvo, (P / 5) * 36864, w, l);
}
template <bool GL>
__device__ __forceinline__ void conv_res(f32x4 (&acc)[8], u32x4 (&A)[8], u32x4 (&B)[8], LAS char* ap, LAS char* bp,
                                         const char* wsrc, const u32 (&cvo)[5], int w, int l) {
#pragma unroll
  for (int q = 0; q < 8; q++) {
#pragma unroll
    for (int i = 7 - q; i >= 0; i--) acc[i + q] = mfma16(A[i], B[q], acc[i + q]);
    B[q] = *(LAS u32x4*)(bp + 512 * (7 - q));
    u32x2 lo = *(LAS u32x2*)(ap + 512 * (7 - q)), hi = *(LAS u32x2*)(ap + 512 * (7 - q) + 8);
    A[7 - q] = (u32x4){lo[0], lo[1], hi[0], hi[1]};
    if (GL) {
      if (q == 0) { w2_piece10<0>(wsrc, cvo, w, l); w2_piece10<1>(wsrc, cvo, w, l); }
      if (q == 1) { w2_piece10<2>(wsrc, cvo, w, l); w2_piece10<3>(wsrc, cvo, w, l); }
      if (q == 2) w2_piece10<4>(wsrc, cvo, w, l);
      if (q == 3) w2_piece10<5>(wsrc, cvo, w, l);
      if (q == 4) w2_piece10<6>(wsrc, cvo, w, l);
      if (q == 5) w2_piece10<7>(wsrc, cvo, w, l);
      if (q == 6) w2_piece10<8>(wsrc, cvo, w, l);
      if (q == 7) w2_piece10<9>(wsrc, cvo, w, l);
    }
    sbar();
  }
}

struct G2Ctx {
  const char* wsrc;
  const char* hsrc;
  char* ws;
  LAS char* apA;
  LAS char* apB;
  u32x4 srd, srx;
  u64 tag;
  u32 cvo[5];
  u32 fyoff, yoff0, yoff1, xoff0, xoff2;
  bool fyact;
  int w, l, b;
};
struct G2Tail { u32x2 pf; u32x4 fh[2], yq[2]; u32x2 xv[3]; };

template <int I>
__device__ __forceinline__ void g2_issue1(const char* wsrc, const char* hsrc, const u32 (&cvo)[5], u32 ldst, int w, int l, u32x4 (&hb)[4]) {
  if (I % 2 == 0) w2_piece<I / 2>(wsrc, cvo, ldst, w, l);
  if (I == 1) ldg16s(hb[0], hsrc, l * 16);
  if (I == 3) ldg16so(hb[1], hsrc, l * 16);
  if (I == 5) ldg16s(hb[2], hsrc + 8192, l * 16);
  if (I == 7) ldg16so(hb[3], hsrc + 8192, l * 16);
}
template <int IT, int SS>
__device__ __forceinline__ void g2_aread(const G2Ctx& c, u32x2& ra, u32x2& rb_) {
  LAS char* p = ((IT & 2) ? c.apB : c.apA) + (IT & 1) * 36864 + (SS / 9) * 9216 + (SS % 9) * 32;
  ra = trread(p); rb_ = trread(p + 2304);
}
template <int IT, int SS>
__device__ __forceinline__ void g2_step(const G2Ctx& c, G2Tail& t, u32x4 (&hb)[4][4], f32x4 (&acc)[2][9], u32x2 (&ra)[3], u32x2 (&rb_)[3]) {
  constexpr int u = SS / 9, n = SS % 9;
  const int w = c.w, l = c.l;
  if (SS + 2 < 18) g2_aread<IT, (SS + 2) % 18>(c, ra[(SS + 2) % 3], rb_[(SS + 2) % 3]);
  else if (SS == 17 && IT < 8) g2_aread<IT + 1, 1>(c, ra[1], rb_[1]);
  if (SS == 16) lgwait();
  u32x4 A = {ra[SS % 3][0], ra[SS % 3][1], rb_[SS % 3][0], rb_[SS % 3][1]};
  acc[0][n] = mfma16(A, hb[IT & 3][u * 2], acc[0][n]);
  acc[1][n] = mfma16(A, hb[IT & 3][u * 2 + 1], acc[1][n]);
  if (SS == 16 && IT < 8) {
    if (IT == 0) vmwait<4 + 3 + 5 + 8>();
    else if (IT + 3 < 9) vmwait<9 + 8>();
    else if (IT + 2 < 9) vmwait<9>();
    else vmwait<0>();
    bar();
    PIN(hb[(IT + 1) & 3][0]); PIN(hb[(IT + 1) & 3][1]); PIN(hb[(IT + 1) & 3][2]); PIN(hb[(IT + 1) & 3][3]);
    if (IT == 1 && c.b < 192 && w == 0 && l == 0) flag_set(c.ws, OFF_FY + c.b * 8, c.tag);
    g2_aread<IT + 1, 0>(c, ra[0], rb_[0]);
  }
  if (IT + 3 < 9) {
    const char* ws_ = c.wsrc + (IT + 3) * 73728;
    const char* hs_ = c.hsrc + (IT + 3) * 16384;
    if (SS % 2 == 1 && SS < 16) g2_issue1<(SS % 2 == 1 ? SS / 2 : 0)>(ws_, hs_, c.cvo, ((IT + 3) & 3) * 36864, w, l, hb[(IT + 3) & 3]);
    if (SS == 17) g2_issue1<8>(ws_, hs_, c.cvo, ((IT + 3) & 3) * 36864, w, l, hb[(IT + 3) & 3]);
  }
  if (IT == 7 && SS == 17) {
    ldg8s(t.pf, c.ws, c.fyoff);
    u32 o = (c.b == 0 && w == 7) ? OFF_FH + l * 32 : OOB;
    ldb16s(t.fh[0], c.srd, o); ldb16s(t.fh[1], c.srd, o + 16);
  }
  if (IT == 8 && SS == 12) {
    vmwait<0>();
    PIN(t.pf); PIN(t.fh[0]); PIN(t.fh[1]);
    if (!flag_ok(t.pf, c.fyact, c.tag)) flag_wait(c.ws, c.fyoff, c.fyact, c.tag);
    if (c.b == 0 && w == 7) {
      const u32 lo = (u32)c.tag, hi = (u32)(c.tag >> 32);
      bool ok = t.fh[0][0] == lo && t.fh[0][1] == hi && t.fh[0][2] == lo && t.fh[0][3] == hi &&
                t.fh[1][0] == lo && t.fh[1][1] == hi && t.fh[1][2] == lo && t.fh[1][3] == hi;
      if (!__all(ok))
        for (int i = 0; i < 4; i++) flag_wait(c.ws, OFF_FH + (i * 64 + l) * 8, true, c.tag);
      if (l == 0) flag_set(c.ws, OFF_E, c.tag);
    }
    ldb16s(t.yq[0], c.srd, c.yoff0); ldb16s(t.yq[1], c.srd, c.yoff1);
    ldb8(t.xv[0], c.srx, c.xoff0); ldb8(t.xv[1], c.srx, c.xoff0 + 32); ldb8(t.xv[2], c.srx, c.xoff2);
  }
  sbar();
}
template <int IT, int SS>
__device__ __forceinline__ void g2_steps(const G2Ctx& c, G2Tail& t, u32x4 (&hb)[4][4], f32x4 (&acc)[2][9], u32x2 (&ra)[3], u32x2 (&rb_)[3]) {
  g2_step<IT, SS>(c, t, hb, acc, ra, rb_);
  if constexpr (SS < 17) g2_steps<IT, SS + 1>(c, t, hb, acc, ra, rb_);
  else if constexpr (IT < 8) g2_steps<IT + 1, 0>(c, t, hb, acc, ra, rb_);
}
__device__ __forceinline__ void fin4(f32x4 s, u32x2 xv, u32x2 yv, float m, float rs, u16* op) {
  float res[4];
#pragma unroll
  for (int e = 0; e < 4; e++) {
    float xe = (e & 1) ? bhi(xv[e >> 1]) : blo(xv[e >> 1]);
    float ye = (e & 1) ? bhi(yv[e >> 1]) : blo(yv[e >> 1]);
    float o = rb(s[e] * rs);
    float g = rb(o * __builtin_amdgcn_rcpf(1.0f + __builtin_amdgcn_exp2f(o * -1.44269504088896f)));
    float xn = rb(rb(xe - m) * rs);
    float sv = rb(xn * ye);
    res[e] = xe + rb(sv * g);
  }
  { u32x2 r_ = {pk(res[0], res[1]), pk(res[2], res[3])}; asm volatile("global_store_dwordx2 %0, %1, off sc0 sc1" :: "v"(op), "v"(r_) : "memory"); }
}
__device__ __forceinline__ f32x4 up4(u32 a, u32 b) { return (f32x4){blo(a), bhi(a), blo(b), bhi(b)}; }

__device__ __forceinline__ void conv_g2(G1Ctx& g, const u16* x, const u16* Dv, const u16* w2, u16* out, int rt, int kq, int dn TSARG) {
  LAS char* lds = g.lds;
  char* ws = g.ws;
  const int w = g.w, l = g.l, b = g.b, cb = g.cb;
  const int tg = w & 3, kh = w >> 2, xbit = rt >> 3, i7 = rt & 7, lg = l >> 4;
  G2Ctx c;
  c.ws = ws; c.tag = g.tag; c.w = w; c.l = l; c.b = b;
#pragma unroll
  for (int i = 0; i < 5; i++) {
    u32 idx = i < 4 ? (w * 4 + i) * 64 + l : (32 + (w >> 1)) * 64 + (w & 1) * 32 + (l & 31);
    u32 row = idx / 18, c16 = idx - row * 18;
    c.cvo[i] = ((row >> 6) * 576 + (row & 63)) * 1152 + c16 * 16;
  }
  c.wsrc = (const char*)w2 + (size_t)(1152 * kq) * 1152 + dn * 288;
  c.hsrc = ws + OFF_H + (size_t)((rt * 144 + 36 * kq + 18 * kh) * 8 + 2 * tg) * 1024;
  f32x4 ca[8];
#pragma unroll
  for (int i = 0; i < 8; i++) ca[i] = (f32x4){0, 0, 0, 0};
  const int ch = (w < 3) ? 0 : (w < 6) ? 1 : 2;
  const int r0 = (w == 0 || w == 4 || w == 6) ? 0 : (w == 1 || w == 5) ? 3 : (w == 7) ? 4 : 6;
  const int nr = (w >= 6) ? 4 : (w == 2 || w == 3) ? 2 : 3;
  const int lb = l & 15, cp = (lb + 1) & 3;
  LAS char* ap = lds + L_KC + (ch * 4 + cp) * 4160 + 2 * (lb + 1 - cp) + 16 * lg + 64 * r0;
  LAS char* bp = lds + L_U + ch * 4608 + 2 * (240 - 16 * lb + 8 * lg) + 64 * r0;
  u32x4 A[8], B[8];
  if (b < 192) {
#pragma unroll
    for (int i = 7; i >= 0; i--) {
      B[7 - i] = *(LAS u32x4*)(bp + 512 * i);
      u32x2 lo = *(LAS u32x2*)(ap + 512 * i), hi = *(LAS u32x2*)(ap + 512 * i + 8);
      A[i] = (u32x4){lo[0], lo[1], hi[0], hi[1]};
    }
    conv_res<true>(ca, A, B, ap + 64, bp + 64, c.wsrc, c.cvo, w, l);
  } else {
    w2_piece10<0>(c.wsrc, c.cvo, w, l); w2_piece10<1>(c.wsrc, c.cvo, w, l); w2_piece10<2>(c.wsrc, c.cvo, w, l);
    w2_piece10<3>(c.wsrc, c.cvo, w, l); w2_piece10<4>(c.wsrc, c.cvo, w, l); w2_piece10<5>(c.wsrc, c.cvo, w, l);
    w2_piece10<6>(c.wsrc, c.cvo, w, l); w2_piece10<7>(c.wsrc, c.cvo, w, l); w2_piece10<8>(c.wsrc, c.cvo, w, l);
    w2_piece10<9>(c.wsrc, c.cvo, w, l);
  }
  vmwait<10>();
  if (l == 0) {
    u32 old = __hip_atomic_fetch_add((LAS u32*)(lds + L_CNT), 1u, __ATOMIC_RELAXED, __HIP_MEMORY_SCOPE_WORKGROUP);
    if (old == 7) flag_set(ws, OFF_FH + b * 8, g.tag);
  }
  TS(8);
  if (b < 192) {
#pragma unroll 1
    for (int r = 1; r < nr; r++) conv_res<false>(ca, A, B, ap + 64 * (r + 1), bp + 64 * (r + 1), c.wsrc, c.cvo, w, l);
  }
  TS(9);
  u32x2 uu[3];
#pragma unroll
  for (int i = 0; i < 3; i++) {
    int u = w * 3 + i;
    uu[i] = *(LAS u32x2*)(lds + L_U + (u >> 3) * 4608 + 2 * (2044 - (256 * (u & 7) + 16 * lb + 4 * lg)));
  }
  const u32 fhoff = OFF_FH + ((b & 7) + 8 * ((l & 3) * 8 + i7)) * 8;
  u32x2 pf;
  ldg8s(pf, ws, fhoff);
  lgwait(); bar();
#pragma unroll
  for (int i = 0; i < 8; i++) *(LAS f32x4*)(lds + L_RED + w * 8192 + i * 1024 + l * 16) = ca[i];
  lgwait(); bar();
  TS(10);
  f32x4 ys[3];
#pragma unroll
  for (int i = 0; i < 3; i++) {
    int u = w * 3 + i, c2 = u >> 3;
    LAS char* rp = lds + L_RED + c2 * 3 * 8192 + (u & 7) * 1024 + l * 16;
    ys[i] = *(LAS f32x4*)(rp) + *(LAS f32x4*)(rp + 8192);
    if (c2 < 2) ys[i] += *(LAS f32x4*)(rp + 2 * 8192);
  }
  vmwait<0>();
  PIN(pf);
  if (!flag_ok(pf, true, g.tag)) flag_wait(ws, fhoff, true, g.tag);
  TS(11);
  u32x4 hb[4][4];
#pragma unroll
  for (int it = 0; it < 3; it++) {
    ldg16s(hb[it][0], c.hsrc + it * 16384, l * 16); ldg16so(hb[it][1], c.hsrc + it * 16384, l * 16);
    ldg16s(hb[it][2], c.hsrc + it * 16384 + 8192, l * 16); ldg16so(hb[it][3], c.hsrc + it * 16384 + 8192, l * 16);
  }
#pragma unroll
  for (int i = 0; i < 3; i++) {
    int u = w * 3 + i, chn = cb * 3 + (u >> 3);
    u32 dw = ((CAS const u32*)Dv)[chn >> 1];
    float dv = (chn & 1) ? bhi(dw) : blo(dw);
    float o0 = rb(ys[i][0]) + rb(bhi(uu[i][1]) * dv), o1 = rb(ys[i][1]) + rb(blo(uu[i][1]) * dv);
    float o2 = rb(ys[i][2]) + rb(bhi(uu[i][0]) * dv), o3 = rb(ys[i][3]) + rb(blo(uu[i][0]) * dv);
    u32 o = b < 192 ? OFF_Y2 + chn * 4096 + (256 * (u & 7) + 16 * lb + 4 * lg) * 2 : OOB;
    __builtin_amdgcn_raw_buffer_store_b64((u32x2){pk(o0, o1), pk(o2, o3)}, g.wr, o, 0, SC);
  }
  lgwait(); bar();
  w2_piece<0>(c.wsrc + 2 * 73728, c.cvo, 73728, w, l); w2_piece<1>(c.wsrc + 2 * 73728, c.cvo, 73728, w, l);
  w2_piece<2>(c.wsrc + 2 * 73728, c.cvo, 73728, w, l); w2_piece<3>(c.wsrc + 2 * 73728, c.cvo, 73728, w, l);
  w2_piece<4>(c.wsrc + 2 * 73728, c.cvo, 73728, w, l);
  c.apA = lds + (kh * 64 + 16 * (lg >> 1) + 4 * (lg & 1) + ((l >> 2) & 3)) * 288 + (l & 3) * 8;
  c.apB = c.apA + 73728;
  c.srd = mksrd(ws, WS_END);
  c.srx = mksrd(x, S_ * D_ * 2);
  const int p = w & 3, tt = w >> 2;
  const int tok = rt * 128 + kq * 32 + tt * 16 + lb, d0 = dn * 144 + 4 * lg;
  c.fyact = l < 48;
  c.fyoff = OFF_FY + (48 * dn + (l < 48 ? l : 0)) * 8;
  {
    u32 yb = OFF_Y2 + (dn * 144 + w * 16 + (l >> 2)) * 4096 + (rt * 128 + kq * 32) * 2 + (l & 3) * 16;
    c.yoff0 = yb;
    c.yoff1 = w == 0 ? yb + 128 * 4096 : OOB;
    c.xoff0 = tok * 1152 + (d0 + p * 32) * 2;
    c.xoff2 = p == 0 ? tok * 1152 + (d0 + 128) * 2 : OOB;
  }
  f32x4 acc[2][9];
#pragma unroll
  for (int n = 0; n < 9; n++) { acc[0][n] = (f32x4){0, 0, 0, 0}; acc[1][n] = (f32x4){0, 0, 0, 0}; }
  G2Tail t;
  u32x2 ra[3], rb_[3];
  g2_aread<0, 0>(c, ra[0], rb_[0]); g2_aread<0, 1>(c, ra[1], rb_[1]);
  vmwait<8 + 3 + 5>();
  PIN(hb[0][0]); PIN(hb[0][1]); PIN(hb[0][2]); PIN(hb[0][3]);
  TS(12);
  g2_steps<0, 0>(c, t, hb, acc, ra, rb_);
  TS(13);
#pragma unroll
  for (int n = 0; n < 9; n++) *(LAS f32x4*)(lds + L_EX + w * 9216 + n * 1024 + l * 16) = kh ? acc[0][n] : acc[1][n];
  lgwait(); bar();
  f32x4 r[9];
#pragma unroll
  for (int n = 0; n < 9; n++) r[n] = (kh ? acc[1][n] : acc[0][n]) + *(LAS f32x4*)(lds + L_EX + (w ^ 4) * 9216 + n * 1024 + l * 16);
  {
    int ob = 2 * tg + xbit + 8 * (dn * 8 + i7);
    u32 po = tg != kq ? OFF_P + (ob * 3 + ((kq - tg - 1) & 3)) * 9216 + kh * 4608 : OOB;
#pragma unroll
    for (int q = 0; q < 4; q++)
      __builtin_amdgcn_raw_buffer_store_b128((u32x4){pk(r[2 * q][0], r[2 * q][1]), pk(r[2 * q][2], r[2 * q][3]),
                                                     pk(r[2 * q + 1][0], r[2 * q + 1][1]), pk(r[2 * q + 1][2], r[2 * q + 1][3])},
                                             g.wr, po + q * 1024 + l * 16, 0, SC);
    __builtin_amdgcn_raw_buffer_store_b64((u32x2){pk(r[8][0], r[8][1]), pk(r[8][2], r[8][3])}, g.wr, po + 4096 + l * 8, 0, SC);
    if (tg == kq) {
#pragma unroll
      for (int n = 0; n < 9; n++) *(LAS f32x4*)(lds + L_OWN + kh * 9216 + n * 1024 + l * 16) = r[n];
    }
  }
  vmwait<5>();
  PIN(t.yq[0]); PIN(t.yq[1]); PIN(t.xv[0]); PIN(t.xv[1]); PIN(t.xv[2]);
  *(LAS u32x4*)(lds + L_Y + (w * 16 + (l >> 2)) * 96 + (l & 3) * 16) = t.yq[0];
  if (w == 0) *(LAS u32x4*)(lds + L_Y + (128 + (l >> 2)) * 96 + (l & 3) * 16) = t.yq[1];
  vmwait<0>(); lgwait(); bar();
  if (w == 0 && l == 0) flag_set(ws, OFF_FX + b * 8, g.tag);
  TS(14);
  const u32 fxoff = OFF_FX + (2 * ((kq + 1 + (l < 3 ? l : 0)) & 3) + xbit + 8 * (dn * 8 + i7)) * 8;
  flag_wait(ws, fxoff, l < 3, g.tag);
  TS(15);
  u32x4 pq[3];
  u32x2 p8[3];
  {
    u32 po = OFF_P + b * 3 * 9216 + tt * 4608;
    u32 o8 = p == 0 ? po + 4096 + l * 8 : OOB;
#pragma unroll
    for (int s = 0; s < 3; s++) {
      pq[s] = __builtin_amdgcn_raw_buffer_load_b128(g.wr, po + s * 9216 + p * 1024 + l * 16, 0, SC);
      p8[s] = __builtin_amdgcn_raw_buffer_load_b64(g.wr, o8 + s * 9216, 0, SC);
    }
  }
  u32 st = *(LAS u32*)(lds + L_ST + (kq * 32 + tt * 16 + lb) * 4);
  float m = blo(st), rs = 1.0f / bhi(st);
  LAS char* yp = lds + L_Y + (4 * lg + ((l >> 2) & 3)) * 96 + tt * 32 + (l & 3) * 8;
  LAS char* op = lds + L_OWN + tt * 9216 + l * 16;
  u32x2 y0 = trread(yp + p * 32 * 96), y1 = trread(yp + (p * 32 + 16) * 96), y2 = trread(yp + 128 * 96);
  f32x4 s0 = *(LAS f32x4*)(op + 2 * p * 1024), s1 = *(LAS f32x4*)(op + (2 * p + 1) * 1024), s2 = *(LAS f32x4*)(op + 8 * 1024);
#pragma unroll
  for (int s = 0; s < 3; s++) { s0 += up4(pq[s][0], pq[s][1]); s1 += up4(pq[s][2], pq[s][3]); }
  u16* o = out + tok * D_ + d0;
  fin4(s0, t.xv[0], y0, m, rs, o + p * 32);
  fin4(s1, t.xv[1], y1, m, rs, o + p * 32 + 16);
  if (p == 0) {
#pragma unroll
    for (int s = 0; s < 3; s++) s2 += up4(p8[s][0], p8[s][1]);
    fin4(s2, t.xv[2], y2, m, rs, o + 128);
  }
}

__global__ void __launch_bounds__(512, 2) k_fused(const u16* x, const u16* k, const u16* v, const u16* sk, const u16* ssk, const u16* Dv,
                                                  const u16* w1, const u16* w2, u16* out, char* ws) {
  extern __shared__ __attribute__((aligned(1024))) char smem[];
  const int tid = threadIdx.x, b = blockIdx.x;
  const int xc = b & 7, i = b >> 3;
  const int rt = (xc & 1) * 8 + (i & 7), kq = xc >> 1, dn = i >> 3;
  G1Ctx c;
  c.lds = (LAS char*)smem;
  c.ws = ws;
  c.ssk = ssk;
  c.wr = mkrsrc(ws, WS_END);
  c.b = b; c.cb = b < 192 ? b : 191;
  c.w = __builtin_amdgcn_readfirstlane(tid >> 6); c.l = tid & 63;
  const u64 t0_ = __builtin_amdgcn_s_memrealtime();
  g1_phase(c, x, k, v, sk, w1, rt, 4 * kq + dn, kq TSPASS);
  conv_g2(c, x, Dv, w2, out, rt, kq, dn TSPASS);
  for (int i_ = 0; i_ < 100000; i_++) { if (__builtin_amdgcn_s_memrealtime() - t0_ >= TFIX) break; __builtin_amdgcn_s_sleep(1); }
}

extern "C" void kernel_launch(void* const* d_in, const int* in_sizes, int n_in, void* d_out, int out_size, void* d_ws, size_t ws_size,
                              hipStream_t stream) {
  (void)in_sizes; (void)n_in; (void)out_size; (void)ws_size;
  (void)hipFuncSetAttribute((const void*)k_fused, hipFuncAttributeMaxDynamicSharedMemorySize, LDS_BYTES);
  k_fused<<<256, 512, LDS_BYTES, stream>>>((const u16*)d_in[0], (const u16*)d_in[1], (const u16*)d_in[2], (const u16*)d_in[3],
                                           (const u16*)d_in[4], (const u16*)d_in[5], (const u16*)d_in[7], (const u16*)d_in[8],
                                           (u16*)d_out, (char*)d_ws);
}
```

```cpp
#include <hip/hip_runtime.h>
#include <stdint.h>

typedef unsigned short u16;
typedef unsigned int u32;
typedef unsigned long long u64;
#define LAS __attribute__((address_space(3)))
#define CAS __attribute__((address_space(4)))
typedef __attribute__((ext_vector_type(8))) __bf16 bf16x8;
typedef __attribute__((ext_vector_type(2))) __bf16 bf16x2;
typedef __attribute__((ext_vector_type(2))) float f32x2;
typedef __attribute__((ext_vector_type(4))) float f32x4;
typedef __attribute__((ext_vector_type(16))) float f32x16;
typedef __attribute__((ext_vector_type(2))) u32 u32x2;
typedef __attribute__((ext_vector_type(4))) u32 u32x4;
typedef __attribute__((ext_vector_type(4))) short v4i16;

static constexpr int S_ = 2048, D_ = 576, F_ = 4608;
static constexpr u32 OFF_E = 0;
static constexpr u32 OFF_FK = 4096;
static constexpr u32 OFF_FH = OFF_FK + 2048;
static constexpr u32 OFF_FY = OFF_FH + 2048;
static constexpr u32 OFF_FX = OFF_FY + 2048;
static constexpr u32 OFF_STATS = 65536;
static constexpr u32 OFF_CS = OFF_STATS + 16384;
static constexpr u32 OFF_UR = 262144;
static constexpr u32 OFF_Y2 = OFF_UR + 576 * 4096;
static constexpr u32 OFF_H = OFF_Y2 + 576 * 4096;
static constexpr u32 OFF_P = OFF_H + 2048 * 4608 * 2;
static constexpr u32 WS_END = OFF_P + 256 * 3 * 9216;
static constexpr int LDS_BYTES = 163840;
static constexpr int SC = 16;
static constexpr u32 OOB = 0xF0000000u;
static constexpr u32 L_B1 = 73728, L_S1 = 147456, L_CSS = 159744, L_CS = 161792, L_ST = 162944, L_CNT = 163456;

#define TS(i)
#define TSARG
#define TSPASS

__device__ __forceinline__ float blo(u32 v) { return __uint_as_float(v << 16); }
__device__ __forceinline__ float bhi(u32 v) { return __uint_as_float(v & 0xffff0000u); }
__device__ __forceinline__ u32 pk(float lo, float hi) {
  f32x2 t = {lo, hi};
  return __builtin_bit_cast(u32, __builtin_convertvector(t, bf16x2));
}
__device__ __forceinline__ float rb(float x) { return blo(pk(x, x)); }
template <int C> __device__ __forceinline__ float dppf(float v) {
  return __int_as_float(__builtin_amdgcn_update_dpp(0, __float_as_int(v), C, 0xf, 0xf, false));
}
__device__ __forceinline__ float swp32add(float a, float b) {
  auto r = __builtin_amdgcn_permlane32_swap(__float_as_uint(a), __float_as_uint(b), false, false);
  return __uint_as_float(r[0]) + __uint_as_float(r[1]);
}
__device__ __forceinline__ float swp16add(float a, float b) {
  auto r = __builtin_amdgcn_permlane16_swap(__float_as_uint(a), __float_as_uint(b), false, false);
  return __uint_as_float(r[0]) + __uint_as_float(r[1]);
}
__device__ __forceinline__ float wave_sum(float v) {
  v += dppf<0x128>(v); v += dppf<0x124>(v); v += dppf<0x122>(v); v += dppf<0x121>(v);
  v = swp16add(v, v);
  return swp32add(v, v);
}
__device__ __forceinline__ void glds(const void* sbase, u32 voff, u32 ldsdst) {
  asm volatile("s_mov_b32 m0, %2\n\ts_nop 0\n\tglobal_load_lds_dwordx4 %0, %1" ::"v"(voff), "s"(sbase), "s"(ldsdst) : "memory");
}
__device__ __forceinline__ void blds(u32x4 srd, u32 voff, u32 ldsdst) {
  asm volatile("s_mov_b32 m0, %2\n\ts_nop 0\n\tbuffer_load_dwordx4 %0, %1, 0 offen lds" ::"v"(voff), "s"(srd), "s"(ldsdst) : "memory");
}
__device__ __forceinline__ void glds4(const void* sbase, u32 voff, u32 ldsdst) {
  asm volatile("s_mov_b32 m0, %2\n\ts_nop 0\n\tglobal_load_lds_dword %0, %1" ::"v"(voff), "s"(sbase), "s"(ldsdst) : "memory");
}
__device__ __forceinline__ void ldg16(u32x4& d, const void* sbase, u32 voff) {
  asm volatile("global_load_dwordx4 %0, %1, %2" : "=v"(d) : "v"(voff), "s"(sbase) : "memory");
}
__device__ __forceinline__ void ldg16s(u32x4& d, const void* sbase, u32 voff) {
  asm volatile("global_load_dwordx4 %0, %1, %2 sc1" : "=v"(d) : "v"(voff), "s"(sbase) : "memory");
}
__device__ __forceinline__ void ldg16so(u32x4& d, const void* sbase, u32 voff) {
  asm volatile("global_load_dwordx4 %0, %1, %2 offset:1024 sc1" : "=v"(d) : "v"(voff), "s"(sbase) : "memory");
}
__device__ __forceinline__ void ldg8(u32x2& d, const void* sbase, u32 voff) {
  asm volatile("global_load_dwordx2 %0, %1, %2" : "=v"(d) : "v"(voff), "s"(sbase) : "memory");
}
__device__ __forceinline__ void ldg8s(u32x2& d, const void* sbase, u32 voff) {
  asm volatile("global_load_dwordx2 %0, %1, %2 sc1" : "=v"(d) : "v"(voff), "s"(sbase) : "memory");
}
__device__ __forceinline__ void ldb8(u32x2& d, u32x4 srd, u32 voff) {
  asm volatile("buffer_load_dwordx2 %0, %1, %2, 0 offen" : "=v"(d) : "v"(voff), "s"(srd) : "memory");
}
__device__ __forceinline__ void ldb16s(u32x4& d, u32x4 srd, u32 voff) {
  asm volatile("buffer_load_dwordx4 %0, %1, %2, 0 offen sc1" : "=v"(d) : "v"(voff), "s"(srd) : "memory");
}
__device__ __forceinline__ void ldb16(u32x4& d, u32x4 srd, u32 voff) {
  asm volatile("buffer_load_dwordx4 %0, %1, %2, 0 offen" : "=v"(d) : "v"(voff), "s"(srd) : "memory");
}
template <int N> __device__ __forceinline__ void vmwait() { asm volatile("s_waitcnt vmcnt(%0)" ::"n"(N) : "memory"); }
__device__ __forceinline__ void lgwait() { asm volatile("s_waitcnt lgkmcnt(0)" ::: "memory"); }
__device__ __forceinline__ void bar() { asm volatile("s_barrier" ::: "memory"); }
#define PIN(x) asm volatile("" : "+v"(x))
__device__ __forceinline__ void sbar() { __builtin_amdgcn_sched_barrier(0); }
__device__ __forceinline__ __amdgpu_buffer_rsrc_t mkrsrc(const void* p, u32 bytes) {
  return __builtin_amdgcn_make_buffer_rsrc((void*)p, 0, bytes, 0x00020000);
}
__device__ __forceinline__ u32x4 mksrd(const void* p, u32 bytes) {
  u64 b = (u64)p;
  return (u32x4){(u32)b, (u32)(b >> 32) & 0xffffu, bytes, 0x00020000u};
}
__device__ __forceinline__ f32x4 mfma16(u32x4 a, u32x4 b, f32x4 c) {
  return __builtin_amdgcn_mfma_f32_16x16x32_bf16(__builtin_bit_cast(bf16x8, a), __builtin_bit_cast(bf16x8, b), c, 0, 0, 0);
}
__device__ __forceinline__ f32x16 mfma32(u32x4 a, u32x4 b, f32x16 c) {
  return __builtin_amdgcn_mfma_f32_32x32x16_bf16(__builtin_bit_cast(bf16x8, a), __builtin_bit_cast(bf16x8, b), c, 0, 0, 0);
}
__device__ __forceinline__ u32x2 trread(LAS char* p) {
  return __builtin_bit_cast(u32x2, __builtin_amdgcn_ds_read_tr16_b64_v4i16((LAS v4i16*)p));
}
__device__ __forceinline__ void flag_set(char* ws, u32 off, u64 tag) {
  __hip_atomic_store((__attribute__((address_space(1))) u64*)(ws + off), tag, __ATOMIC_RELAXED, __HIP_MEMORY_SCOPE_AGENT);
}
__device__ __forceinline__ void flag_wait(char* ws, u32 off, bool act, u64 tag) {
#pragma unroll 1
  for (int spins = 0; spins < 4000000; spins++) {
    u64 f = __hip_atomic_load((__attribute__((address_space(1))) u64*)(ws + off), __ATOMIC_RELAXED, __HIP_MEMORY_SCOPE_AGENT);
    if (__all(f == tag || !act)) break;
    __builtin_amdgcn_s_sleep(1);
  }
  asm volatile("" ::: "memory");
}
__device__ __forceinline__ bool flag_ok(u32x2 f, bool act, u64 tag) {
  return __all(!act || (f[0] == (u32)tag && f[1] == (u32)(tag >> 32)));
}

__device__ __forceinline__ void lds_arrive(LAS char* p, int l) {
  if (l == 0) __hip_atomic_fetch_add((LAS u32*)p, 1u, __ATOMIC_RELAXED, __HIP_MEMORY_SCOPE_WORKGROUP);
}
__device__ __forceinline__ void lds_await(LAS char* p, u32 n) {
#pragma unroll 1
  for (int spins = 0; spins < 40000000; spins++) {
    u32 v = __builtin_amdgcn_readfirstlane(__hip_atomic_load((LAS u32*)p, __ATOMIC_RELAXED, __HIP_MEMORY_SCOPE_WORKGROUP));
    if (v >= n) break;
  }
  asm volatile("" ::: "memory");
}

static constexpr u32 L_KC = L_B1;
static constexpr u32 L_U = L_B1 + 12 * 4160;

struct G1Ctx {
  LAS char* lds;
  char* ws;
  const u16* ssk;
  __amdgpu_buffer_rsrc_t wr;
  float nm, cc;
  u64 tag;
  int b, cb, w, l;
};

template <int MODE>
__device__ __forceinline__ void g1_pair(const G1Ctx& c, const u32x4 (&xf)[36], f32x16& acc, const f32x16& pa, LAS char* a0, LAS char* n0,
                                        u32x2 (&ra)[3], u32x2 (&rb_)[3], f32x4 (&cs)[2], LAS char* csp, LAS char* csn, u32 hoff,
                                        const char* dsrc, u32 dvoff, u32 ddst, int pp, u32 kfoff, bool kfact) {
  LAS char* a1 = a0 + 18 * 2048;
  const int w = c.w, l = c.l;
  float X[16], E[16];
  u32 P[8];
  u32x2 pf, k0;
  u32x4 uq[2];
#pragma unroll
  for (int ks = 0; ks < 36; ks++) {
    if (ks + 2 < 36) {
      LAS char* ap = (ks + 2 < 18 ? a0 + (ks + 2) * 2048 : a1 + (ks + 2 - 18) * 2048);
      ra[(ks + 2) % 3] = trread(ap); rb_[(ks + 2) % 3] = trread(ap + 512);
    }
    if (MODE != 2 && ks == 35) { ra[1] = trread(n0 + 2048); rb_[1] = trread(n0 + 2048 + 512); }
    if (ks == 34) lgwait();
    u32x4 A = {ra[ks % 3][0], ra[ks % 3][1], rb_[ks % 3][0], rb_[ks % 3][1]};
    if (ks == 0) { f32x16 z = {}; acc = mfma32(A, xf[0], z); }
    else acc = mfma32(A, xf[ks], acc);
    if (ks == 34) {
      vmwait<1>();
      if (MODE == 0) {
        PIN(pf);
        if (pp == 1 && !flag_ok(pf, kfact, c.tag)) flag_wait(c.ws, kfoff, kfact, c.tag);
      }
      bar();
      if (MODE == 0 && pp == 0 && w == 0 && l == 0) flag_set(c.ws, OFF_FK + c.b * 8, c.tag);
      if (MODE != 2) { ra[0] = trread(n0); rb_[0] = trread(n0 + 512); }
    }
    if (ks % 8 == 6 && ks / 8 < 3) cs[(ks / 8 + 1) & 1] = *(LAS f32x4*)(csp + (ks / 8 + 1) * 32);
    if (ks % 2 == 1 && ks / 2 < 16) {
      int e = ks / 2;
      float xx = __builtin_fmaf(c.nm, cs[(e >> 2) & 1][e & 3], pa[e]);
      PIN(xx);
      float tt = xx * c.cc;
      PIN(tt);
      X[e] = xx; E[e] = tt;
    }
    if (ks % 2 == 0 && ks >= 2 && ks / 2 - 1 < 16) {
      int e = ks / 2 - 1;
      float tt = __builtin_amdgcn_exp2f(E[e]);
      PIN(tt);
      E[e] = tt;
    }
    if (ks % 2 == 1 && ks >= 3 && (ks - 3) / 2 < 16) {
      int e = (ks - 3) / 2;
      float tt = 1.0f + E[e];
      PIN(tt);
      tt = __builtin_amdgcn_rcpf(tt);
      PIN(tt);
      E[e] = tt;
    }
    if (ks % 2 == 0 && ks >= 4 && (ks - 4) / 2 < 16) {
      int e = (ks - 4) / 2;
      float hh = X[e] * E[e];
      PIN(hh);
      X[e] = hh;
      if (e & 1) { u32 pq = pk(X[e - 1], X[e]); PIN(pq); P[e >> 1] = pq; }
    }
    if (ks == 19) __builtin_amdgcn_raw_buffer_store_b128((u32x4){P[0], P[1], P[2], P[3]}, c.wr, hoff, 0, SC);
    if (ks == 35) {
      __builtin_amdgcn_raw_buffer_store_b128((u32x4){P[4], P[5], P[6], P[7]}, c.wr, hoff + 512, 0, SC);
      if (MODE != 2) cs[0] = *(LAS f32x4*)(csn);
    }
    if (MODE != 2) {
      if (ks % 2 == 0 && ks / 2 < 9) glds(dsrc + (size_t)(ks / 2) * (64 * 9216), dvoff, ddst + (ks / 2) * 8192);
      if (MODE == 0 && ks == 1) ldg8s(pf, c.ws, kfoff);
    } else {
      if (ks % 2 == 0 && ks / 2 < 6) {
        int idx = w * 6 + ks / 2, cc = idx >> 2, part = idx & 3;
        u32x4 srd = mksrd(c.ssk + (size_t)(c.cb * 3 + (cc >> 2)) * 2048, 4096u);
        blds(srd, (cc & 3) * 2 + part * 1024 + l * 16, L_KC + cc * 4160 + 32 + part * 1024);
      }
      if (ks == 12 || ks == 14) {
        int idx = (w < 6 ? w : 5) * 2 + (ks - 12) / 2;
        ldg16s(uq[(ks - 12) / 2], c.ws + OFF_UR + (size_t)(c.cb * 3 + (idx >> 2)) * 4096 + (idx & 3) * 1024, l * 16);
      }
      if (ks == 16) ldg8(k0, c.ssk + (size_t)c.cb * 3 * 2048, (l < 8 ? l >> 2 : 2) * 4096);
      if (ks == 3) {
        if (w == 6 && l < 12) *(LAS u32x4*)(c.lds + L_KC + l * 4160) = (u32x4){0, 0, 0, 0};
        if (w < 3 && l < 32) *(LAS u32x4*)(c.lds + L_U + w * 4608 + 4096 + l * 16) = (u32x4){0, 0, 0, 0};
      }
      if (ks == 30) {
        vmwait<1>();
        PIN(uq[0]); PIN(uq[1]); PIN(k0);
        if (w < 6) {
          int idx = w * 2;
          *(LAS u32x4*)(c.lds + L_U + (idx >> 2) * 4608 + (idx & 3) * 1024 + l * 16) = uq[0];
          *(LAS u32x4*)(c.lds + L_U + (idx >> 2) * 4608 + ((idx & 3) + 1) * 1024 + l * 16) = uq[1];
        }
        if (w == 6 && l < 12) {
          int cp = l & 3;
          u32x4 c1 = {0, 0, 0, 0};
          if (cp == 1) c1[3] = k0[0] << 16;
          if (cp == 2) c1[3] = k0[0];
          if (cp == 3) { c1[2] = k0[0] << 16; c1[3] = (k0[0] >> 16) | (k0[1] << 16); }
          *(LAS u32x4*)(c.lds + L_KC + l * 4160 + 16) = c1;
        }
      }
    }
    sbar();
  }
}

__device__ __forceinline__ void g1_epi_final(const G1Ctx& c, const f32x16& pa, u32 csoff, u32 hoff) {
  u32 P[8];
#pragma unroll
  for (int q = 0; q < 4; q++) {
    f32x4 cs = *(LAS f32x4*)(c.lds + csoff + q * 32);
    float h[4];
#pragma unroll
    for (int i = 0; i < 4; i++) {
      float X = __builtin_fmaf(c.nm, cs[i], pa[4 * q + i]);
      h[i] = X * __builtin_amdgcn_rcpf(1.0f + __builtin_amdgcn_exp2f(X * c.cc));
    }
    P[2 * q] = pk(h[0], h[1]); P[2 * q + 1] = pk(h[2], h[3]);
  }
  __builtin_amdgcn_raw_buffer_store_b128((u32x4){P[0], P[1], P[2], P[3]}, c.wr, hoff, 0, SC);
  __builtin_amdgcn_raw_buffer_store_b128((u32x4){P[4], P[5], P[6], P[7]}, c.wr, hoff + 512, 0, SC);
}

__device__ __forceinline__ void kv_compute(const u32x2 (&kr)[11], const u32x2 (&vr)[8], const u32x4 (&sq)[2],
                                           __amdgpu_buffer_rsrc_t wr, int ch, int t) {
#pragma unroll
  for (int q = 0; q < 4; q++) {
    u32 sa = sq[q >> 1][(q & 1) * 2], sb = sq[q >> 1][(q & 1) * 2 + 1];
    float w0 = blo(sa), w1 = bhi(sa), w2 = blo(sb), w3 = bhi(sb);
    float kk[11], o[8];
#pragma unroll
    for (int i = 0; i < 11; i++) kk[i] = (q & 1) ? bhi(kr[i][q >> 1]) : blo(kr[i][q >> 1]);
#pragma unroll
    for (int i = 0; i < 8; i++) {
      float vv = (q & 1) ? bhi(vr[i][q >> 1]) : blo(vr[i][q >> 1]);
      o[i] = (kk[i] * w3 + kk[i + 1] * w2 + kk[i + 2] * w1 + kk[i + 3] * w0) * vv;
    }
    u32x4 r = {pk(o[7], o[6]), pk(o[5], o[4]), pk(o[3], o[2]), pk(o[1], o[0])};
    __builtin_amdgcn_raw_buffer_store_b128(r, wr, OFF_UR + (ch + q) * 4096 + 2 * (2040 - t), 0, SC);
    sbar();
  }
}

__device__ __forceinline__ u32 ln_stats(const u32x4 (&sr)[2], int l) {
  float e[16];
#pragma unroll
  for (int i = 0; i < 8; i++) { e[2 * i] = blo(sr[i >> 2][i & 3]); e[2 * i + 1] = bhi(sr[i >> 2][i & 3]); }
  float s = 0;
#pragma unroll
  for (int i = 0; i < 16; i++) s += e[i];
  if (l >= 36) s = 0;
  float mean = wave_sum(s) * (1.0f / 576.0f);
  float vv = 0;
#pragma unroll
  for (int i = 0; i < 16; i++) { float d = e[i] - mean; vv += d * d; }
  if (l >= 36) vv = 0;
  float var = rb(wave_sum(vv) * (1.0f / 576.0f));
  float sd = rb(__builtin_sqrtf(rb(var + 1e-5f)));
  return pk(mean, sd);
}

__device__ __forceinline__ void g1_phase(G1Ctx& c, const u16* x, const u16* k, const u16* v, const u16* sk, const u16* w1, const u16* w2,
                                         int rt, int j, int kq TSARG) {
  LAS char* lds = c.lds;
  char* ws = c.ws;
  const int w = c.w, l = c.l, b = c.b, tid = w * 64 + l;
  const int tg = w & 3, fg = w >> 2, odd = j & 1, kh = l >> 5;
  const char* w1b = (const char*)w1 + j * 576;
  const char* sb = w1b + (odd ? 0 : 512);
  const char* pb = w1b + (odd ? 64 : 0);
  const u32 svo = (l >> 2) * 9216 + (l & 3) * 16;
  const bool kvw = w >= 5 && b < 192;
  const int kid = 3 * b + w - 5, kch = (kid >> 6) * 64 + (l & 15) * 4, kt = (kid & 63) * 32 + (l >> 4) * 8;
  if (tid == 0) *(LAS u32x2*)(lds + L_CNT) = (u32x2){0, 0};
  if (w < 4) {
    u32x2 ep;
    ldg8s(ep, ws + OFF_E, 0);
    const char* xb = (const char*)x + (size_t)rt * 128 * 1152;
#pragma unroll
    for (int h = 0; h < 4; h++) {
      u32 vo = (32 * w + 8 * h + (l >> 3)) * 1152 + (((l & 7) ^ (4 * (h & 1) + (l >> 4))) * 16);
#pragma unroll
      for (int kb = 0; kb < 9; kb++) glds(xb + kb * 128, vo, kb * 16384 + (32 * w + 8 * h) * 128);
    }
#pragma unroll
    for (int i = 0; i < 3; i++) glds(sb + (size_t)(w * 3 + i) * 16 * 9216, svo, L_S1 + (w * 3 + i) * 1024);
    {
      int n = ((b >> 3) & 7) * 120 + w * 30 + (l < 30 ? l : 29);
      u32 o = n < 384 ? (192 + n) * 9216 + (odd ? 0 : 512) : (n - 384) * 9216 + (odd ? 64 : 0);
      glds4(w1b, o, L_CSS + w * 256);
    }
    vmwait<1>();
    PIN(ep);
    c.tag = (((u64)(u32)__builtin_amdgcn_readfirstlane(ep[1]) << 32) | (u32)__builtin_amdgcn_readfirstlane(ep[0])) + 1;
  } else {
    const int pw = w - 4;
    u32x2 ep;
    ldg8s(ep, ws + OFF_E, 0);
    __builtin_amdgcn_s_setprio(3);
    u32x4 sr[2][2], cq[6];
    const int stok = rt * 128 + j * 8 + 2 * pw;
    {
      u32 o = stok * 1152 + (l < 36 ? l : 35) * 32;
      ldg16(sr[0][0], x, o); ldg16(sr[0][1], x, o + 16); ldg16(sr[1][0], x, o + 1152); ldg16(sr[1][1], x, o + 1168);
    }
    const int cuq = (b & 1) * 32 + (b >> 3);
    const int cu = cuq < 54 ? cuq : 53;
    const int ccl = 18 * kq + cu % 18, cpp = cu / 18;
    {
      u32 o = (cpp * 192 + (l >> 1)) * 9216 + ccl * 128 + pw * 32 + (l & 1) * 16;
#pragma unroll
      for (int i = 0; i < 6; i++) ldg16(cq[i], w1, o + i * 32 * 9216);
    }
    vmwait<6>();
    PIN(ep); PIN(sr[0][0]); PIN(sr[0][1]); PIN(sr[1][0]); PIN(sr[1][1]);
    c.tag = (((u64)(u32)__builtin_amdgcn_readfirstlane(ep[1]) << 32) | (u32)__builtin_amdgcn_readfirstlane(ep[0])) + 1;
    {
      u32 s0 = ln_stats(sr[0], l), s1 = ln_stats(sr[1], l);
      __builtin_amdgcn_raw_buffer_store_b64((u32x2){l ? s1 : s0, (u32)c.tag}, c.wr, l < 2 ? OFF_STATS + (stok + l) * 8 : OOB, 0, SC);
    }
    vmwait<1>();
#pragma unroll
    for (int i = 0; i < 6; i++) PIN(cq[i]);
    {
      float a[8];
#pragma unroll
      for (int i = 0; i < 4; i++) {
        a[2 * i] = (blo(cq[0][i]) + blo(cq[1][i])) + (blo(cq[2][i]) + blo(cq[3][i])) + (blo(cq[4][i]) + blo(cq[5][i]));
        a[2 * i + 1] = (bhi(cq[0][i]) + bhi(cq[1][i])) + (bhi(cq[2][i]) + bhi(cq[3][i])) + (bhi(cq[4][i]) + bhi(cq[5][i]));
      }
      float b0 = swp32add(a[0], a[4]), b1 = swp32add(a[1], a[5]), b2 = swp32add(a[2], a[6]), b3 = swp32add(a[3], a[7]);
      float c0 = swp16add(b0, b2), c1 = swp16add(b1, b3);
      c0 += dppf<0x128>(c0); c1 += dppf<0x128>(c1);
      c0 += dppf<0x124>(c0); c1 += dppf<0x124>(c1);
      c0 += dppf<0x122>(c0); c1 += dppf<0x122>(c1);
      u32 col = ccl * 64 + pw * 16 + (l & 1) * 8 + (l >> 5) * 4 + ((l >> 4) & 1) * 2;
      __builtin_amdgcn_raw_buffer_store_b128((u32x4){__float_as_uint(c0), (u32)c.tag, __float_as_uint(c1), (u32)c.tag}, c.wr,
                                             (cuq < 54 && (l & 14) == 0) ? OFF_CS + (cpp * F_ + col) * 8 : OOB, 0, SC);
    }
    __builtin_amdgcn_s_setprio(0);
  }
  lgwait(); bar();
  TS(1);
  u32x4 xf[36];
  auto xread = [&]() {
    u32 row = tg * 32 + (l & 31);
#pragma unroll
    for (int q = 0; q < 4; q++) {
      u32 a = row * 128 + (((2 * q + (l >> 5)) ^ ((row >> 1) & 7)) * 16);
#pragma unroll
      for (int kb = 0; kb < 9; kb++) xf[kb * 4 + q] = *(LAS u32x4*)(lds + a + kb * 16384);
    }
    lgwait(); bar();
    TS(2);
  };
  const u32 pvo = (l >> 3) * 9216 + (((l & 7) ^ (((l >> 4) & 1) << 2)) * 16);
  const char* pwp = pb + (size_t)w * 8 * 9216;
  f32x16 acc0 = {}, acc1 = {};
  if (w < 4) {
    xread();
#pragma unroll
    for (int i = 0; i < 6; i++) glds(sb + (size_t)(12 + w + 4 * i) * 16 * 9216, svo, (w + 4 * i) * 1024);
    u32 sa = (8 * kh + ((l >> 2) & 3)) * 64 + ((l >> 4) & 1) * 32 + (l & 3) * 8;
    LAS char* s0 = lds + L_S1 + sa;
    LAS char* s1 = lds + sa - 12 * 1024;
    u32x2 ra[3], rb_[3];
    ra[0] = trread(s0); rb_[0] = trread(s0 + 256);
    ra[1] = trread(s0 + 1024); rb_[1] = trread(s0 + 1024 + 256);
#pragma unroll
    for (int ks = 0; ks < 36; ks++) {
      if (ks < 9) {
        glds(pb + (size_t)(w + 8 * ks) * 8 * 9216, pvo, L_B1 + (w + 8 * ks) * 1024);
        glds(pb + (size_t)(w + 8 * ks + 4) * 8 * 9216, pvo, L_B1 + (w + 8 * ks + 4) * 1024);
      }
      if (ks == 10) {
        vmwait<18>();
        TS(17);
        lds_arrive(lds + L_CNT + 4, l);
        lds_await(lds + L_CNT + 4, 4);
      }
      if (ks + 2 < 36) {
        LAS char* ap = (ks + 2 < 12 ? s0 : s1) + (ks + 2) * 1024;
        ra[(ks + 2) % 3] = trread(ap); rb_[(ks + 2) % 3] = trread(ap + 256);
      }
      u32x4 A = {ra[ks % 3][0], ra[ks % 3][1], rb_[ks % 3][0], rb_[ks % 3][1]};
      acc1 = mfma32(A, xf[ks], acc1);
      sbar();
    }
    TS(18);
    vmwait<0>();
    TS(19);
  } else if (w == 4) {
    u32x4 srd = mksrd(ws, WS_END), sg, cg[3][3];
    const u32 tg32 = (u32)c.tag;
    const u32 so = OFF_STATS + rt * 1024 + l * 16, co = OFF_CS + j * 2304 + l * 16;
    auto sweep = [&]() {
      ldb16s(sg, srd, so);
#pragma unroll
      for (int p = 0; p < 3; p++) {
        ldb16s(cg[p][0], srd, co + p * F_ * 8); ldb16s(cg[p][1], srd, co + p * F_ * 8 + 1024);
        ldb16s(cg[p][2], srd, l < 16 ? co + p * F_ * 8 + 2048 : OOB);
      }
    };
    sweep();
    xread();
    TS(17);
#pragma unroll 1
    for (int spins = 0; spins < 1000000; spins++) {
      vmwait<0>();
      PIN(sg);
      bool ok = sg[1] == tg32 && sg[3] == tg32;
#pragma unroll
      for (int p = 0; p < 3; p++) {
        PIN(cg[p][0]); PIN(cg[p][1]); PIN(cg[p][2]);
        ok = ok && cg[p][0][1] == tg32 && cg[p][0][3] == tg32 && cg[p][1][1] == tg32 && cg[p][1][3] == tg32;
        ok = ok && (l >= 16 || (cg[p][2][1] == tg32 && cg[p][2][3] == tg32));
      }
      if (__all(ok)) break;
      sweep();
    }
    vmwait<0>();
    TS(18);
    *(LAS u32x2*)(lds + L_ST + l * 8) = (u32x2){sg[0], sg[2]};
#pragma unroll
    for (int i = 0; i < 3; i++) {
      float c0 = __uint_as_float(cg[0][i][0]) + __uint_as_float(cg[1][i][0]) + __uint_as_float(cg[2][i][0]);
      float c1 = __uint_as_float(cg[0][i][2]) + __uint_as_float(cg[1][i][2]) + __uint_as_float(cg[2][i][2]);
      *(LAS f32x2*)(lds + (i < 2 || l < 16 ? L_CS + i * 512 : L_CSS + 1024) + l * 8) = (f32x2){c0, c1};
    }
    {
      const char* kp = (const char*)c.ssk + (size_t)c.cb * 3 * 4096;
      glds4(kp, l * 128, L_CSS);
      glds4(kp, 8192 + (l & 31) * 128, L_CSS + 256);
      const char* wp = (const char*)w2 + (size_t)kq * 1327104 + (b >> 3) * 41472;
#pragma unroll
      for (int i = 0; i < 6; i++) glds4(wp, (i < 5 ? i * 64 + l : 320 + (l & 3)) * 128, L_CSS + 512);
    }
    TS(19);
  } else if (kvw) {
    u32x2 kr[11], vr[8];
    u32x4 sq[2];
    {
      u32x4 rk = mksrd(k, S_ * D_ * 2), rv = mksrd(v, S_ * D_ * 2), rs = mksrd(sk, D_ * 8);
      u32 o = kt * 1152 + kch * 2;
#pragma unroll
      for (int i = 0; i < 11; i++) ldb8(kr[i], rk, o + (i - 3) * 1152);
#pragma unroll
      for (int i = 0; i < 8; i++) ldb8(vr[i], rv, o + i * 1152);
      ldb16(sq[0], rs, kch * 8); ldb16(sq[1], rs, kch * 8 + 16);
    }
    xread();
    __builtin_amdgcn_s_setprio(2);
    vmwait<0>();
#pragma unroll
    for (int i = 0; i < 11; i++) PIN(kr[i]);
#pragma unroll
    for (int i = 0; i < 8; i++) PIN(vr[i]);
    PIN(sq[0]); PIN(sq[1]);
    TS(17);
    kv_compute(kr, vr, sq, c.wr, kch, kt);
    TS(18);
    __builtin_amdgcn_s_setprio(0);
  } else {
    xread();
  }
  lgwait(); bar();
  TS(3);
  {
    u32 st = *(LAS u32*)(lds + L_ST + (tg * 32 + (l & 31)) * 4);
    c.nm = -blo(st);
    c.cc = -1.44269504088896f / bhi(st);
  }
  const u32 abase = (8 * kh + ((l >> 2) & 3)) * 128 + ((fg * 64 + ((l >> 4) & 1) * 32 + (l & 3) * 8) ^ (((l >> 3) & 1) << 6));
  const u32 hbase = OFF_H + (rt * 144 + 9 * j) * 8192 + (2 * tg + ((l >> 4) & 1)) * 1024 + (kh * 16 + (l & 15)) * 16;
  LAS char* aB0 = lds + abase;
  LAS char* aB1 = lds + L_B1 + abase;
  LAS char* csb = lds + L_CS + kh * 16;
  int ptile = odd ? 0 : 8;
  u32 hoff = (w < 4) ? hbase + ptile * 8192 : OOB;
  const int klo = (((c.cb * 3) >> 6) * 64) / 3, khi = (((c.cb * 3 + 2) >> 6) * 64 + 63) / 3;
  const bool kfact = klo + l <= khi;
  const u32 kfoff = OFF_FK + (kfact ? klo + l : klo) * 8;
  u32x2 ra[3], rb_[3];
  f32x4 cs[2];
  ra[0] = trread(aB1); rb_[0] = trread(aB1 + 512);
  ra[1] = trread(aB1 + 2048); rb_[1] = trread(aB1 + 2048 + 512);
  cs[0] = *(LAS f32x4*)(csb + ptile * 128);
#pragma unroll 1
  for (int pp = 0; pp < 2; pp++) {
    int t0 = odd + 4 * pp + fg, t1 = t0 + 2;
    g1_pair<0>(c, xf, acc0, acc1, aB1, aB0, ra, rb_, cs, csb + ptile * 128, csb + t0 * 128, hoff,
               pwp + (2 * pp + 1) * 128, pvo, w * 1024, pp, kfoff, kfact);
    if (pp == 0) { TS(4); } else { TS(6); }
    hoff = hbase + t0 * 8192;
    ptile = t0;
    if (pp == 1) break;
    g1_pair<1>(c, xf, acc1, acc0, aB0, aB1, ra, rb_, cs, csb + t0 * 128, csb + t1 * 128, hoff,
               pwp + 2 * 128, pvo, L_B1 + w * 1024, pp, kfoff, kfact);
    TS(5);
    ptile = t1;
    hoff = hbase + t1 * 8192;
  }
  g1_pair<2>(c, xf, acc1, acc0, aB0, aB1, ra, rb_, cs, csb + ptile * 128, csb, hoff, pwp, pvo, 0, 1, kfoff, kfact);
  TS(7);
  ptile += 2;
  g1_epi_final(c, acc1, L_CS + ptile * 128 + kh * 16, hbase + ptile * 8192);
}

static constexpr u32 L_RED = L_B1;
static constexpr u32 L_EX = 73728;
static constexpr u32 L_OWN = 0;
static constexpr u32 L_Y = 18432;

template <int P>
__device__ __forceinline__ void w2_piece(const char* wsrc, const u32 (&cvo)[5], u32 ldst, int w, int l) {
  if (P < 4) glds(wsrc, cvo[P < 4 ? P : 0], ldst + (w * 4 + P) * 1024);
  else if (l < 32) glds(wsrc, cvo[4], ldst + (32 + (w >> 1)) * 1024 + (w & 1) * 512);
}
template <int P>
__device__ __forceinline__ void w2_piece10(const char* wsrc, const u32 (&cvo)[5], int w, int l) {
  w2_piece<P % 5>(wsrc + (P / 5) * 73728, cvo, (P / 5) * 36864, w, l);
}
template <bool GL>
__device__ __forceinline__ void conv_res(f32x4 (&acc)[8], u32x4 (&A)[8], u32x4 (&B)[8], LAS char* ap, LAS char* bp,
                                         const char* wsrc, const u32 (&cvo)[5], int w, int l) {
#pragma unroll
  for (int q = 0; q < 8; q++) {
#pragma unroll
    for (int i = 7 - q; i >= 0; i--) acc[i + q] = mfma16(A[i], B[q], acc[i + q]);
    B[q] = *(LAS u32x4*)(bp + 512 * (7 - q));
    u32x2 lo = *(LAS u32x2*)(ap + 512 * (7 - q)), hi = *(LAS u32x2*)(ap + 512 * (7 - q) + 8);
    A[7 - q] = (u32x4){lo[0], lo[1], hi[0], hi[1]};
    if (GL) {
      if (q == 0) { w2_piece10<0>(wsrc, cvo, w, l); w2_piece10<1>(wsrc, cvo, w, l); }
      if (q == 1) { w2_piece10<2>(wsrc, cvo, w, l); w2_piece10<3>(wsrc, cvo, w, l); }
      if (q == 2) w2_piece10<4>(wsrc, cvo, w, l);
      if (q == 3) w2_piece10<5>(wsrc, cvo, w, l);
      if (q == 4) w2_piece10<6>(wsrc, cvo, w, l);
      if (q == 5) w2_piece10<7>(wsrc, cvo, w, l);
      if (q == 6) w2_piece10<8>(wsrc, cvo, w, l);
      if (q == 7) w2_piece10<9>(wsrc, cvo, w, l);
    }
    sbar();
  }
}

struct G2Ctx {
  const char* wsrc;
  const char* hsrc;
  char* ws;
  LAS char* apA;
  LAS char* apB;
  u32x4 srd, srx;
  u64 tag;
  u32 cvo[5];
  u32 fyoff, yoff0, yoff1, xoff0, xoff2;
  bool fyact;
  int w, l, b;
};
struct G2Tail { u32x2 pf; u32x4 fh[2], yq[2]; u32x2 xv[3]; };

template <int I>
__device__ __forceinline__ void g2_issue1(const char* wsrc, const char* hsrc, const u32 (&cvo)[5], u32 ldst, int w, int l, u32x4 (&hb)[4]) {
  if (I % 2 == 0) w2_piece<I / 2>(wsrc, cvo, ldst, w, l);
  if (I == 1) ldg16s(hb[0], hsrc, l * 16);
  if (I == 3) ldg16so(hb[1], hsrc, l * 16);
  if (I == 5) ldg16s(hb[2], hsrc + 8192, l * 16);
  if (I == 7) ldg16so(hb[3], hsrc + 8192, l * 16);
}
template <int IT, int SS>
__device__ __forceinline__ void g2_aread(const G2Ctx& c, u32x2& ra, u32x2& rb_) {
  LAS char* p = ((IT & 2) ? c.apB : c.apA) + (IT & 1) * 36864 + (SS / 9) * 9216 + (SS % 9) * 32;
  ra = trread(p); rb_ = trread(p + 2304);
}
template <int IT, int SS>
__device__ __forceinline__ void g2_step(const G2Ctx& c, G2Tail& t, u32x4 (&hb)[4][4], f32x4 (&acc)[2][9], u32x2 (&ra)[3], u32x2 (&rb_)[3]) {
  constexpr int u = SS / 9, n = SS % 9;
  const int w = c.w, l = c.l;
  if (SS + 2 < 18) g2_aread<IT, (SS + 2) % 18>(c, ra[(SS + 2) % 3], rb_[(SS + 2) % 3]);
  else if (SS == 17 && IT < 8) g2_aread<IT + 1, 1>(c, ra[1], rb_[1]);
  if (SS == 16) lgwait();
  u32x4 A = {ra[SS % 3][0], ra[SS % 3][1], rb_[SS % 3][0], rb_[SS % 3][1]};
  acc[0][n] = mfma16(A, hb[IT & 3][u * 2], acc[0][n]);
  acc[1][n] = mfma16(A, hb[IT & 3][u * 2 + 1], acc[1][n]);
  if (SS == 16 && IT < 8) {
    if (IT == 0) vmwait<4 + 3 + 5 + 8>();
    else if (IT + 3 < 9) vmwait<9 + 8>();
    else if (IT + 2 < 9) vmwait<9>();
    else vmwait<0>();
    bar();
    PIN(hb[(IT + 1) & 3][0]); PIN(hb[(IT + 1) & 3][1]); PIN(hb[(IT + 1) & 3][2]); PIN(hb[(IT + 1) & 3][3]);
    if (IT == 1 && c.b < 192 && w == 0 && l == 0) flag_set(c.ws, OFF_FY + c.b * 8, c.tag);
    g2_aread<IT + 1, 0>(c, ra[0], rb_[0]);
  }
  if (IT + 3 < 9) {
    const char* ws_ = c.wsrc + (IT + 3) * 73728;
    const char* hs_ = c.hsrc + (IT + 3) * 16384;
    if (SS % 2 == 1 && SS < 16) g2_issue1<(SS % 2 == 1 ? SS / 2 : 0)>(ws_, hs_, c.cvo, ((IT + 3) & 3) * 36864, w, l, hb[(IT + 3) & 3]);
    if (SS == 17) g2_issue1<8>(ws_, hs_, c.cvo, ((IT + 3) & 3) * 36864, w, l, hb[(IT + 3) & 3]);
  }
  if (IT == 7 && SS == 17) {
    ldg8s(t.pf, c.ws, c.fyoff);
    u32 o = (c.b == 0 && w == 7) ? OFF_FH + l * 32 : OOB;
    ldb16s(t.fh[0], c.srd, o); ldb16s(t.fh[1], c.srd, o + 16);
  }
  if (IT == 8 && SS == 12) {
    vmwait<0>();
    PIN(t.pf); PIN(t.fh[0]); PIN(t.fh[1]);
    if (!flag_ok(t.pf, c.fyact, c.tag)) flag_wait(c.ws, c.fyoff, c.fyact, c.tag);
    if (c.b == 0 && w == 7) {
      const u32 lo = (u32)c.tag, hi = (u32)(c.tag >> 32);
      bool ok = t.fh[0][0] == lo && t.fh[0][1] == hi && t.fh[0][2] == lo && t.fh[0][3] == hi &&
                t.fh[1][0] == lo && t.fh[1][1] == hi && t.fh[1][2] == lo && t.fh[1][3] == hi;
      if (!__all(ok))
        for (int i = 0; i < 4; i++) flag_wait(c.ws, OFF_FH + (i * 64 + l) * 8, true, c.tag);
      if (l == 0) flag_set(c.ws, OFF_E, c.tag);
    }
    ldb16s(t.yq[0], c.srd, c.yoff0); ldb16s(t.yq[1], c.srd, c.yoff1);
    ldb8(t.xv[0], c.srx, c.xoff0); ldb8(t.xv[1], c.srx, c.xoff0 + 32); ldb8(t.xv[2], c.srx, c.xoff2);
  }
  sbar();
}
template <int IT, int SS>
__device__ __forceinline__ void g2_steps(const G2Ctx& c, G2Tail& t, u32x4 (&hb)[4][4], f32x4 (&acc)[2][9], u32x2 (&ra)[3], u32x2 (&rb_)[3]) {
  g2_step<IT, SS>(c, t, hb, acc, ra, rb_);
  if constexpr (SS < 17) g2_steps<IT, SS + 1>(c, t, hb, acc, ra, rb_);
  else if constexpr (IT < 8) g2_steps<IT + 1, 0>(c, t, hb, acc, ra, rb_);
}
__device__ __forceinline__ void fin4(f32x4 s, u32x2 xv, u32x2 yv, float m, float rs, u16* op) {
  float res[4];
#pragma unroll
  for (int e = 0; e < 4; e++) {
    float xe = (e & 1) ? bhi(xv[e >> 1]) : blo(xv[e >> 1]);
    float ye = (e & 1) ? bhi(yv[e >> 1]) : blo(yv[e >> 1]);
    float o = rb(s[e] * rs);
    float g = rb(o * __builtin_amdgcn_rcpf(1.0f + __builtin_amdgcn_exp2f(o * -1.44269504088896f)));
    float xn = rb(rb(xe - m) * rs);
    float sv = rb(xn * ye);
    res[e] = xe + rb(sv * g);
  }
  { u32x2 r_ = {pk(res[0], res[1]), pk(res[2], res[3])}; asm volatile("global_store_dwordx2 %0, %1, off sc0 sc1" :: "v"(op), "v"(r_) : "memory"); }
}
__device__ __forceinline__ f32x4 up4(u32 a, u32 b) { return (f32x4){blo(a), bhi(a), blo(b), bhi(b)}; }

__device__ __forceinline__ void conv_g2(G1Ctx& g, const u16* x, const u16* Dv, const u16* w2, u16* out, int rt, int kq, int dn TSARG) {
  LAS char* lds = g.lds;
  char* ws = g.ws;
  const int w = g.w, l = g.l, b = g.b, cb = g.cb;
  const int tg = w & 3, kh = w >> 2, xbit = rt >> 3, i7 = rt & 7, lg = l >> 4;
  G2Ctx c;
  c.ws = ws; c.tag = g.tag; c.w = w; c.l = l; c.b = b;
#pragma unroll
  for (int i = 0; i < 5; i++) {
    u32 idx = i < 4 ? (w * 4 + i) * 64 + l : (32 + (w >> 1)) * 64 + (w & 1) * 32 + (l & 31);
    u32 row = idx / 18, c16 = idx - row * 18;
    c.cvo[i] = ((row >> 6) * 576 + (row & 63)) * 1152 + c16 * 16;
  }
  c.wsrc = (const char*)w2 + (size_t)(1152 * kq) * 1152 + dn * 288;
  c.hsrc = ws + OFF_H + (size_t)((rt * 144 + 36 * kq + 18 * kh) * 8 + 2 * tg) * 1024;
  f32x4 ca[8];
#pragma unroll
  for (int i = 0; i < 8; i++) ca[i] = (f32x4){0, 0, 0, 0};
  const int ch = (w < 3) ? 0 : (w < 6) ? 1 : 2;
  const int r0 = (w == 0 || w == 4 || w == 6) ? 0 : (w == 1 || w == 5) ? 3 : (w == 7) ? 4 : 6;
  const int nr = (w >= 6) ? 4 : (w == 2 || w == 3) ? 2 : 3;
  const int lb = l & 15, cp = (lb + 1) & 3;
  LAS char* ap = lds + L_KC + (ch * 4 + cp) * 4160 + 2 * (lb + 1 - cp) + 16 * lg + 64 * r0;
  LAS char* bp = lds + L_U + ch * 4608 + 2 * (240 - 16 * lb + 8 * lg) + 64 * r0;
  u32x4 A[8], B[8];
  if (b < 192) {
#pragma unroll
    for (int i = 7; i >= 0; i--) {
      B[7 - i] = *(LAS u32x4*)(bp + 512 * i);
      u32x2 lo = *(LAS u32x2*)(ap + 512 * i), hi = *(LAS u32x2*)(ap + 512 * i + 8);
      A[i] = (u32x4){lo[0], lo[1], hi[0], hi[1]};
    }
    conv_res<true>(ca, A, B, ap + 64, bp + 64, c.wsrc, c.cvo, w, l);
  } else {
    w2_piece10<0>(c.wsrc, c.cvo, w, l); w2_piece10<1>(c.wsrc, c.cvo, w, l); w2_piece10<2>(c.wsrc, c.cvo, w, l);
    w2_piece10<3>(c.wsrc, c.cvo, w, l); w2_piece10<4>(c.wsrc, c.cvo, w, l); w2_piece10<5>(c.wsrc, c.cvo, w, l);
    w2_piece10<6>(c.wsrc, c.cvo, w, l); w2_piece10<7>(c.wsrc, c.cvo, w, l); w2_piece10<8>(c.wsrc, c.cvo, w, l);
    w2_piece10<9>(c.wsrc, c.cvo, w, l);
  }
  vmwait<10>();
  if (l == 0) {
    u32 old = __hip_atomic_fetch_add((LAS u32*)(lds + L_CNT), 1u, __ATOMIC_RELAXED, __HIP_MEMORY_SCOPE_WORKGROUP);
    if (old == 7) flag_set(ws, OFF_FH + b * 8, g.tag);
  }
  TS(8);
  if (b < 192) {
#pragma unroll 1
    for (int r = 1; r < nr; r++) conv_res<false>(ca, A, B, ap + 64 * (r + 1), bp + 64 * (r + 1), c.wsrc, c.cvo, w, l);
  }
  TS(9);
  u32x2 uu[3];
#pragma unroll
  for (int i = 0; i < 3; i++) {
    int u = w * 3 + i;
    uu[i] = *(LAS u32x2*)(lds + L_U + (u >> 3) * 4608 + 2 * (2044 - (256 * (u & 7) + 16 * lb + 4 * lg)));
  }
  const u32 fhoff = OFF_FH + ((b & 7) + 8 * ((l & 3) * 8 + i7)) * 8;
  u32x2 pf;
  ldg8s(pf, ws, fhoff);
  lgwait(); bar();
#pragma unroll
  for (int i = 0; i < 8; i++) *(LAS f32x4*)(lds + L_RED + w * 8192 + i * 1024 + l * 16) = ca[i];
  lgwait(); bar();
  TS(10);
  f32x4 ys[3];
#pragma unroll
  for (int i = 0; i < 3; i++) {
    int u = w * 3 + i, c2 = u >> 3;
    LAS char* rp = lds + L_RED + c2 * 3 * 8192 + (u & 7) * 1024 + l * 16;
    ys[i] = *(LAS f32x4*)(rp) + *(LAS f32x4*)(rp + 8192);
    if (c2 < 2) ys[i] += *(LAS f32x4*)(rp + 2 * 8192);
  }
  vmwait<0>();
  PIN(pf);
  if (!flag_ok(pf, true, g.tag)) flag_wait(ws, fhoff, true, g.tag);
  TS(11);
  u32x4 hb[4][4];
#pragma unroll
  for (int it = 0; it < 3; it++) {
    ldg16s(hb[it][0], c.hsrc + it * 16384, l * 16); ldg16so(hb[it][1], c.hsrc + it * 16384, l * 16);
    ldg16s(hb[it][2], c.hsrc + it * 16384 + 8192, l * 16); ldg16so(hb[it][3], c.hsrc + it * 16384 + 8192, l * 16);
  }
#pragma unroll
  for (int i = 0; i < 3; i++) {
    int u = w * 3 + i, chn = cb * 3 + (u >> 3);
    u32 dw = ((CAS const u32*)Dv)[chn >> 1];
    float dv = (chn & 1) ? bhi(dw) : blo(dw);
    float o0 = rb(ys[i][0]) + rb(bhi(uu[i][1]) * dv), o1 = rb(ys[i][1]) + rb(blo(uu[i][1]) * dv);
    float o2 = rb(ys[i][2]) + rb(bhi(uu[i][0]) * dv), o3 = rb(ys[i][3]) + rb(blo(uu[i][0]) * dv);
    u32 o = b < 192 ? OFF_Y2 + chn * 4096 + (256 * (u & 7) + 16 * lb + 4 * lg) * 2 : OOB;
    __builtin_amdgcn_raw_buffer_store_b64((u32x2){pk(o0, o1), pk(o2, o3)}, g.wr, o, 0, SC);
  }
  lgwait(); bar();
  w2_piece<0>(c.wsrc + 2 * 73728, c.cvo, 73728, w, l); w2_piece<1>(c.wsrc + 2 * 73728, c.cvo, 73728, w, l);
  w2_piece<2>(c.wsrc + 2 * 73728, c.cvo, 73728, w, l); w2_piece<3>(c.wsrc + 2 * 73728, c.cvo, 73728, w, l);
  w2_piece<4>(c.wsrc + 2 * 73728, c.cvo, 73728, w, l);
  c.apA = lds + (kh * 64 + 16 * (lg >> 1) + 4 * (lg & 1) + ((l >> 2) & 3)) * 288 + (l & 3) * 8;
  c.apB = c.apA + 73728;
  c.srd = mksrd(ws, WS_END);
  c.srx = mksrd(x, S_ * D_ * 2);
  const int p = w & 3, tt = w >> 2;
  const int tok = rt * 128 + kq * 32 + tt * 16 + lb, d0 = dn * 144 + 4 * lg;
  c.fyact = l < 48;
  c.fyoff = OFF_FY + (48 * dn + (l < 48 ? l : 0)) * 8;
  {
    u32 yb = OFF_Y2 + (dn * 144 + w * 16 + (l >> 2)) * 4096 + (rt * 128 + kq * 32) * 2 + (l & 3) * 16;
    c.yoff0 = yb;
    c.yoff1 = w == 0 ? yb + 128 * 4096 : OOB;
    c.xoff0 = tok * 1152 + (d0 + p * 32) * 2;
    c.xoff2 = p == 0 ? tok * 1152 + (d0 + 128) * 2 : OOB;
  }
  f32x4 acc[2][9];
#pragma unroll
  for (int n = 0; n < 9; n++) { acc[0][n] = (f32x4){0, 0, 0, 0}; acc[1][n] = (f32x4){0, 0, 0, 0}; }
  G2Tail t;
  u32x2 ra[3], rb_[3];
  g2_aread<0, 0>(c, ra[0], rb_[0]); g2_aread<0, 1>(c, ra[1], rb_[1]);
  vmwait<8 + 3 + 5>();
  PIN(hb[0][0]); PIN(hb[0][1]); PIN(hb[0][2]); PIN(hb[0][3]);
  TS(12);
  g2_steps<0, 0>(c, t, hb, acc, ra, rb_);
  TS(13);
#pragma unroll
  for (int n = 0; n < 9; n++) *(LAS f32x4*)(lds + L_EX + w * 9216 + n * 1024 + l * 16) = kh ? acc[0][n] : acc[1][n];
  lgwait(); bar();
  f32x4 r[9];
#pragma unroll
  for (int n = 0; n < 9; n++) r[n] = (kh ? acc[1][n] : acc[0][n]) + *(LAS f32x4*)(lds + L_EX + (w ^ 4) * 9216 + n * 1024 + l * 16);
  {
    int ob = 2 * tg + xbit + 8 * (dn * 8 + i7);
    u32 po = tg != kq ? OFF_P + (ob * 3 + ((kq - tg - 1) & 3)) * 9216 + kh * 4608 : OOB;
#pragma unroll
    for (int q = 0; q < 4; q++)
      __builtin_amdgcn_raw_buffer_store_b128((u32x4){pk(r[2 * q][0], r[2 * q][1]), pk(r[2 * q][2], r[2 * q][3]),
                                                     pk(r[2 * q + 1][0], r[2 * q + 1][1]), pk(r[2 * q + 1][2], r[2 * q + 1][3])},
                                             g.wr, po + q * 1024 + l * 16, 0, SC);
    __builtin_amdgcn_raw_buffer_store_b64((u32x2){pk(r[8][0], r[8][1]), pk(r[8][2], r[8][3])}, g.wr, po + 4096 + l * 8, 0, SC);
    if (tg == kq) {
#pragma unroll
      for (int n = 0; n < 9; n++) *(LAS f32x4*)(lds + L_OWN + kh * 9216 + n * 1024 + l * 16) = r[n];
    }
  }
  vmwait<5>();
  PIN(t.yq[0]); PIN(t.yq[1]); PIN(t.xv[0]); PIN(t.xv[1]); PIN(t.xv[2]);
  *(LAS u32x4*)(lds + L_Y + (w * 16 + (l >> 2)) * 96 + (l & 3) * 16) = t.yq[0];
  if (w == 0) *(LAS u32x4*)(lds + L_Y + (128 + (l >> 2)) * 96 + (l & 3) * 16) = t.yq[1];
  vmwait<0>(); lgwait(); bar();
  if (w == 0 && l == 0) flag_set(ws, OFF_FX + b * 8, g.tag);
  TS(14);
  const u32 fxoff = OFF_FX + (2 * ((kq + 1 + (l < 3 ? l : 0)) & 3) + xbit + 8 * (dn * 8 + i7)) * 8;
  flag_wait(ws, fxoff, l < 3, g.tag);
  TS(15);
  u32x4 pq[3];
  u32x2 p8[3];
  {
    u32 po = OFF_P + b * 3 * 9216 + tt * 4608;
    u32 o8 = p == 0 ? po + 4096 + l * 8 : OOB;
#pragma unroll
    for (int s = 0; s < 3; s++) {
      pq[s] = __builtin_amdgcn_raw_buffer_load_b128(g.wr, po + s * 9216 + p * 1024 + l * 16, 0, SC);
      p8[s] = __builtin_amdgcn_raw_buffer_load_b64(g.wr, o8 + s * 9216, 0, SC);
    }
  }
  u32 st = *(LAS u32*)(lds + L_ST + (kq * 32 + tt * 16 + lb) * 4);
  float m = blo(st), rs = 1.0f / bhi(st);
  LAS char* yp = lds + L_Y + (4 * lg + ((l >> 2) & 3)) * 96 + tt * 32 + (l & 3) * 8;
  LAS char* op = lds + L_OWN + tt * 9216 + l * 16;
  u32x2 y0 = trread(yp + p * 32 * 96), y1 = trread(yp + (p * 32 + 16) * 96), y2 = trread(yp + 128 * 96);
  f32x4 s0 = *(LAS f32x4*)(op + 2 * p * 1024), s1 = *(LAS f32x4*)(op + (2 * p + 1) * 1024), s2 = *(LAS f32x4*)(op + 8 * 1024);
#pragma unroll
  for (int s = 0; s < 3; s++) { s0 += up4(pq[s][0], pq[s][1]); s1 += up4(pq[s][2], pq[s][3]); }
  u16* o = out + tok * D_ + d0;
  fin4(s0, t.xv[0], y0, m, rs, o + p * 32);
  fin4(s1, t.xv[1], y1, m, rs, o + p * 32 + 16);
  if (p == 0) {
#pragma unroll
    for (int s = 0; s < 3; s++) s2 += up4(p8[s][0], p8[s][1]);
    fin4(s2, t.xv[2], y2, m, rs, o + 128);
  }
}

__global__ void __launch_bounds__(512, 2) k_fused(const u16* x, const u16* k, const u16* v, const u16* sk, const u16* ssk, const u16* Dv,
                                                  const u16* w1, const u16* w2, u16* out, char* ws) {
  extern __shared__ __attribute__((aligned(1024))) char smem[];
  const int tid = threadIdx.x, b = blockIdx.x;
  const int xc = b & 7, i = b >> 3;
  const int rt = (xc & 1) * 8 + (i & 7), kq = xc >> 1, dn = i >> 3;
  G1Ctx c;
  c.lds = (LAS char*)smem;
  c.ws = ws;
  c.ssk = ssk;
  c.wr = mkrsrc(ws, WS_END);
  c.b = b; c.cb = b < 192 ? b : 191;
  c.w = __builtin_amdgcn_readfirstlane(tid >> 6); c.l = tid & 63;
  g1_phase(c, x, k, v, sk, w1, w2, rt, 4 * kq + dn, kq TSPASS);
  conv_g2(c, x, Dv, w2, out, rt, kq, dn TSPASS);
}

extern "C" void kernel_launch(void* const* d_in, const int* in_sizes, int n_in, void* d_out, int out_size, void* d_ws, size_t ws_size,
                              hipStream_t stream) {
  (void)in_sizes; (void)n_in; (void)out_size; (void)ws_size;
  (void)hipFuncSetAttribute((const void*)k_fused, hipFuncAttributeMaxDynamicSharedMemorySize, LDS_BYTES);
  k_fused<<<256, 512, LDS_BYTES, stream>>>((const u16*)d_in[0], (const u16*)d_in[1], (const u16*)d_in[2], (const u16*)d_in[3],
                                           (const u16*)d_in[4], (const u16*)d_in[5], (const u16*)d_in[7], (const u16*)d_in[8],
                                           (u16*)d_out, (char*)d_ws);
}
```

```cpp
#include <hip/hip_runtime.h>
#include <stdint.h>

typedef unsigned short u16;
typedef unsigned int u32;
typedef unsigned long long u64;
#define LAS __attribute__((address_space(3)))
#define CAS __attribute__((address_space(4)))
typedef __attribute__((ext_vector_type(8))) __bf16 bf16x8;
typedef __attribute__((ext_vector_type(2))) __bf16 bf16x2;
typedef __attribute__((ext_vector_type(2))) float f32x2;
typedef __attribute__((ext_vector_type(4))) float f32x4;
typedef __attribute__((ext_vector_type(16))) float f32x16;
typedef __attribute__((ext_vector_type(2))) u32 u32x2;
typedef __attribute__((ext_vector_type(4))) u32 u32x4;
typedef __attribute__((ext_vector_type(4))) short v4i16;

static constexpr int S_ = 2048, D_ = 576, F_ = 4608;
static constexpr u32 OFF_E = 0;
static constexpr u32 OFF_FK = 4096;
static constexpr u32 OFF_FH = OFF_FK + 2048;
static constexpr u32 OFF_FY = OFF_FH + 2048;
static constexpr u32 OFF_FX = OFF_FY + 2048;
static constexpr u32 OFF_STATS = 65536;
static constexpr u32 OFF_CS = OFF_STATS + 16384;
static constexpr u32 OFF_UR = 262144;
static constexpr u32 OFF_Y2 = OFF_UR + 576 * 4096;
static constexpr u32 OFF_H = OFF_Y2 + 576 * 4096;
static constexpr u32 OFF_P = OFF_H + 2048 * 4608 * 2;
static constexpr u32 WS_END = OFF_P + 256 * 3 * 9216;
static constexpr int LDS_BYTES = 163840;
static constexpr int SC = 16;
static constexpr u32 OOB = 0xF0000000u;
static constexpr u32 EMPTY = 0xFFFFFFFFu;
static constexpr u32 L_B1 = 73728, L_S1 = 147456, L_CSS = 159744, L_CS = 161792, L_ST = 162944, L_CNT = 163456;

#define TS(i)
#define TSARG
#define TSPASS

__device__ __forceinline__ float blo(u32 v) { return __uint_as_float(v << 16); }
__device__ __forceinline__ float bhi(u32 v) { return __uint_as_float(v & 0xffff0000u); }
__device__ __forceinline__ u32 pk(float lo, float hi) {
  f32x2 t = {lo, hi};
  return __builtin_bit_cast(u32, __builtin_convertvector(t, bf16x2));
}
__device__ __forceinline__ float rb(float x) { return blo(pk(x, x)); }
template <int C> __device__ __forceinline__ float dppf(float v) {
  return __int_as_float(__builtin_amdgcn_update_dpp(0, __float_as_int(v), C, 0xf, 0xf, false));
}
__device__ __forceinline__ float swp32add(float a, float b) {
  auto r = __builtin_amdgcn_permlane32_swap(__float_as_uint(a), __float_as_uint(b), false, false);
  return __uint_as_float(r[0]) + __uint_as_float(r[1]);
}
__device__ __forceinline__ float swp16add(float a, float b) {
  auto r = __builtin_amdgcn_permlane16_swap(__float_as_uint(a), __float_as_uint(b), false, false);
  return __uint_as_float(r[0]) + __uint_as_float(r[1]);
}
__device__ __forceinline__ float wave_sum(float v) {
  v += dppf<0x128>(v); v += dppf<0x124>(v); v += dppf<0x122>(v); v += dppf<0x121>(v);
  v = swp16add(v, v);
  return swp32add(v, v);
}
__device__ __forceinline__ void glds(const void* sbase, u32 voff, u32 ldsdst) {
  asm volatile("s_mov_b32 m0, %2\n\ts_nop 0\n\tglobal_load_lds_dwordx4 %0, %1" ::"v"(voff), "s"(sbase), "s"(ldsdst) : "memory");
}
__device__ __forceinline__ void blds(u32x4 srd, u32 voff, u32 ldsdst) {
  asm volatile("s_mov_b32 m0, %2\n\ts_nop 0\n\tbuffer_load_dwordx4 %0, %1, 0 offen lds" ::"v"(voff), "s"(srd), "s"(ldsdst) : "memory");
}
__device__ __forceinline__ void glds4(const void* sbase, u32 voff, u32 ldsdst) {
  asm volatile("s_mov_b32 m0, %2\n\ts_nop 0\n\tglobal_load_lds_dword %0, %1" ::"v"(voff), "s"(sbase), "s"(ldsdst) : "memory");
}
__device__ __forceinline__ void ldg16(u32x4& d, const void* sbase, u32 voff) {
  asm volatile("global_load_dwordx4 %0, %1, %2" : "=v"(d) : "v"(voff), "s"(sbase) : "memory");
}
__device__ __forceinline__ void ldg16s(u32x4& d, const void* sbase, u32 voff) {
  asm volatile("global_load_dwordx4 %0, %1, %2 sc1" : "=v"(d) : "v"(voff), "s"(sbase) : "memory");
}
__device__ __forceinline__ void ldg16so(u32x4& d, const void* sbase, u32 voff) {
  asm volatile("global_load_dwordx4 %0, %1, %2 offset:1024 sc1" : "=v"(d) : "v"(voff), "s"(sbase) : "memory");
}
__device__ __forceinline__ void ldg8(u32x2& d, const void* sbase, u32 voff) {
  asm volatile("global_load_dwordx2 %0, %1, %2" : "=v"(d) : "v"(voff), "s"(sbase) : "memory");
}
__device__ __forceinline__ void ldg8s(u32x2& d, const void* sbase, u32 voff) {
  asm volatile("global_load_dwordx2 %0, %1, %2 sc1" : "=v"(d) : "v"(voff), "s"(sbase) : "memory");
}
__device__ __forceinline__ void ldb8(u32x2& d, u32x4 srd, u32 voff) {
  asm volatile("buffer_load_dwordx2 %0, %1, %2, 0 offen" : "=v"(d) : "v"(voff), "s"(srd) : "memory");
}
__device__ __forceinline__ void ldb8s(u32x2& d, u32x4 srd, u32 voff) {
  asm volatile("buffer_load_dwordx2 %0, %1, %2, 0 offen sc1" : "=v"(d) : "v"(voff), "s"(srd) : "memory");
}
__device__ __forceinline__ void ldb16s(u32x4& d, u32x4 srd, u32 voff) {
  asm volatile("buffer_load_dwordx4 %0, %1, %2, 0 offen sc1" : "=v"(d) : "v"(voff), "s"(srd) : "memory");
}
__device__ __forceinline__ void ldb16(u32x4& d, u32x4 srd, u32 voff) {
  asm volatile("buffer_load_dwordx4 %0, %1, %2, 0 offen" : "=v"(d) : "v"(voff), "s"(srd) : "memory");
}
template <int N> __device__ __forceinline__ void vmwait() { asm volatile("s_waitcnt vmcnt(%0)" ::"n"(N) : "memory"); }
__device__ __forceinline__ void lgwait() { asm volatile("s_waitcnt lgkmcnt(0)" ::: "memory"); }
__device__ __forceinline__ void bar() { asm volatile("s_barrier" ::: "memory"); }
#define PIN(x) asm volatile("" : "+v"(x))
__device__ __forceinline__ void sbar() { __builtin_amdgcn_sched_barrier(0); }
__device__ __forceinline__ __amdgpu_buffer_rsrc_t mkrsrc(const void* p, u32 bytes) {
  return __builtin_amdgcn_make_buffer_rsrc((void*)p, 0, bytes, 0x00020000);
}
__device__ __forceinline__ u32x4 mksrd(const void* p, u32 bytes) {
  u64 b = (u64)p;
  return (u32x4){(u32)b, (u32)(b >> 32) & 0xffffu, bytes, 0x00020000u};
}
__device__ __forceinline__ f32x4 mfma16(u32x4 a, u32x4 b, f32x4 c) {
  return __builtin_amdgcn_mfma_f32_16x16x32_bf16(__builtin_bit_cast(bf16x8, a), __builtin_bit_cast(bf16x8, b), c, 0, 0, 0);
}
__device__ __forceinline__ f32x16 mfma32(u32x4 a, u32x4 b, f32x16 c) {
  return __builtin_amdgcn_mfma_f32_32x32x16_bf16(__builtin_bit_cast(bf16x8, a), __builtin_bit_cast(bf16x8, b), c, 0, 0, 0);
}
__device__ __forceinline__ u32x2 trread(LAS char* p) {
  return __builtin_bit_cast(u32x2, __builtin_amdgcn_ds_read_tr16_b64_v4i16((LAS v4i16*)p));
}
__device__ __forceinline__ void flag_set(char* ws, u32 off, u64 tag) {
  __hip_atomic_store((__attribute__((address_space(1))) u64*)(ws + off), tag, __ATOMIC_RELAXED, __HIP_MEMORY_SCOPE_AGENT);
}
__device__ __forceinline__ void flag_wait(char* ws, u32 off, bool act, u64 tag) {
#pragma unroll 1
  for (int spins = 0; spins < 4000000; spins++) {
    u64 f = __hip_atomic_load((__attribute__((address_space(1))) u64*)(ws + off), __ATOMIC_RELAXED, __HIP_MEMORY_SCOPE_AGENT);
    if (__all(f == tag || !act)) break;
    __builtin_amdgcn_s_sleep(1);
  }
  asm volatile("" ::: "memory");
}
__device__ __forceinline__ bool flag_ok(u32x2 f, bool act, u64 tag) {
  return __all(!act || (f[0] == (u32)tag && f[1] == (u32)(tag >> 32)));
}

__device__ __forceinline__ void lds_arrive(LAS char* p, int l) {
  if (l == 0) __hip_atomic_fetch_add((LAS u32*)p, 1u, __ATOMIC_RELAXED, __HIP_MEMORY_SCOPE_WORKGROUP);
}
__device__ __forceinline__ void lds_await(LAS char* p, u32 n) {
#pragma unroll 1
  for (int spins = 0; spins < 40000000; spins++) {
    u32 v = __builtin_amdgcn_readfirstlane(__hip_atomic_load((LAS u32*)p, __ATOMIC_RELAXED, __HIP_MEMORY_SCOPE_WORKGROUP));
    if (v >= n) break;
  }
  asm volatile("" ::: "memory");
}

static constexpr u32 L_KC = L_B1;
static constexpr u32 L_U = L_B1 + 12 * 4160;

struct G1Ctx {
  LAS char* lds;
  char* ws;
  const u16* ssk;
  __amdgpu_buffer_rsrc_t wr;
  float nm, cc;
  u64 tag, pc;
  u32 clen;
  int b, cb, w, l;
};

template <int MODE>
__device__ __forceinline__ void g1_pair(const G1Ctx& c, const u32x4 (&xf)[36], f32x16& acc, const f32x16& pa, LAS char* a0, LAS char* n0,
                                        u32x2 (&ra)[3], u32x2 (&rb_)[3], f32x4 (&cs)[2], LAS char* csp, LAS char* csn, u32 hoff,
                                        const char* dsrc, u32 dvoff, u32 ddst, int pp, u32 kfoff, bool kfact) {
  LAS char* a1 = a0 + 18 * 2048;
  const int w = c.w, l = c.l;
  float X[16], E[16];
  u32 P[8];
  u32x2 pf, k0;
  u32x4 uq[2];
#pragma unroll
  for (int ks = 0; ks < 36; ks++) {
    if (ks + 2 < 36) {
      LAS char* ap = (ks + 2 < 18 ? a0 + (ks + 2) * 2048 : a1 + (ks + 2 - 18) * 2048);
      ra[(ks + 2) % 3] = trread(ap); rb_[(ks + 2) % 3] = trread(ap + 512);
    }
    if (MODE != 2 && ks == 35) { ra[1] = trread(n0 + 2048); rb_[1] = trread(n0 + 2048 + 512); }
    if (ks == 34) lgwait();
    u32x4 A = {ra[ks % 3][0], ra[ks % 3][1], rb_[ks % 3][0], rb_[ks % 3][1]};
    if (ks == 0) { f32x16 z = {}; acc = mfma32(A, xf[0], z); }
    else acc = mfma32(A, xf[ks], acc);
    if (ks == 34) {
      vmwait<1>();
      if (MODE == 0) {
        PIN(pf);
        if (pp == 1 && !flag_ok(pf, kfact, c.tag)) flag_wait(c.ws, kfoff, kfact, c.tag);
      }
      bar();
      if (MODE == 0 && pp == 0 && w == 0 && l == 0) flag_set(c.ws, OFF_FK + c.b * 8, c.tag);
      if (MODE != 2) { ra[0] = trread(n0); rb_[0] = trread(n0 + 512); }
    }
    if (ks % 8 == 6 && ks / 8 < 3) cs[(ks / 8 + 1) & 1] = *(LAS f32x4*)(csp + (ks / 8 + 1) * 32);
    if (ks % 2 == 1 && ks / 2 < 16) {
      int e = ks / 2;
      float xx = __builtin_fmaf(c.nm, cs[(e >> 2) & 1][e & 3], pa[e]);
      PIN(xx);
      float tt = xx * c.cc;
      PIN(tt);
      X[e] = xx; E[e] = tt;
    }
    if (ks % 2 == 0 && ks >= 2 && ks / 2 - 1 < 16) {
      int e = ks / 2 - 1;
      float tt = __builtin_amdgcn_exp2f(E[e]);
      PIN(tt);
      E[e] = tt;
    }
    if (ks % 2 == 1 && ks >= 3 && (ks - 3) / 2 < 16) {
      int e = (ks - 3) / 2;
      float tt = 1.0f + E[e];
      PIN(tt);
      tt = __builtin_amdgcn_rcpf(tt);
      PIN(tt);
      E[e] = tt;
    }
    if (ks % 2 == 0 && ks >= 4 && (ks - 4) / 2 < 16) {
      int e = (ks - 4) / 2;
      float hh = X[e] * E[e];
      PIN(hh);
      X[e] = hh;
      if (e & 1) { u32 pq = pk(X[e - 1], X[e]); PIN(pq); P[e >> 1] = pq; }
    }
    if (ks == 19) __builtin_amdgcn_raw_buffer_store_b128((u32x4){P[0], P[1], P[2], P[3]}, c.wr, hoff, 0, SC);
    if (ks == 35) {
      __builtin_amdgcn_raw_buffer_store_b128((u32x4){P[4], P[5], P[6], P[7]}, c.wr, hoff + 512, 0, SC);
      if (MODE != 2) cs[0] = *(LAS f32x4*)(csn);
    }
    if (MODE != 2) {
      if (ks % 2 == 0 && ks / 2 < 9) glds(dsrc + (size_t)(ks / 2) * (64 * 9216), dvoff, ddst + (ks / 2) * 8192);
      if (MODE == 0 && ks == 1) ldg8s(pf, c.ws, kfoff);
    } else {
      if (ks % 2 == 0 && ks / 2 < 6) {
        int idx = w * 6 + ks / 2, cc = idx >> 2, part = idx & 3;
        u32x4 srd = mksrd(c.ssk + (size_t)(c.cb * 3 + (cc >> 2)) * 2048, 4096u);
        blds(srd, (cc & 3) * 2 + part * 1024 + l * 16, L_KC + cc * 4160 + 32 + part * 1024);
      }
      if (ks == 12 || ks == 14) {
        int idx = (w < 6 ? w : 5) * 2 + (ks - 12) / 2;
        ldg16s(uq[(ks - 12) / 2], c.ws + OFF_UR + (size_t)(c.cb * 3 + (idx >> 2)) * 4096 + (idx & 3) * 1024, l * 16);
      }
      if (ks == 16) ldg8(k0, c.ssk + (size_t)c.cb * 3 * 2048, (l < 8 ? l >> 2 : 2) * 4096);
      if (ks == 3) {
        if (w == 6 && l < 12) *(LAS u32x4*)(c.lds + L_KC + l * 4160) = (u32x4){0, 0, 0, 0};
        if (w < 3 && l < 32) *(LAS u32x4*)(c.lds + L_U + w * 4608 + 4096 + l * 16) = (u32x4){0, 0, 0, 0};
      }
      if (ks == 30) {
        vmwait<1>();
        PIN(uq[0]); PIN(uq[1]); PIN(k0);
        if (w < 6) {
          int idx = w * 2;
          *(LAS u32x4*)(c.lds + L_U + (idx >> 2) * 4608 + (idx & 3) * 1024 + l * 16) = uq[0];
          *(LAS u32x4*)(c.lds + L_U + (idx >> 2) * 4608 + ((idx & 3) + 1) * 1024 + l * 16) = uq[1];
        }
        if (w == 6 && l < 12) {
          int cp = l & 3;
          u32x4 c1 = {0, 0, 0, 0};
          if (cp == 1) c1[3] = k0[0] << 16;
          if (cp == 2) c1[3] = k0[0];
          if (cp == 3) { c1[2] = k0[0] << 16; c1[3] = (k0[0] >> 16) | (k0[1] << 16); }
          *(LAS u32x4*)(c.lds + L_KC + l * 4160 + 16) = c1;
        }
      }
    }
    sbar();
  }
}

__device__ __forceinline__ void g1_epi_final(const G1Ctx& c, const f32x16& pa, u32 csoff, u32 hoff) {
  u32 P[8];
#pragma unroll
  for (int q = 0; q < 4; q++) {
    f32x4 cs = *(LAS f32x4*)(c.lds + csoff + q * 32);
    float h[4];
#pragma unroll
    for (int i = 0; i < 4; i++) {
      float X = __builtin_fmaf(c.nm, cs[i], pa[4 * q + i]);
      h[i] = X * __builtin_amdgcn_rcpf(1.0f + __builtin_amdgcn_exp2f(X * c.cc));
    }
    P[2 * q] = pk(h[0], h[1]); P[2 * q + 1] = pk(h[2], h[3]);
  }
  __builtin_amdgcn_raw_buffer_store_b128((u32x4){P[0], P[1], P[2], P[3]}, c.wr, hoff, 0, SC);
  __builtin_amdgcn_raw_buffer_store_b128((u32x4){P[4], P[5], P[6], P[7]}, c.wr, hoff + 512, 0, SC);
}

__device__ __forceinline__ void kv_compute(const u32x2 (&kr)[11], const u32x2 (&vr)[8], const u32x4 (&sq)[2],
                                           __amdgpu_buffer_rsrc_t wr, int ch, int t) {
#pragma unroll
  for (int q = 0; q < 4; q++) {
    u32 sa = sq[q >> 1][(q & 1) * 2], sb = sq[q >> 1][(q & 1) * 2 + 1];
    float w0 = blo(sa), w1 = bhi(sa), w2 = blo(sb), w3 = bhi(sb);
    float kk[11], o[8];
#pragma unroll
    for (int i = 0; i < 11; i++) kk[i] = (q & 1) ? bhi(kr[i][q >> 1]) : blo(kr[i][q >> 1]);
#pragma unroll
    for (int i = 0; i < 8; i++) {
      float vv = (q & 1) ? bhi(vr[i][q >> 1]) : blo(vr[i][q >> 1]);
      o[i] = (kk[i] * w3 + kk[i + 1] * w2 + kk[i + 2] * w1 + kk[i + 3] * w0) * vv;
    }
    u32x4 r = {pk(o[7], o[6]), pk(o[5], o[4]), pk(o[3], o[2]), pk(o[1], o[0])};
    __builtin_amdgcn_raw_buffer_store_b128(r, wr, OFF_UR + (ch + q) * 4096 + 2 * (2040 - t), 0, SC);
    sbar();
  }
}

__device__ __forceinline__ u32 ln_stats(const u32x4 (&sr)[2], int l) {
  float e[16];
#pragma unroll
  for (int i = 0; i < 8; i++) { e[2 * i] = blo(sr[i >> 2][i & 3]); e[2 * i + 1] = bhi(sr[i >> 2][i & 3]); }
  float s = 0;
#pragma unroll
  for (int i = 0; i < 16; i++) s += e[i];
  if (l >= 36) s = 0;
  float mean = wave_sum(s) * (1.0f / 576.0f);
  float vv = 0;
#pragma unroll
  for (int i = 0; i < 16; i++) { float d = e[i] - mean; vv += d * d; }
  if (l >= 36) vv = 0;
  float var = rb(wave_sum(vv) * (1.0f / 576.0f));
  float sd = rb(__builtin_sqrtf(rb(var + 1e-5f)));
  return pk(mean, sd);
}

__device__ __forceinline__ void g1_phase(G1Ctx& c, const u16* x, const u16* k, const u16* v, const u16* sk, const u16* w1, const u16* w2,
                                         int rt, int j, int kq TSARG) {
  LAS char* lds = c.lds;
  char* ws = c.ws;
  const int w = c.w, l = c.l, b = c.b, tid = w * 64 + l;
  const int tg = w & 3, fg = w >> 2, odd = j & 1, kh = l >> 5;
  const char* w1b = (const char*)w1 + j * 576;
  const char* sb = w1b + (odd ? 0 : 512);
  const char* pb = w1b + (odd ? 64 : 0);
  const u32 svo = (l >> 2) * 9216 + (l & 3) * 16;
  const bool kvw = w >= 5 && b < 192;
  const int kid = 3 * b + w - 5, kch = (kid >> 6) * 64 + (l & 15) * 4, kt = (kid & 63) * 32 + (l >> 4) * 8;
  if (tid == 0) *(LAS u32x2*)(lds + L_CNT) = (u32x2){0, 0};
  if (w < 4) {
    u32x2 ep;
    ldg8s(ep, ws + OFF_E, 0);
    const char* xb = (const char*)x + (size_t)rt * 128 * 1152;
#pragma unroll
    for (int h = 0; h < 4; h++) {
      u32 vo = (32 * w + 8 * h + (l >> 3)) * 1152 + (((l & 7) ^ (4 * (h & 1) + (l >> 4))) * 16);
#pragma unroll
      for (int kb = 0; kb < 9; kb++) glds(xb + kb * 128, vo, kb * 16384 + (32 * w + 8 * h) * 128);
    }
#pragma unroll
    for (int i = 0; i < 3; i++) glds(sb + (size_t)(w * 3 + i) * 16 * 9216, svo, L_S1 + (w * 3 + i) * 1024);
    {
      int n = ((b >> 3) & 7) * 120 + w * 30 + (l < 30 ? l : 29);
      u32 o = n < 384 ? (192 + n) * 9216 + (odd ? 0 : 512) : (n - 384) * 9216 + (odd ? 64 : 0);
      glds4(w1b, o, L_CSS + w * 256);
    }
    vmwait<1>();
    PIN(ep);
    c.tag = (((u64)(u32)__builtin_amdgcn_readfirstlane(ep[1]) << 32) | (u32)__builtin_amdgcn_readfirstlane(ep[0])) + 1;
  } else {
    const int pw = w - 4;
    u32x2 ep;
    ldg8s(ep, ws + OFF_E, 0);
    __builtin_amdgcn_s_setprio(3);
    u32x4 sr[2][2], cq[6];
    const int stok = rt * 128 + j * 8 + 2 * pw;
    {
      u32 o = stok * 1152 + (l < 36 ? l : 35) * 32;
      ldg16(sr[0][0], x, o); ldg16(sr[0][1], x, o + 16); ldg16(sr[1][0], x, o + 1152); ldg16(sr[1][1], x, o + 1168);
    }
    const int cuq = (b & 1) * 32 + (b >> 3);
    const int cu = cuq < 54 ? cuq : 53;
    const int ccl = 18 * kq + cu % 18, cpp = cu / 18;
    {
      u32 o = (cpp * 192 + (l >> 1)) * 9216 + ccl * 128 + pw * 32 + (l & 1) * 16;
#pragma unroll
      for (int i = 0; i < 6; i++) ldg16(cq[i], w1, o + i * 32 * 9216);
    }
    vmwait<6>();
    PIN(ep); PIN(sr[0][0]); PIN(sr[0][1]); PIN(sr[1][0]); PIN(sr[1][1]);
    c.tag = (((u64)(u32)__builtin_amdgcn_readfirstlane(ep[1]) << 32) | (u32)__builtin_amdgcn_readfirstlane(ep[0])) + 1;
    {
      u32 s0 = ln_stats(sr[0], l), s1 = ln_stats(sr[1], l);
      __builtin_amdgcn_raw_buffer_store_b64((u32x2){l ? s1 : s0, (u32)c.tag}, c.wr, l < 2 ? OFF_STATS + (stok + l) * 8 : OOB, 0, SC);
    }
    vmwait<1>();
#pragma unroll
    for (int i = 0; i < 6; i++) PIN(cq[i]);
    {
      float a[8];
#pragma unroll
      for (int i = 0; i < 4; i++) {
        a[2 * i] = (blo(cq[0][i]) + blo(cq[1][i])) + (blo(cq[2][i]) + blo(cq[3][i])) + (blo(cq[4][i]) + blo(cq[5][i]));
        a[2 * i + 1] = (bhi(cq[0][i]) + bhi(cq[1][i])) + (bhi(cq[2][i]) + bhi(cq[3][i])) + (bhi(cq[4][i]) + bhi(cq[5][i]));
      }
      float b0 = swp32add(a[0], a[4]), b1 = swp32add(a[1], a[5]), b2 = swp32add(a[2], a[6]), b3 = swp32add(a[3], a[7]);
      float c0 = swp16add(b0, b2), c1 = swp16add(b1, b3);
      c0 += dppf<0x128>(c0); c1 += dppf<0x128>(c1);
      c0 += dppf<0x124>(c0); c1 += dppf<0x124>(c1);
      c0 += dppf<0x122>(c0); c1 += dppf<0x122>(c1);
      u32 col = ccl * 64 + pw * 16 + (l & 1) * 8 + (l >> 5) * 4 + ((l >> 4) & 1) * 2;
      __builtin_amdgcn_raw_buffer_store_b128((u32x4){__float_as_uint(c0), (u32)c.tag, __float_as_uint(c1), (u32)c.tag}, c.wr,
                                             (cuq < 54 && (l & 14) == 0) ? OFF_CS + (cpp * F_ + col) * 8 : OOB, 0, SC);
    }
    __builtin_amdgcn_s_setprio(0);
  }
  lgwait(); bar();
  TS(1);
  u32x4 xf[36];
  auto xread = [&]() {
    u32 row = tg * 32 + (l & 31);
#pragma unroll
    for (int q = 0; q < 4; q++) {
      u32 a = row * 128 + (((2 * q + (l >> 5)) ^ ((row >> 1) & 7)) * 16);
#pragma unroll
      for (int kb = 0; kb < 9; kb++) xf[kb * 4 + q] = *(LAS u32x4*)(lds + a + kb * 16384);
    }
    lgwait(); bar();
    TS(2);
  };
  const u32 pvo = (l >> 3) * 9216 + (((l & 7) ^ (((l >> 4) & 1) << 2)) * 16);
  const char* pwp = pb + (size_t)w * 8 * 9216;
  f32x16 acc0 = {}, acc1 = {};
  if (w < 4) {
    xread();
#pragma unroll
    for (int i = 0; i < 6; i++) glds(sb + (size_t)(12 + w + 4 * i) * 16 * 9216, svo, (w + 4 * i) * 1024);
    u32 sa = (8 * kh + ((l >> 2) & 3)) * 64 + ((l >> 4) & 1) * 32 + (l & 3) * 8;
    LAS char* s0 = lds + L_S1 + sa;
    LAS char* s1 = lds + sa - 12 * 1024;
    u32x2 ra[3], rb_[3];
    ra[0] = trread(s0); rb_[0] = trread(s0 + 256);
    ra[1] = trread(s0 + 1024); rb_[1] = trread(s0 + 1024 + 256);
#pragma unroll
    for (int ks = 0; ks < 36; ks++) {
      if (ks < 9) {
        glds(pb + (size_t)(w + 8 * ks) * 8 * 9216, pvo, L_B1 + (w + 8 * ks) * 1024);
        glds(pb + (size_t)(w + 8 * ks + 4) * 8 * 9216, pvo, L_B1 + (w + 8 * ks + 4) * 1024);
      }
      if (ks == 10) {
        vmwait<18>();
        TS(17);
        lds_arrive(lds + L_CNT + 4, l);
        lds_await(lds + L_CNT + 4, 4);
      }
      if (ks + 2 < 36) {
        LAS char* ap = (ks + 2 < 12 ? s0 : s1) + (ks + 2) * 1024;
        ra[(ks + 2) % 3] = trread(ap); rb_[(ks + 2) % 3] = trread(ap + 256);
      }
      u32x4 A = {ra[ks % 3][0], ra[ks % 3][1], rb_[ks % 3][0], rb_[ks % 3][1]};
      acc1 = mfma32(A, xf[ks], acc1);
      sbar();
    }
    TS(18);
    vmwait<0>();
    TS(19);
  } else if (w == 4) {
    u32x4 srd = mksrd(ws, WS_END), sg, cg[3][3];
    const u32 tg32 = (u32)c.tag;
    const u32 so = OFF_STATS + rt * 1024 + l * 16, co = OFF_CS + j * 2304 + l * 16;
    auto sweep = [&]() {
      ldb16s(sg, srd, so);
#pragma unroll
      for (int p = 0; p < 3; p++) {
        ldb16s(cg[p][0], srd, co + p * F_ * 8); ldb16s(cg[p][1], srd, co + p * F_ * 8 + 1024);
        ldb16s(cg[p][2], srd, l < 16 ? co + p * F_ * 8 + 2048 : OOB);
      }
    };
    sweep();
    xread();
    TS(17);
#pragma unroll 1
    for (int spins = 0; spins < 1000000; spins++) {
      vmwait<0>();
      PIN(sg);
      bool ok = sg[1] == tg32 && sg[3] == tg32;
#pragma unroll
      for (int p = 0; p < 3; p++) {
        PIN(cg[p][0]); PIN(cg[p][1]); PIN(cg[p][2]);
        ok = ok && cg[p][0][1] == tg32 && cg[p][0][3] == tg32 && cg[p][1][1] == tg32 && cg[p][1][3] == tg32;
        ok = ok && (l >= 16 || (cg[p][2][1] == tg32 && cg[p][2][3] == tg32));
      }
      if (__all(ok)) break;
      sweep();
    }
    vmwait<0>();
    TS(18);
    *(LAS u32x2*)(lds + L_ST + l * 8) = (u32x2){sg[0], sg[2]};
#pragma unroll
    for (int i = 0; i < 3; i++) {
      float c0 = __uint_as_float(cg[0][i][0]) + __uint_as_float(cg[1][i][0]) + __uint_as_float(cg[2][i][0]);
      float c1 = __uint_as_float(cg[0][i][2]) + __uint_as_float(cg[1][i][2]) + __uint_as_float(cg[2][i][2]);
      *(LAS f32x2*)(lds + (i < 2 || l < 16 ? L_CS + i * 512 : L_CSS + 1024) + l * 8) = (f32x2){c0, c1};
    }
    {
      const char* kp = (const char*)c.ssk + (size_t)c.cb * 3 * 4096;
      glds4(kp, l * 128, L_CSS);
      glds4(kp, 8192 + (l & 31) * 128, L_CSS + 256);
      const char* wp = (const char*)w2 + (size_t)kq * 1327104 + (b >> 3) * 41472;
#pragma unroll
      for (int i = 0; i < 6; i++) glds4(wp, (i < 5 ? i * 64 + l : 320 + (l & 3)) * 128, L_CSS + 512);
    }
    TS(19);
  } else if (kvw) {
    u32x2 kr[11], vr[8];
    u32x4 sq[2];
    {
      u32x4 rk = mksrd(k, S_ * D_ * 2), rv = mksrd(v, S_ * D_ * 2), rs = mksrd(sk, D_ * 8);
      u32 o = kt * 1152 + kch * 2;
#pragma unroll
      for (int i = 0; i < 11; i++) ldb8(kr[i], rk, o + (i - 3) * 1152);
#pragma unroll
      for (int i = 0; i < 8; i++) ldb8(vr[i], rv, o + i * 1152);
      ldb16(sq[0], rs, kch * 8); ldb16(sq[1], rs, kch * 8 + 16);
    }
    xread();
    __builtin_amdgcn_s_setprio(2);
    vmwait<0>();
#pragma unroll
    for (int i = 0; i < 11; i++) PIN(kr[i]);
#pragma unroll
    for (int i = 0; i < 8; i++) PIN(vr[i]);
    PIN(sq[0]); PIN(sq[1]);
    TS(17);
    kv_compute(kr, vr, sq, c.wr, kch, kt);
    TS(18);
    __builtin_amdgcn_s_setprio(0);
  } else {
    xread();
  }
  lgwait(); bar();
  TS(3);
  {
    u32 st = *(LAS u32*)(lds + L_ST + (tg * 32 + (l & 31)) * 4);
    c.nm = -blo(st);
    c.cc = -1.44269504088896f / bhi(st);
  }
  const u32 abase = (8 * kh + ((l >> 2) & 3)) * 128 + ((fg * 64 + ((l >> 4) & 1) * 32 + (l & 3) * 8) ^ (((l >> 3) & 1) << 6));
  const u32 hbase = OFF_H + (rt * 144 + 9 * j) * 8192 + (2 * tg + ((l >> 4) & 1)) * 1024 + (kh * 16 + (l & 15)) * 16;
  LAS char* aB0 = lds + abase;
  LAS char* aB1 = lds + L_B1 + abase;
  LAS char* csb = lds + L_CS + kh * 16;
  int ptile = odd ? 0 : 8;
  u32 hoff = (w < 4) ? hbase + ptile * 8192 : OOB;
  const int klo = (((c.cb * 3) >> 6) * 64) / 3, khi = (((c.cb * 3 + 2) >> 6) * 64 + 63) / 3;
  const bool kfact = klo + l <= khi;
  const u32 kfoff = OFF_FK + (kfact ? klo + l : klo) * 8;
  u32x2 ra[3], rb_[3];
  f32x4 cs[2];
  ra[0] = trread(aB1); rb_[0] = trread(aB1 + 512);
  ra[1] = trread(aB1 + 2048); rb_[1] = trread(aB1 + 2048 + 512);
  cs[0] = *(LAS f32x4*)(csb + ptile * 128);
#pragma unroll 1
  for (int pp = 0; pp < 2; pp++) {
    int t0 = odd + 4 * pp + fg, t1 = t0 + 2;
    g1_pair<0>(c, xf, acc0, acc1, aB1, aB0, ra, rb_, cs, csb + ptile * 128, csb + t0 * 128, hoff,
               pwp + (2 * pp + 1) * 128, pvo, w * 1024, pp, kfoff, kfact);
    if (pp == 0) { TS(4); } else { TS(6); }
    hoff = hbase + t0 * 8192;
    ptile = t0;
    if (pp == 1) break;
    g1_pair<1>(c, xf, acc1, acc0, aB0, aB1, ra, rb_, cs, csb + t0 * 128, csb + t1 * 128, hoff,
               pwp + 2 * 128, pvo, L_B1 + w * 1024, pp, kfoff, kfact);
    TS(5);
    ptile = t1;
    hoff = hbase + t1 * 8192;
  }
  g1_pair<2>(c, xf, acc1, acc0, aB0, aB1, ra, rb_, cs, csb + ptile * 128, csb, hoff, pwp, pvo, 0, 1, kfoff, kfact);
  TS(7);
  ptile += 2;
  g1_epi_final(c, acc1, L_CS + ptile * 128 + kh * 16, hbase + ptile * 8192);
}

static constexpr u32 L_RED = L_B1;
static constexpr u32 L_EX = 73728;
static constexpr u32 L_OWN = 0;
static constexpr u32 L_Y = 18432;

template <int P>
__device__ __forceinline__ void w2_piece(const char* wsrc, const u32 (&cvo)[5], u32 ldst, int w, int l) {
  if (P < 4) glds(wsrc, cvo[P < 4 ? P : 0], ldst + (w * 4 + P) * 1024);
  else if (l < 32) glds(wsrc, cvo[4], ldst + (32 + (w >> 1)) * 1024 + (w & 1) * 512);
}
template <int P>
__device__ __forceinline__ void w2_piece10(const char* wsrc, const u32 (&cvo)[5], int w, int l) {
  w2_piece<P % 5>(wsrc + (P / 5) * 73728, cvo, (P / 5) * 36864, w, l);
}
template <bool GL>
__device__ __forceinline__ void conv_res(f32x4 (&acc)[8], u32x4 (&A)[8], u32x4 (&B)[8], LAS char* ap, LAS char* bp,
                                         const char* wsrc, const u32 (&cvo)[5], int w, int l) {
#pragma unroll
  for (int q = 0; q < 8; q++) {
#pragma unroll
    for (int i = 7 - q; i >= 0; i--) acc[i + q] = mfma16(A[i], B[q], acc[i + q]);
    B[q] = *(LAS u32x4*)(bp + 512 * (7 - q));
    u32x2 lo = *(LAS u32x2*)(ap + 512 * (7 - q)), hi = *(LAS u32x2*)(ap + 512 * (7 - q) + 8);
    A[7 - q] = (u32x4){lo[0], lo[1], hi[0], hi[1]};
    if (GL) {
      if (q == 0) { w2_piece10<0>(wsrc, cvo, w, l); w2_piece10<1>(wsrc, cvo, w, l); }
      if (q == 1) { w2_piece10<2>(wsrc, cvo, w, l); w2_piece10<3>(wsrc, cvo, w, l); }
      if (q == 2) w2_piece10<4>(wsrc, cvo, w, l);
      if (q == 3) w2_piece10<5>(wsrc, cvo, w, l);
      if (q == 4) w2_piece10<6>(wsrc, cvo, w, l);
      if (q == 5) w2_piece10<7>(wsrc, cvo, w, l);
      if (q == 6) w2_piece10<8>(wsrc, cvo, w, l);
      if (q == 7) w2_piece10<9>(wsrc, cvo, w, l);
    }
    sbar();
  }
}

struct G2Ctx {
  const char* wsrc;
  const char* hsrc;
  char* ws;
  LAS char* apA;
  LAS char* apB;
  u32x4 srd, srx;
  u64 tag;
  u32 cvo[5];
  u32 fyoff, yoff0, yoff1, xoff0, xoff2;
  u32 clr[4];
  __amdgpu_buffer_rsrc_t wr;
  bool fyact;
  int w, l, b;
};
struct G2Tail { u32x2 pf; u32x4 fh[2], yq[2]; u32x2 xv[3]; };

template <int I>
__device__ __forceinline__ void g2_issue1(const char* wsrc, const char* hsrc, const u32 (&cvo)[5], u32 ldst, int w, int l, u32x4 (&hb)[4]) {
  if (I % 2 == 0) w2_piece<I / 2>(wsrc, cvo, ldst, w, l);
  if (I == 1) ldg16s(hb[0], hsrc, l * 16);
  if (I == 3) ldg16so(hb[1], hsrc, l * 16);
  if (I == 5) ldg16s(hb[2], hsrc + 8192, l * 16);
  if (I == 7) ldg16so(hb[3], hsrc + 8192, l * 16);
}
template <int IT, int SS>
__device__ __forceinline__ void g2_aread(const G2Ctx& c, u32x2& ra, u32x2& rb_) {
  LAS char* p = ((IT & 2) ? c.apB : c.apA) + (IT & 1) * 36864 + (SS / 9) * 9216 + (SS % 9) * 32;
  ra = trread(p); rb_ = trread(p + 2304);
}
template <int IT, int SS>
__device__ __forceinline__ void g2_step(const G2Ctx& c, G2Tail& t, u32x4 (&hb)[4][4], f32x4 (&acc)[2][9], u32x2 (&ra)[3], u32x2 (&rb_)[3]) {
  constexpr int u = SS / 9, n = SS % 9;
  const int w = c.w, l = c.l;
  if (SS + 2 < 18) g2_aread<IT, (SS + 2) % 18>(c, ra[(SS + 2) % 3], rb_[(SS + 2) % 3]);
  else if (SS == 17 && IT < 8) g2_aread<IT + 1, 1>(c, ra[1], rb_[1]);
  if (SS == 16) lgwait();
  u32x4 A = {ra[SS % 3][0], ra[SS % 3][1], rb_[SS % 3][0], rb_[SS % 3][1]};
  acc[0][n] = mfma16(A, hb[IT & 3][u * 2], acc[0][n]);
  acc[1][n] = mfma16(A, hb[IT & 3][u * 2 + 1], acc[1][n]);
  if (SS == 16 && IT < 8) {
    if (IT == 0) vmwait<4 + 3 + 5 + 8 + 2>();
    else if (IT + 3 < 9) vmwait<9 + 8 + (IT == 1 ? 3 : IT == 2 ? 2 : IT == 3 ? 1 : 0)>();
    else if (IT + 2 < 9) vmwait<9>();
    else vmwait<0>();
    bar();
    PIN(hb[(IT + 1) & 3][0]); PIN(hb[(IT + 1) & 3][1]); PIN(hb[(IT + 1) & 3][2]); PIN(hb[(IT + 1) & 3][3]);
    if (IT == 1 && c.b < 192 && w == 0 && l == 0) flag_set(c.ws, OFF_FY + c.b * 8, c.tag);
    if (IT == 4 && w == 0 && l == 0) flag_set(c.ws, OFF_FX + c.b * 8, c.tag);
    g2_aread<IT + 1, 0>(c, ra[0], rb_[0]);
  }
  if ((IT == 0 && (SS == 4 || SS == 12)) || ((IT == 1 || IT == 2) && SS == 8))
    __builtin_amdgcn_raw_buffer_store_b128((u32x4){EMPTY, EMPTY, EMPTY, EMPTY}, c.wr, c.clr[IT == 0 ? SS / 12 : IT + 1], 0, SC);
  if (IT + 3 < 9) {
    const char* ws_ = c.wsrc + (IT + 3) * 73728;
    const char* hs_ = c.hsrc + (IT + 3) * 16384;
    if (SS % 2 == 1 && SS < 16) g2_issue1<(SS % 2 == 1 ? SS / 2 : 0)>(ws_, hs_, c.cvo, ((IT + 3) & 3) * 36864, w, l, hb[(IT + 3) & 3]);
    if (SS == 17) g2_issue1<8>(ws_, hs_, c.cvo, ((IT + 3) & 3) * 36864, w, l, hb[(IT + 3) & 3]);
  }
  if (IT == 7 && SS == 17) {
    ldg8s(t.pf, c.ws, c.fyoff);
    u32 o = (c.b == 0 && w == 7) ? OFF_FH + l * 32 : OOB;
    ldb16s(t.fh[0], c.srd, o); ldb16s(t.fh[1], c.srd, o + 16);
  }
  if (IT == 8 && SS == 12) {
    vmwait<0>();
    PIN(t.pf); PIN(t.fh[0]); PIN(t.fh[1]);
    if (!flag_ok(t.pf, c.fyact, c.tag)) flag_wait(c.ws, c.fyoff, c.fyact, c.tag);
    if (c.b == 0 && w == 7) {
      const u32 lo = (u32)c.tag, hi = (u32)(c.tag >> 32);
      bool ok = t.fh[0][0] == lo && t.fh[0][1] == hi && t.fh[0][2] == lo && t.fh[0][3] == hi &&
                t.fh[1][0] == lo && t.fh[1][1] == hi && t.fh[1][2] == lo && t.fh[1][3] == hi;
      if (!__all(ok))
        for (int i = 0; i < 4; i++) flag_wait(c.ws, OFF_FH + (i * 64 + l) * 8, true, c.tag);
      if (l == 0) flag_set(c.ws, OFF_E, c.tag);
    }
    ldb16s(t.yq[0], c.srd, c.yoff0); ldb16s(t.yq[1], c.srd, c.yoff1);
    ldb8(t.xv[0], c.srx, c.xoff0); ldb8(t.xv[1], c.srx, c.xoff0 + 32); ldb8(t.xv[2], c.srx, c.xoff2);
  }
  sbar();
}
template <int IT, int SS>
__device__ __forceinline__ void g2_steps(const G2Ctx& c, G2Tail& t, u32x4 (&hb)[4][4], f32x4 (&acc)[2][9], u32x2 (&ra)[3], u32x2 (&rb_)[3]) {
  g2_step<IT, SS>(c, t, hb, acc, ra, rb_);
  if constexpr (SS < 17) g2_steps<IT, SS + 1>(c, t, hb, acc, ra, rb_);
  else if constexpr (IT < 8) g2_steps<IT + 1, 0>(c, t, hb, acc, ra, rb_);
}
__device__ __forceinline__ void fin4(f32x4 s, u32x2 xv, u32x2 yv, float m, float rs, u16* op) {
  float res[4];
#pragma unroll
  for (int e = 0; e < 4; e++) {
    float xe = (e & 1) ? bhi(xv[e >> 1]) : blo(xv[e >> 1]);
    float ye = (e & 1) ? bhi(yv[e >> 1]) : blo(yv[e >> 1]);
    float o = rb(s[e] * rs);
    float g = rb(o * __builtin_amdgcn_rcpf(1.0f + __builtin_amdgcn_exp2f(o * -1.44269504088896f)));
    float xn = rb(rb(xe - m) * rs);
    float sv = rb(xn * ye);
    res[e] = xe + rb(sv * g);
  }
  u32x2 r = {pk(res[0], res[1]), pk(res[2], res[3])};
  asm volatile("global_store_dwordx2 %0, %1, off sc0 sc1" ::"v"(op), "v"(r) : "memory");
}
__device__ __forceinline__ u32 umax(u32 a, u32 b) { return a > b ? a : b; }
__device__ __forceinline__ u32 pkw(float a, float b) { u32 v = pk(a, b); return v < EMPTY - 1 ? v : EMPTY - 1; }
#define PDELAY 8
__device__ __forceinline__ f32x4 up4(u32 a, u32 b) { return (f32x4){blo(a), bhi(a), blo(b), bhi(b)}; }

__device__ __forceinline__ void conv_g2(G1Ctx& g, const u16* x, const u16* Dv, const u16* w2, u16* out, int rt, int kq, int dn TSARG) {
  LAS char* lds = g.lds;
  char* ws = g.ws;
  const int w = g.w, l = g.l, b = g.b, cb = g.cb;
  const int tg = w & 3, kh = w >> 2, xbit = rt >> 3, i7 = rt & 7, lg = l >> 4;
  G2Ctx c;
  c.ws = ws; c.tag = g.tag; c.w = w; c.l = l; c.b = b;
#pragma unroll
  for (int i = 0; i < 5; i++) {
    u32 idx = i < 4 ? (w * 4 + i) * 64 + l : (32 + (w >> 1)) * 64 + (w & 1) * 32 + (l & 31);
    u32 row = idx / 18, c16 = idx - row * 18;
    c.cvo[i] = ((row >> 6) * 576 + (row & 63)) * 1152 + c16 * 16;
  }
  c.wsrc = (const char*)w2 + (size_t)(1152 * kq) * 1152 + dn * 288;
  c.hsrc = ws + OFF_H + (size_t)((rt * 144 + 36 * kq + 18 * kh) * 8 + 2 * tg) * 1024;
  f32x4 ca[8];
#pragma unroll
  for (int i = 0; i < 8; i++) ca[i] = (f32x4){0, 0, 0, 0};
  const int ch = (w < 3) ? 0 : (w < 6) ? 1 : 2;
  const int r0 = (w == 0 || w == 4 || w == 6) ? 0 : (w == 1 || w == 5) ? 3 : (w == 7) ? 4 : 6;
  const int nr = (w >= 6) ? 4 : (w == 2 || w == 3) ? 2 : 3;
  const int lb = l & 15, cp = (lb + 1) & 3;
  LAS char* ap = lds + L_KC + (ch * 4 + cp) * 4160 + 2 * (lb + 1 - cp) + 16 * lg + 64 * r0;
  LAS char* bp = lds + L_U + ch * 4608 + 2 * (240 - 16 * lb + 8 * lg) + 64 * r0;
  u32x4 A[8], B[8];
  if (b < 192) {
#pragma unroll
    for (int i = 7; i >= 0; i--) {
      B[7 - i] = *(LAS u32x4*)(bp + 512 * i);
      u32x2 lo = *(LAS u32x2*)(ap + 512 * i), hi = *(LAS u32x2*)(ap + 512 * i + 8);
      A[i] = (u32x4){lo[0], lo[1], hi[0], hi[1]};
    }
    conv_res<true>(ca, A, B, ap + 64, bp + 64, c.wsrc, c.cvo, w, l);
  } else {
    w2_piece10<0>(c.wsrc, c.cvo, w, l); w2_piece10<1>(c.wsrc, c.cvo, w, l); w2_piece10<2>(c.wsrc, c.cvo, w, l);
    w2_piece10<3>(c.wsrc, c.cvo, w, l); w2_piece10<4>(c.wsrc, c.cvo, w, l); w2_piece10<5>(c.wsrc, c.cvo, w, l);
    w2_piece10<6>(c.wsrc, c.cvo, w, l); w2_piece10<7>(c.wsrc, c.cvo, w, l); w2_piece10<8>(c.wsrc, c.cvo, w, l);
    w2_piece10<9>(c.wsrc, c.cvo, w, l);
  }
  vmwait<10>();
  if (l == 0) {
    u32 old = __hip_atomic_fetch_add((LAS u32*)(lds + L_CNT), 1u, __ATOMIC_RELAXED, __HIP_MEMORY_SCOPE_WORKGROUP);
    if (old == 7) flag_set(ws, OFF_FH + b * 8, g.tag);
  }
  TS(8);
  if (b < 192) {
#pragma unroll 1
    for (int r = 1; r < nr; r++) conv_res<false>(ca, A, B, ap + 64 * (r + 1), bp + 64 * (r + 1), c.wsrc, c.cvo, w, l);
  }
  TS(9);
  u32x2 uu[3];
#pragma unroll
  for (int i = 0; i < 3; i++) {
    int u = w * 3 + i;
    uu[i] = *(LAS u32x2*)(lds + L_U + (u >> 3) * 4608 + 2 * (2044 - (256 * (u & 7) + 16 * lb + 4 * lg)));
  }
  const u32 fhoff = OFF_FH + ((b & 7) + 8 * ((l & 3) * 8 + i7)) * 8;
  u32x2 pf;
  ldg8s(pf, ws, fhoff);
  lgwait(); bar();
#pragma unroll
  for (int i = 0; i < 8; i++) *(LAS f32x4*)(lds + L_RED + w * 8192 + i * 1024 + l * 16) = ca[i];
  lgwait(); bar();
  TS(10);
  f32x4 ys[3];
#pragma unroll
  for (int i = 0; i < 3; i++) {
    int u = w * 3 + i, c2 = u >> 3;
    LAS char* rp = lds + L_RED + c2 * 3 * 8192 + (u & 7) * 1024 + l * 16;
    ys[i] = *(LAS f32x4*)(rp) + *(LAS f32x4*)(rp + 8192);
    if (c2 < 2) ys[i] += *(LAS f32x4*)(rp + 2 * 8192);
  }
  vmwait<0>();
  PIN(pf);
  if (!flag_ok(pf, true, g.tag)) flag_wait(ws, fhoff, true, g.tag);
  TS(11);
  u32x4 hb[4][4];
#pragma unroll
  for (int it = 0; it < 3; it++) {
    ldg16s(hb[it][0], c.hsrc + it * 16384, l * 16); ldg16so(hb[it][1], c.hsrc + it * 16384, l * 16);
    ldg16s(hb[it][2], c.hsrc + it * 16384 + 8192, l * 16); ldg16so(hb[it][3], c.hsrc + it * 16384 + 8192, l * 16);
  }
#pragma unroll
  for (int i = 0; i < 3; i++) {
    int u = w * 3 + i, chn = cb * 3 + (u >> 3);
    u32 dw = ((CAS const u32*)Dv)[chn >> 1];
    float dv = (chn & 1) ? bhi(dw) : blo(dw);
    float o0 = rb(ys[i][0]) + rb(bhi(uu[i][1]) * dv), o1 = rb(ys[i][1]) + rb(blo(uu[i][1]) * dv);
    float o2 = rb(ys[i][2]) + rb(bhi(uu[i][0]) * dv), o3 = rb(ys[i][3]) + rb(blo(uu[i][0]) * dv);
    u32 o = b < 192 ? OFF_Y2 + chn * 4096 + (256 * (u & 7) + 16 * lb + 4 * lg) * 2 : OOB;
    __builtin_amdgcn_raw_buffer_store_b64((u32x2){pk(o0, o1), pk(o2, o3)}, g.wr, o, 0, SC);
  }
  lgwait(); bar();
  w2_piece<0>(c.wsrc + 2 * 73728, c.cvo, 73728, w, l); w2_piece<1>(c.wsrc + 2 * 73728, c.cvo, 73728, w, l);
  w2_piece<2>(c.wsrc + 2 * 73728, c.cvo, 73728, w, l); w2_piece<3>(c.wsrc + 2 * 73728, c.cvo, 73728, w, l);
  w2_piece<4>(c.wsrc + 2 * 73728, c.cvo, 73728, w, l);
  c.apA = lds + (kh * 64 + 16 * (lg >> 1) + 4 * (lg & 1) + ((l >> 2) & 3)) * 288 + (l & 3) * 8;
  c.apB = c.apA + 73728;
  c.srd = mksrd(ws, WS_END);
  c.srx = mksrd(x, S_ * D_ * 2);
  const int p = w & 3, tt = w >> 2;
  const int tok = rt * 128 + kq * 32 + tt * 16 + lb, d0 = dn * 144 + 4 * lg;
  c.fyact = l < 51;
  c.fyoff = l < 48 ? OFF_FY + (48 * dn + l) * 8 : OFF_FX + (2 * ((kq + 1 + (l < 51 ? l - 48 : 0)) & 3) + xbit + 8 * (dn * 8 + i7)) * 8;
  c.wr = g.wr;
#pragma unroll
  for (int i = 0; i < 4; i++) {
    int n = w * 4 + i, s = n / 9;
    int ob = 2 * ((kq + 1 + s) & 3) + xbit + 8 * (dn * 8 + i7);
    c.clr[i] = n < 27 ? OFF_P + (ob * 3 + 2 - s) * 9216 + (n - 9 * s) * 1024 + l * 16 : OOB;
  }
  {
    u32 yb = OFF_Y2 + (dn * 144 + w * 16 + (l >> 2)) * 4096 + (rt * 128 + kq * 32) * 2 + (l & 3) * 16;
    c.yoff0 = yb;
    c.yoff1 = w == 0 ? yb + 128 * 4096 : OOB;
    c.xoff0 = tok * 1152 + (d0 + p * 32) * 2;
    c.xoff2 = p == 0 ? tok * 1152 + (d0 + 128) * 2 : OOB;
  }
  f32x4 acc[2][9];
#pragma unroll
  for (int n = 0; n < 9; n++) { acc[0][n] = (f32x4){0, 0, 0, 0}; acc[1][n] = (f32x4){0, 0, 0, 0}; }
  G2Tail t;
  u32x2 ra[3], rb_[3];
  g2_aread<0, 0>(c, ra[0], rb_[0]); g2_aread<0, 1>(c, ra[1], rb_[1]);
  vmwait<8 + 3 + 5>();
  PIN(hb[0][0]); PIN(hb[0][1]); PIN(hb[0][2]); PIN(hb[0][3]);
  TS(12);
  g2_steps<0, 0>(c, t, hb, acc, ra, rb_);
  TS(13);
#pragma unroll
  for (int n = 0; n < 9; n++) *(LAS f32x4*)(lds + L_EX + w * 9216 + n * 1024 + l * 16) = kh ? acc[0][n] : acc[1][n];
  lgwait(); bar();
  f32x4 r[9];
#pragma unroll
  for (int n = 0; n < 9; n++) r[n] = (kh ? acc[1][n] : acc[0][n]) + *(LAS f32x4*)(lds + L_EX + (w ^ 4) * 9216 + n * 1024 + l * 16);
  {
    int ob = 2 * tg + xbit + 8 * (dn * 8 + i7);
    u32 po = tg != kq ? OFF_P + (ob * 3 + ((kq - tg - 1) & 3)) * 9216 + kh * 4608 : OOB;
#pragma unroll
    for (int q = 0; q < 4; q++)
      __builtin_amdgcn_raw_buffer_store_b128((u32x4){pkw(r[2 * q][0], r[2 * q][1]), pkw(r[2 * q][2], r[2 * q][3]),
                                                     pkw(r[2 * q + 1][0], r[2 * q + 1][1]), pkw(r[2 * q + 1][2], r[2 * q + 1][3])},
                                             g.wr, po + q * 1024 + l * 16, 0, SC);
    __builtin_amdgcn_raw_buffer_store_b64((u32x2){pkw(r[8][0], r[8][1]), pkw(r[8][2], r[8][3])}, g.wr, po + 4096 + l * 8, 0, SC);
    if (tg == kq) {
#pragma unroll
      for (int n = 0; n < 9; n++) *(LAS f32x4*)(lds + L_OWN + kh * 9216 + n * 1024 + l * 16) = r[n];
    }
  }
  vmwait<5>();
  PIN(t.yq[0]); PIN(t.yq[1]); PIN(t.xv[0]); PIN(t.xv[1]); PIN(t.xv[2]);
  *(LAS u32x4*)(lds + L_Y + (w * 16 + (l >> 2)) * 96 + (l & 3) * 16) = t.yq[0];
  if (w == 0) *(LAS u32x4*)(lds + L_Y + (128 + (l >> 2)) * 96 + (l & 3) * 16) = t.yq[1];
  lgwait(); bar();
  TS(14);
  u32 st = *(LAS u32*)(lds + L_ST + (kq * 32 + tt * 16 + lb) * 4);
  float m = blo(st), rs = 1.0f / bhi(st);
  LAS char* yp = lds + L_Y + (4 * lg + ((l >> 2) & 3)) * 96 + tt * 32 + (l & 3) * 8;
  LAS char* op = lds + L_OWN + tt * 9216 + l * 16;
  u32x2 y0 = trread(yp + p * 32 * 96), y1 = trread(yp + (p * 32 + 16) * 96), y2 = trread(yp + 128 * 96);
  f32x4 s0 = *(LAS f32x4*)(op + 2 * p * 1024), s1 = *(LAS f32x4*)(op + (2 * p + 1) * 1024), s2 = *(LAS f32x4*)(op + 8 * 1024);
  u32x4 pq[3];
  u32x2 p8[3];
  {
    const u32 po = OFF_P + b * 3 * 9216 + tt * 4608;
    const u32 o16 = po + p * 1024 + l * 16, o8 = p == 0 ? po + 4096 + l * 8 : OOB;
    auto sweep = [&]() {
#pragma unroll
      for (int s = 0; s < 3; s++) { ldb16s(pq[s], c.srd, o16 + s * 9216); ldb8s(p8[s], c.srd, o8 + s * 9216); }
    };
    __builtin_amdgcn_s_sleep(PDELAY);
    sweep();
#pragma unroll 1
    for (int spins = 0; spins < 1000000; spins++) {
      vmwait<0>();
      u32 mx = 0;
#pragma unroll
      for (int s = 0; s < 3; s++) {
        PIN(pq[s]); PIN(p8[s]);
        mx = umax(mx, umax(umax(pq[s][0], pq[s][1]), umax(pq[s][2], pq[s][3])));
        mx = umax(mx, umax(p8[s][0], p8[s][1]));
      }
      if (__all(mx != EMPTY)) break;
      sweep();
    }
    vmwait<0>();
#pragma unroll
    for (int s = 0; s < 3; s++) { PIN(pq[s]); PIN(p8[s]); }
  }
  TS(15);
#pragma unroll
  for (int s = 0; s < 3; s++) { s0 += up4(pq[s][0], pq[s][1]); s1 += up4(pq[s][2], pq[s][3]); }
  u16* o = out + tok * D_ + d0;
  fin4(s0, t.xv[0], y0, m, rs, o + p * 32);
  fin4(s1, t.xv[1], y1, m, rs, o + p * 32 + 16);
  if (p == 0) {
#pragma unroll
    for (int s = 0; s < 3; s++) s2 += up4(p8[s][0], p8[s][1]);
    fin4(s2, t.xv[2], y2, m, rs, o + 128);
  }
}

__global__ void __launch_bounds__(512, 2) k_fused(const u16* x, const u16* k, const u16* v, const u16* sk, const u16* ssk, const u16* Dv,
                                                  const u16* w1, const u16* w2, u16* out, char* ws) {
  extern __shared__ __attribute__((aligned(1024))) char smem[];
  const int tid = threadIdx.x, b = blockIdx.x;
  const int xc = b & 7, i = b >> 3;
  const int rt = (xc & 1) * 8 + (i & 7), kq = xc >> 1, dn = i >> 3;
  G1Ctx c;
  c.lds = (LAS char*)smem;
  c.ws = ws;
  c.ssk = ssk;
  c.wr = mkrsrc(ws, WS_END);
  c.b = b; c.cb = b < 192 ? b : 191;
  c.w = __builtin_amdgcn_readfirstlane(tid >> 6); c.l = tid & 63;
  g1_phase(c, x, k, v, sk, w1, w2, rt, 4 * kq + dn, kq TSPASS);
  conv_g2(c, x, Dv, w2, out, rt, kq, dn TSPASS);
}

extern "C" void kernel_launch(void* const* d_in, const int* in_sizes, int n_in, void* d_out, int out_size, void* d_ws, size_t ws_size,
                              hipStream_t stream) {
  (void)in_sizes; (void)n_in; (void)out_size; (void)ws_size;
  (void)hipFuncSetAttribute((const void*)k_fused, hipFuncAttributeMaxDynamicSharedMemorySize, LDS_BYTES);
  k_fused<<<256, 512, LDS_BYTES, stream>>>((const u16*)d_in[0], (const u16*)d_in[1], (const u16*)d_in[2], (const u16*)d_in[3],
                                           (const u16*)d_in[4], (const u16*)d_in[5], (const u16*)d_in[7], (const u16*)d_in[8],
                                           (u16*)d_out, (char*)d_ws);
}
```

```cpp
#include <hip/hip_runtime.h>
#include <stdint.h>

typedef unsigned short u16;
typedef unsigned int u32;
typedef unsigned long long u64;
#define LAS __attribute__((address_space(3)))
#define CAS __attribute__((address_space(4)))
typedef __attribute__((ext_vector_type(8))) __bf16 bf16x8;
typedef __attribute__((ext_vector_type(2))) __bf16 bf16x2;
typedef __attribute__((ext_vector_type(2))) float f32x2;
typedef __attribute__((ext_vector_type(4))) float f32x4;
typedef __attribute__((ext_vector_type(16))) float f32x16;
typedef __attribute__((ext_vector_type(2))) u32 u32x2;
typedef __attribute__((ext_vector_type(4))) u32 u32x4;
typedef __attribute__((ext_vector_type(4))) short v4i16;

static constexpr int S_ = 2048, D_ = 576, F_ = 4608;
static constexpr u32 OFF_E = 0;
static constexpr u32 OFF_FK = 4096;
static constexpr u32 OFF_FH = OFF_FK + 2048;
static constexpr u32 OFF_FY = OFF_FH + 2048;
static constexpr u32 OFF_FX = OFF_FY + 2048;
static constexpr u32 OFF_STATS = 65536;
static constexpr u32 OFF_CS = OFF_STATS + 16384;
static constexpr u32 OFF_UR = 262144;
static constexpr u32 OFF_Y2 = OFF_UR + 576 * 4096;
static constexpr u32 OFF_H = OFF_Y2 + 576 * 4096;
static constexpr u32 OFF_P = OFF_H + 2048 * 4608 * 2;
static constexpr u32 WS_END = OFF_P + 256 * 3 * 9216;
static constexpr int LDS_BYTES = 163840;
static constexpr int SC = 16;
static constexpr u32 OOB = 0xF0000000u;
static constexpr u32 EMPTY = 0xFFFFFFFFu;
static constexpr u32 L_B1 = 73728, L_S1 = 147456, L_CSS = 159744, L_CS = 161792, L_ST = 162944, L_CNT = 163456;

#define TS(i)
#define TSARG
#define TSPASS

__device__ __forceinline__ float blo(u32 v) { return __uint_as_float(v << 16); }
__device__ __forceinline__ float bhi(u32 v) { return __uint_as_float(v & 0xffff0000u); }
__device__ __forceinline__ u32 pk(float lo, float hi) {
  f32x2 t = {lo, hi};
  return __builtin_bit_cast(u32, __builtin_convertvector(t, bf16x2));
}
__device__ __forceinline__ float rb(float x) { return blo(pk(x, x)); }
template <int C> __device__ __forceinline__ float dppf(float v) {
  return __int_as_float(__builtin_amdgcn_update_dpp(0, __float_as_int(v), C, 0xf, 0xf, false));
}
__device__ __forceinline__ float swp32add(float a, float b) {
  auto r = __builtin_amdgcn_permlane32_swap(__float_as_uint(a), __float_as_uint(b), false, false);
  return __uint_as_float(r[0]) + __uint_as_float(r[1]);
}
__device__ __forceinline__ float swp16add(float a, float b) {
  auto r = __builtin_amdgcn_permlane16_swap(__float_as_uint(a), __float_as_uint(b), false, false);
  return __uint_as_float(r[0]) + __uint_as_float(r[1]);
}
__device__ __forceinline__ float wave_sum(float v) {
  v += dppf<0x128>(v); v += dppf<0x124>(v); v += dppf<0x122>(v); v += dppf<0x121>(v);
  v = swp16add(v, v);
  return swp32add(v, v);
}
__device__ __forceinline__ void glds(const void* sbase, u32 voff, u32 ldsdst) {
  asm volatile("s_mov_b32 m0, %2\n\ts_nop 0\n\tglobal_load_lds_dwordx4 %0, %1" ::"v"(voff), "s"(sbase), "s"(ldsdst) : "memory");
}
__device__ __forceinline__ void blds(u32x4 srd, u32 voff, u32 ldsdst) {
  asm volatile("s_mov_b32 m0, %2\n\ts_nop 0\n\tbuffer_load_dwordx4 %0, %1, 0 offen lds" ::"v"(voff), "s"(srd), "s"(ldsdst) : "memory");
}
__device__ __forceinline__ void glds4(const void* sbase, u32 voff, u32 ldsdst) {
  asm volatile("s_mov_b32 m0, %2\n\ts_nop 0\n\tglobal_load_lds_dword %0, %1" ::"v"(voff), "s"(sbase), "s"(ldsdst) : "memory");
}
__device__ __forceinline__ void ldg16(u32x4& d, const void* sbase, u32 voff) {
  asm volatile("global_load_dwordx4 %0, %1, %2" : "=v"(d) : "v"(voff), "s"(sbase) : "memory");
}
__device__ __forceinline__ void ldg16s(u32x4& d, const void* sbase, u32 voff) {
  asm volatile("global_load_dwordx4 %0, %1, %2 sc1" : "=v"(d) : "v"(voff), "s"(sbase) : "memory");
}
__device__ __forceinline__ void ldg16so(u32x4& d, const void* sbase, u32 voff) {
  asm volatile("global_load_dwordx4 %0, %1, %2 offset:1024 sc1" : "=v"(d) : "v"(voff), "s"(sbase) : "memory");
}
__device__ __forceinline__ void ldg8(u32x2& d, const void* sbase, u32 voff) {
  asm volatile("global_load_dwordx2 %0, %1, %2" : "=v"(d) : "v"(voff), "s"(sbase) : "memory");
}
__device__ __forceinline__ void ldg8s(u32x2& d, const void* sbase, u32 voff) {
  asm volatile("global_load_dwordx2 %0, %1, %2 sc1" : "=v"(d) : "v"(voff), "s"(sbase) : "memory");
}
__device__ __forceinline__ void ldb8(u32x2& d, u32x4 srd, u32 voff) {
  asm volatile("buffer_load_dwordx2 %0, %1, %2, 0 offen" : "=v"(d) : "v"(voff), "s"(srd) : "memory");
}
__device__ __forceinline__ void ldb4s(u32& d, u32x4 srd, u32 voff) {
  asm volatile("buffer_load_dword %0, %1, %2, 0 offen sc1" : "=v"(d) : "v"(voff), "s"(srd) : "memory");
}
__device__ __forceinline__ void ldb2(u32& d, u32x4 srd, u32 voff) {
  asm volatile("buffer_load_ushort %0, %1, %2, 0 offen" : "=v"(d) : "v"(voff), "s"(srd) : "memory");
}
__device__ __forceinline__ void ldb16s(u32x4& d, u32x4 srd, u32 voff) {
  asm volatile("buffer_load_dwordx4 %0, %1, %2, 0 offen sc1" : "=v"(d) : "v"(voff), "s"(srd) : "memory");
}
__device__ __forceinline__ void ldb16(u32x4& d, u32x4 srd, u32 voff) {
  asm volatile("buffer_load_dwordx4 %0, %1, %2, 0 offen" : "=v"(d) : "v"(voff), "s"(srd) : "memory");
}
template <int N> __device__ __forceinline__ void vmwait() { asm volatile("s_waitcnt vmcnt(%0)" ::"n"(N) : "memory"); }
__device__ __forceinline__ void lgwait() { asm volatile("s_waitcnt lgkmcnt(0)" ::: "memory"); }
__device__ __forceinline__ void bar() { asm volatile("s_barrier" ::: "memory"); }
#define PIN(x) asm volatile("" : "+v"(x))
__device__ __forceinline__ void sbar() { __builtin_amdgcn_sched_barrier(0); }
__device__ __forceinline__ __amdgpu_buffer_rsrc_t mkrsrc(const void* p, u32 bytes) {
  return __builtin_amdgcn_make_buffer_rsrc((void*)p, 0, bytes, 0x00020000);
}
__device__ __forceinline__ u32x4 mksrd(const void* p, u32 bytes) {
  u64 b = (u64)p;
  return (u32x4){(u32)b, (u32)(b >> 32) & 0xffffu, bytes, 0x00020000u};
}
__device__ __forceinline__ f32x4 mfma16(u32x4 a, u32x4 b, f32x4 c) {
  return __builtin_amdgcn_mfma_f32_16x16x32_bf16(__builtin_bit_cast(bf16x8, a), __builtin_bit_cast(bf16x8, b), c, 0, 0, 0);
}
__device__ __forceinline__ f32x16 mfma32(u32x4 a, u32x4 b, f32x16 c) {
  return __builtin_amdgcn_mfma_f32_32x32x16_bf16(__builtin_bit_cast(bf16x8, a), __builtin_bit_cast(bf16x8, b), c, 0, 0, 0);
}
__device__ __forceinline__ u32x2 trread(LAS char* p) {
  return __builtin_bit_cast(u32x2, __builtin_amdgcn_ds_read_tr16_b64_v4i16((LAS v4i16*)p));
}
__device__ __forceinline__ void flag_set(char* ws, u32 off, u64 tag) {
  __hip_atomic_store((__attribute__((address_space(1))) u64*)(ws + off), tag, __ATOMIC_RELAXED, __HIP_MEMORY_SCOPE_AGENT);
}
__device__ __forceinline__ void flag_wait(char* ws, u32 off, bool act, u64 tag) {
#pragma unroll 1
  for (int spins = 0; spins < 4000000; spins++) {
    u64 f = __hip_atomic_load((__attribute__((address_space(1))) u64*)(ws + off), __ATOMIC_RELAXED, __HIP_MEMORY_SCOPE_AGENT);
    if (__all(f == tag || !act)) break;
    __builtin_amdgcn_s_sleep(1);
  }
  asm volatile("" ::: "memory");
}
__device__ __forceinline__ bool flag_ok(u32x2 f, bool act, u64 tag) {
  return __all(!act || (f[0] == (u32)tag && f[1] == (u32)(tag >> 32)));
}

__device__ __forceinline__ void lds_arrive(LAS char* p, int l) {
  if (l == 0) __hip_atomic_fetch_add((LAS u32*)p, 1u, __ATOMIC_RELAXED, __HIP_MEMORY_SCOPE_WORKGROUP);
}
__device__ __forceinline__ void lds_await(LAS char* p, u32 n) {
#pragma unroll 1
  for (int spins = 0; spins < 40000000; spins++) {
    u32 v = __builtin_amdgcn_readfirstlane(__hip_atomic_load((LAS u32*)p, __ATOMIC_RELAXED, __HIP_MEMORY_SCOPE_WORKGROUP));
    if (v >= n) break;
  }
  asm volatile("" ::: "memory");
}

static constexpr u32 L_KC = L_B1;
static constexpr u32 L_U = L_B1 + 12 * 4160;

struct G1Ctx {
  LAS char* lds;
  char* ws;
  const u16* ssk;
  __amdgpu_buffer_rsrc_t wr;
  float nm, cc;
  u64 tag, pc;
  u32 clen;
  int b, cb, w, l;
};

template <int MODE>
__device__ __forceinline__ void g1_pair(const G1Ctx& c, const u32x4 (&xf)[36], f32x16& acc, const f32x16& pa, LAS char* a0, LAS char* n0,
                                        u32x2 (&ra)[3], u32x2 (&rb_)[3], f32x4 (&cs)[2], LAS char* csp, LAS char* csn, u32 hoff,
                                        const char* dsrc, u32 dvoff, u32 ddst, int pp, u32 kfoff, bool kfact) {
  LAS char* a1 = a0 + 18 * 2048;
  const int w = c.w, l = c.l;
  float X[16], E[16];
  u32 P[8];
  u32x2 pf, k0;
  u32x4 uq[2];
#pragma unroll
  for (int ks = 0; ks < 36; ks++) {
    if (ks + 2 < 36) {
      LAS char* ap = (ks + 2 < 18 ? a0 + (ks + 2) * 2048 : a1 + (ks + 2 - 18) * 2048);
      ra[(ks + 2) % 3] = trread(ap); rb_[(ks + 2) % 3] = trread(ap + 512);
    }
    if (MODE != 2 && ks == 35) { ra[1] = trread(n0 + 2048); rb_[1] = trread(n0 + 2048 + 512); }
    if (ks == 34) lgwait();
    u32x4 A = {ra[ks % 3][0], ra[ks % 3][1], rb_[ks % 3][0], rb_[ks % 3][1]};
    if (ks == 0) { f32x16 z = {}; acc = mfma32(A, xf[0], z); }
    else acc = mfma32(A, xf[ks], acc);
    if (ks == 34) {
      vmwait<1>();
      if (MODE == 0) {
        PIN(pf);
        if (pp == 1 && !flag_ok(pf, kfact, c.tag)) flag_wait(c.ws, kfoff, kfact, c.tag);
      }
      bar();
      if (MODE == 0 && pp == 0 && w == 0 && l == 0) flag_set(c.ws, OFF_FK + c.b * 8, c.tag);
      if (MODE != 2) { ra[0] = trread(n0); rb_[0] = trread(n0 + 512); }
    }
    if (ks % 8 == 6 && ks / 8 < 3) cs[(ks / 8 + 1) & 1] = *(LAS f32x4*)(csp + (ks / 8 + 1) * 32);
    if (ks % 2 == 1 && ks / 2 < 16) {
      int e = ks / 2;
      float xx = __builtin_fmaf(c.nm, cs[(e >> 2) & 1][e & 3], pa[e]);
      PIN(xx);
      float tt = xx * c.cc;
      PIN(tt);
      X[e] = xx; E[e] = tt;
    }
    if (ks % 2 == 0 && ks >= 2 && ks / 2 - 1 < 16) {
      int e = ks / 2 - 1;
      float tt = __builtin_amdgcn_exp2f(E[e]);
      PIN(tt);
      E[e] = tt;
    }
    if (ks % 2 == 1 && ks >= 3 && (ks - 3) / 2 < 16) {
      int e = (ks - 3) / 2;
      float tt = 1.0f + E[e];
      PIN(tt);
      tt = __builtin_amdgcn_rcpf(tt);
      PIN(tt);
      E[e] = tt;
    }
    if (ks % 2 == 0 && ks >= 4 && (ks - 4) / 2 < 16) {
      int e = (ks - 4) / 2;
      float hh = X[e] * E[e];
      PIN(hh);
      X[e] = hh;
      if (e & 1) { u32 pq = pk(X[e - 1], X[e]); PIN(pq); P[e >> 1] = pq; }
    }
    if (ks == 19) __builtin_amdgcn_raw_buffer_store_b128((u32x4){P[0], P[1], P[2], P[3]}, c.wr, hoff, 0, SC);
    if (ks == 35) {
      __builtin_amdgcn_raw_buffer_store_b128((u32x4){P[4], P[5], P[6], P[7]}, c.wr, hoff + 512, 0, SC);
      if (MODE != 2) cs[0] = *(LAS f32x4*)(csn);
    }
    if (MODE != 2) {
      if (ks % 2 == 0 && ks / 2 < 9) glds(dsrc + (size_t)(ks / 2) * (64 * 9216), dvoff, ddst + (ks / 2) * 8192);
      if (MODE == 0 && ks == 1) ldg8s(pf, c.ws, kfoff);
    } else {
      if (ks % 2 == 0 && ks / 2 < 6) {
        int idx = w * 6 + ks / 2, cc = idx >> 2, part = idx & 3;
        u32x4 srd = mksrd(c.ssk + (size_t)(c.cb * 3 + (cc >> 2)) * 2048, 4096u);
        blds(srd, (cc & 3) * 2 + part * 1024 + l * 16, L_KC + cc * 4160 + 32 + part * 1024);
      }
      if (ks == 12 || ks == 14) {
        int idx = (w < 6 ? w : 5) * 2 + (ks - 12) / 2;
        ldg16s(uq[(ks - 12) / 2], c.ws + OFF_UR + (size_t)(c.cb * 3 + (idx >> 2)) * 4096 + (idx & 3) * 1024, l * 16);
      }
      if (ks == 16) ldg8(k0, c.ssk + (size_t)c.cb * 3 * 2048, (l < 8 ? l >> 2 : 2) * 4096);
      if (ks == 3) {
        if (w == 6 && l < 12) *(LAS u32x4*)(c.lds + L_KC + l * 4160) = (u32x4){0, 0, 0, 0};
        if (w < 3 && l < 32) *(LAS u32x4*)(c.lds + L_U + w * 4608 + 4096 + l * 16) = (u32x4){0, 0, 0, 0};
      }
      if (ks == 30) {
        vmwait<1>();
        PIN(uq[0]); PIN(uq[1]); PIN(k0);
        if (w < 6) {
          int idx = w * 2;
          *(LAS u32x4*)(c.lds + L_U + (idx >> 2) * 4608 + (idx & 3) * 1024 + l * 16) = uq[0];
          *(LAS u32x4*)(c.lds + L_U + (idx >> 2) * 4608 + ((idx & 3) + 1) * 1024 + l * 16) = uq[1];
        }
        if (w == 6 && l < 12) {
          int cp = l & 3;
          u32x4 c1 = {0, 0, 0, 0};
          if (cp == 1) c1[3] = k0[0] << 16;
          if (cp == 2) c1[3] = k0[0];
          if (cp == 3) { c1[2] = k0[0] << 16; c1[3] = (k0[0] >> 16) | (k0[1] << 16); }
          *(LAS u32x4*)(c.lds + L_KC + l * 4160 + 16) = c1;
        }
      }
    }
    sbar();
  }
}

__device__ __forceinline__ void g1_epi_final(const G1Ctx& c, const f32x16& pa, u32 csoff, u32 hoff) {
  u32 P[8];
#pragma unroll
  for (int q = 0; q < 4; q++) {
    f32x4 cs = *(LAS f32x4*)(c.lds + csoff + q * 32);
    float h[4];
#pragma unroll
    for (int i = 0; i < 4; i++) {
      float X = __builtin_fmaf(c.nm, cs[i], pa[4 * q + i]);
      h[i] = X * __builtin_amdgcn_rcpf(1.0f + __builtin_amdgcn_exp2f(X * c.cc));
    }
    P[2 * q] = pk(h[0], h[1]); P[2 * q + 1] = pk(h[2], h[3]);
  }
  __builtin_amdgcn_raw_buffer_store_b128((u32x4){P[0], P[1], P[2], P[3]}, c.wr, hoff, 0, SC);
  __builtin_amdgcn_raw_buffer_store_b128((u32x4){P[4], P[5], P[6], P[7]}, c.wr, hoff + 512, 0, SC);
}

__device__ __forceinline__ void kv_compute(const u32x2 (&kr)[11], const u32x2 (&vr)[8], const u32x4 (&sq)[2],
                                           __amdgpu_buffer_rsrc_t wr, int ch, int t) {
#pragma unroll
  for (int q = 0; q < 4; q++) {
    u32 sa = sq[q >> 1][(q & 1) * 2], sb = sq[q >> 1][(q & 1) * 2 + 1];
    float w0 = blo(sa), w1 = bhi(sa), w2 = blo(sb), w3 = bhi(sb);
    float kk[11], o[8];
#pragma unroll
    for (int i = 0; i < 11; i++) kk[i] = (q & 1) ? bhi(kr[i][q >> 1]) : blo(kr[i][q >> 1]);
#pragma unroll
    for (int i = 0; i < 8; i++) {
      float vv = (q & 1) ? bhi(vr[i][q >> 1]) : blo(vr[i][q >> 1]);
      o[i] = (kk[i] * w3 + kk[i + 1] * w2 + kk[i + 2] * w1 + kk[i + 3] * w0) * vv;
    }
    u32x4 r = {pk(o[7], o[6]), pk(o[5], o[4]), pk(o[3], o[2]), pk(o[1], o[0])};
    __builtin_amdgcn_raw_buffer_store_b128(r, wr, OFF_UR + (ch + q) * 4096 + 2 * (2040 - t), 0, SC);
    sbar();
  }
}

__device__ __forceinline__ u32 ln_stats(const u32x4 (&sr)[2], int l) {
  float e[16];
#pragma unroll
  for (int i = 0; i < 8; i++) { e[2 * i] = blo(sr[i >> 2][i & 3]); e[2 * i + 1] = bhi(sr[i >> 2][i & 3]); }
  float s = 0;
#pragma unroll
  for (int i = 0; i < 16; i++) s += e[i];
  if (l >= 36) s = 0;
  float mean = wave_sum(s) * (1.0f / 576.0f);
  float vv = 0;
#pragma unroll
  for (int i = 0; i < 16; i++) { float d = e[i] - mean; vv += d * d; }
  if (l >= 36) vv = 0;
  float var = rb(wave_sum(vv) * (1.0f / 576.0f));
  float sd = rb(__builtin_sqrtf(rb(var + 1e-5f)));
  return pk(mean, sd);
}

__device__ __forceinline__ void g1_phase(G1Ctx& c, const u16* x, const u16* k, const u16* v, const u16* sk, const u16* w1, const u16* w2,
                                         int rt, int j, int kq TSARG) {
  LAS char* lds = c.lds;
  char* ws = c.ws;
  const int w = c.w, l = c.l, b = c.b, tid = w * 64 + l;
  const int tg = w & 3, fg = w >> 2, odd = j & 1, kh = l >> 5;
  const char* w1b = (const char*)w1 + j * 576;
  const char* sb = w1b + (odd ? 0 : 512);
  const char* pb = w1b + (odd ? 64 : 0);
  const u32 svo = (l >> 2) * 9216 + (l & 3) * 16;
  const bool kvw = w >= 5 && b < 192;
  const int kid = 3 * b + w - 5, kch = (kid >> 6) * 64 + (l & 15) * 4, kt = (kid & 63) * 32 + (l >> 4) * 8;
  if (tid == 0) *(LAS u32x2*)(lds + L_CNT) = (u32x2){0, 0};
  if (w < 4) {
    u32x2 ep;
    ldg8s(ep, ws + OFF_E, 0);
    const char* xb = (const char*)x + (size_t)rt * 128 * 1152;
#pragma unroll
    for (int h = 0; h < 4; h++) {
      u32 vo = (32 * w + 8 * h + (l >> 3)) * 1152 + (((l & 7) ^ (4 * (h & 1) + (l >> 4))) * 16);
#pragma unroll
      for (int kb = 0; kb < 9; kb++) glds(xb + kb * 128, vo, kb * 16384 + (32 * w + 8 * h) * 128);
    }
#pragma unroll
    for (int i = 0; i < 3; i++) glds(sb + (size_t)(w * 3 + i) * 16 * 9216, svo, L_S1 + (w * 3 + i) * 1024);
    {
      int n = ((b >> 3) & 7) * 120 + w * 30 + (l < 30 ? l : 29);
      u32 o = n < 384 ? (192 + n) * 9216 + (odd ? 0 : 512) : (n - 384) * 9216 + (odd ? 64 : 0);
      glds4(w1b, o, L_CSS + w * 256);
    }
    vmwait<1>();
    PIN(ep);
    c.tag = (((u64)(u32)__builtin_amdgcn_readfirstlane(ep[1]) << 32) | (u32)__builtin_amdgcn_readfirstlane(ep[0])) + 1;
  } else {
    const int pw = w - 4;
    u32x2 ep;
    ldg8s(ep, ws + OFF_E, 0);
    __builtin_amdgcn_s_setprio(3);
    u32x4 sr[2][2], cq[6];
    const int stok = rt * 128 + j * 8 + 2 * pw;
    {
      u32 o = stok * 1152 + (l < 36 ? l : 35) * 32;
      ldg16(sr[0][0], x, o); ldg16(sr[0][1], x, o + 16); ldg16(sr[1][0], x, o + 1152); ldg16(sr[1][1], x, o + 1168);
    }
    const int cuq = (b & 1) * 32 + (b >> 3);
    const int cu = cuq < 54 ? cuq : 53;
    const int ccl = 18 * kq + cu % 18, cpp = cu / 18;
    {
      u32 o = (cpp * 192 + (l >> 1)) * 9216 + ccl * 128 + pw * 32 + (l & 1) * 16;
#pragma unroll
      for (int i = 0; i < 6; i++) ldg16(cq[i], w1, o + i * 32 * 9216);
    }
    vmwait<6>();
    PIN(ep); PIN(sr[0][0]); PIN(sr[0][1]); PIN(sr[1][0]); PIN(sr[1][1]);
    c.tag = (((u64)(u32)__builtin_amdgcn_readfirstlane(ep[1]) << 32) | (u32)__builtin_amdgcn_readfirstlane(ep[0])) + 1;
    {
      u32 s0 = ln_stats(sr[0], l), s1 = ln_stats(sr[1], l);
      __builtin_amdgcn_raw_buffer_store_b64((u32x2){l ? s1 : s0, (u32)c.tag}, c.wr, l < 2 ? OFF_STATS + (stok + l) * 8 : OOB, 0, SC);
    }
    vmwait<1>();
#pragma unroll
    for (int i = 0; i < 6; i++) PIN(cq[i]);
    {
      float a[8];
#pragma unroll
      for (int i = 0; i < 4; i++) {
        a[2 * i] = (blo(cq[0][i]) + blo(cq[1][i])) + (blo(cq[2][i]) + blo(cq[3][i])) + (blo(cq[4][i]) + blo(cq[5][i]));
        a[2 * i + 1] = (bhi(cq[0][i]) + bhi(cq[1][i])) + (bhi(cq[2][i]) + bhi(cq[3][i])) + (bhi(cq[4][i]) + bhi(cq[5][i]));
      }
      float b0 = swp32add(a[0], a[4]), b1 = swp32add(a[1], a[5]), b2 = swp32add(a[2], a[6]), b3 = swp32add(a[3], a[7]);
      float c0 = swp16add(b0, b2), c1 = swp16add(b1, b3);
      c0 += dppf<0x128>(c0); c1 += dppf<0x128>(c1);
      c0 += dppf<0x124>(c0); c1 += dppf<0x124>(c1);
      c0 += dppf<0x122>(c0); c1 += dppf<0x122>(c1);
      u32 col = ccl * 64 + pw * 16 + (l & 1) * 8 + (l >> 5) * 4 + ((l >> 4) & 1) * 2;
      __builtin_amdgcn_raw_buffer_store_b128((u32x4){__float_as_uint(c0), (u32)c.tag, __float_as_uint(c1), (u32)c.tag}, c.wr,
                                             (cuq < 54 && (l & 14) == 0) ? OFF_CS + (cpp * F_ + col) * 8 : OOB, 0, SC);
    }
    __builtin_amdgcn_s_setprio(0);
  }
  lgwait(); bar();
  TS(1);
  u32x4 xf[36];
  auto xread = [&]() {
    u32 row = tg * 32 + (l & 31);
#pragma unroll
    for (int q = 0; q < 4; q++) {
      u32 a = row * 128 + (((2 * q + (l >> 5)) ^ ((row >> 1) & 7)) * 16);
#pragma unroll
      for (int kb = 0; kb < 9; kb++) xf[kb * 4 + q] = *(LAS u32x4*)(lds + a + kb * 16384);
    }
    lgwait(); bar();
    TS(2);
  };
  const u32 pvo = (l >> 3) * 9216 + (((l & 7) ^ (((l >> 4) & 1) << 2)) * 16);
  const char* pwp = pb + (size_t)w * 8 * 9216;
  f32x16 acc0 = {}, acc1 = {};
  if (w < 4) {
    xread();
#pragma unroll
    for (int i = 0; i < 6; i++) glds(sb + (size_t)(12 + w + 4 * i) * 16 * 9216, svo, (w + 4 * i) * 1024);
    u32 sa = (8 * kh + ((l >> 2) & 3)) * 64 + ((l >> 4) & 1) * 32 + (l & 3) * 8;
    LAS char* s0 = lds + L_S1 + sa;
    LAS char* s1 = lds + sa - 12 * 1024;
    u32x2 ra[3], rb_[3];
    ra[0] = trread(s0); rb_[0] = trread(s0 + 256);
    ra[1] = trread(s0 + 1024); rb_[1] = trread(s0 + 1024 + 256);
#pragma unroll
    for (int ks = 0; ks < 36; ks++) {
      if (ks < 9) {
        glds(pb + (size_t)(w + 8 * ks) * 8 * 9216, pvo, L_B1 + (w + 8 * ks) * 1024);
        glds(pb + (size_t)(w + 8 * ks + 4) * 8 * 9216, pvo, L_B1 + (w + 8 * ks + 4) * 1024);
      }
      if (ks == 10) {
        vmwait<18>();
        TS(17);
        lds_arrive(lds + L_CNT + 4, l);
        lds_await(lds + L_CNT + 4, 4);
      }
      if (ks + 2 < 36) {
        LAS char* ap = (ks + 2 < 12 ? s0 : s1) + (ks + 2) * 1024;
        ra[(ks + 2) % 3] = trread(ap); rb_[(ks + 2) % 3] = trread(ap + 256);
      }
      u32x4 A = {ra[ks % 3][0], ra[ks % 3][1], rb_[ks % 3][0], rb_[ks % 3][1]};
      acc1 = mfma32(A, xf[ks], acc1);
      sbar();
    }
    TS(18);
    vmwait<0>();
    TS(19);
  } else if (w == 4) {
    u32x4 srd = mksrd(ws, WS_END), sg, cg[3][3];
    const u32 tg32 = (u32)c.tag;
    const u32 so = OFF_STATS + rt * 1024 + l * 16, co = OFF_CS + j * 2304 + l * 16;
    auto sweep = [&]() {
      ldb16s(sg, srd, so);
#pragma unroll
      for (int p = 0; p < 3; p++) {
        ldb16s(cg[p][0], srd, co + p * F_ * 8); ldb16s(cg[p][1], srd, co + p * F_ * 8 + 1024);
        ldb16s(cg[p][2], srd, l < 16 ? co + p * F_ * 8 + 2048 : OOB);
      }
    };
    sweep();
    xread();
    TS(17);
#pragma unroll 1
    for (int spins = 0; spins < 1000000; spins++) {
      vmwait<0>();
      PIN(sg);
      bool ok = sg[1] == tg32 && sg[3] == tg32;
#pragma unroll
      for (int p = 0; p < 3; p++) {
        PIN(cg[p][0]); PIN(cg[p][1]); PIN(cg[p][2]);
        ok = ok && cg[p][0][1] == tg32 && cg[p][0][3] == tg32 && cg[p][1][1] == tg32 && cg[p][1][3] == tg32;
        ok = ok && (l >= 16 || (cg[p][2][1] == tg32 && cg[p][2][3] == tg32));
      }
      if (__all(ok)) break;
      sweep();
    }
    vmwait<0>();
    TS(18);
    *(LAS u32x2*)(lds + L_ST + l * 8) = (u32x2){sg[0], sg[2]};
#pragma unroll
    for (int i = 0; i < 3; i++) {
      float c0 = __uint_as_float(cg[0][i][0]) + __uint_as_float(cg[1][i][0]) + __uint_as_float(cg[2][i][0]);
      float c1 = __uint_as_float(cg[0][i][2]) + __uint_as_float(cg[1][i][2]) + __uint_as_float(cg[2][i][2]);
      *(LAS f32x2*)(lds + (i < 2 || l < 16 ? L_CS + i * 512 : L_CSS + 1024) + l * 8) = (f32x2){c0, c1};
    }
    {
      const char* kp = (const char*)c.ssk + (size_t)c.cb * 3 * 4096;
      glds4(kp, l * 128, L_CSS);
      glds4(kp, 8192 + (l & 31) * 128, L_CSS + 256);
      const char* wp = (const char*)w2 + (size_t)kq * 1327104 + (b >> 3) * 41472;
#pragma unroll
      for (int i = 0; i < 6; i++) glds4(wp, (i < 5 ? i * 64 + l : 320 + (l & 3)) * 128, L_CSS + 512);
    }
    TS(19);
  } else if (kvw) {
    u32x2 kr[11], vr[8];
    u32x4 sq[2];
    {
      u32x4 rk = mksrd(k, S_ * D_ * 2), rv = mksrd(v, S_ * D_ * 2), rs = mksrd(sk, D_ * 8);
      u32 o = kt * 1152 + kch * 2;
#pragma unroll
      for (int i = 0; i < 11; i++) ldb8(kr[i], rk, o + (i - 3) * 1152);
#pragma unroll
      for (int i = 0; i < 8; i++) ldb8(vr[i], rv, o + i * 1152);
      ldb16(sq[0], rs, kch * 8); ldb16(sq[1], rs, kch * 8 + 16);
    }
    xread();
    __builtin_amdgcn_s_setprio(2);
    vmwait<0>();
#pragma unroll
    for (int i = 0; i < 11; i++) PIN(kr[i]);
#pragma unroll
    for (int i = 0; i < 8; i++) PIN(vr[i]);
    PIN(sq[0]); PIN(sq[1]);
    TS(17);
    kv_compute(kr, vr, sq, c.wr, kch, kt);
    TS(18);
    __builtin_amdgcn_s_setprio(0);
  } else {
    xread();
  }
  lgwait(); bar();
  TS(3);
  {
    u32 st = *(LAS u32*)(lds + L_ST + (tg * 32 + (l & 31)) * 4);
    c.nm = -blo(st);
    c.cc = -1.44269504088896f / bhi(st);
  }
  const u32 abase = (8 * kh + ((l >> 2) & 3)) * 128 + ((fg * 64 + ((l >> 4) & 1) * 32 + (l & 3) * 8) ^ (((l >> 3) & 1) << 6));
  const u32 hbase = OFF_H + (rt * 144 + 9 * j) * 8192 + (2 * tg + ((l >> 4) & 1)) * 1024 + (kh * 16 + (l & 15)) * 16;
  LAS char* aB0 = lds + abase;
  LAS char* aB1 = lds + L_B1 + abase;
  LAS char* csb = lds + L_CS + kh * 16;
  int ptile = odd ? 0 : 8;
  u32 hoff = (w < 4) ? hbase + ptile * 8192 : OOB;
  const int klo = (((c.cb * 3) >> 6) * 64) / 3, khi = (((c.cb * 3 + 2) >> 6) * 64 + 63) / 3;
  const bool kfact = klo + l <= khi;
  const u32 kfoff = OFF_FK + (kfact ? klo + l : klo) * 8;
  u32x2 ra[3], rb_[3];
  f32x4 cs[2];
  ra[0] = trread(aB1); rb_[0] = trread(aB1 + 512);
  ra[1] = trread(aB1 + 2048); rb_[1] = trread(aB1 + 2048 + 512);
  cs[0] = *(LAS f32x4*)(csb + ptile * 128);
#pragma unroll 1
  for (int pp = 0; pp < 2; pp++) {
    int t0 = odd + 4 * pp + fg, t1 = t0 + 2;
    g1_pair<0>(c, xf, acc0, acc1, aB1, aB0, ra, rb_, cs, csb + ptile * 128, csb + t0 * 128, hoff,
               pwp + (2 * pp + 1) * 128, pvo, w * 1024, pp, kfoff, kfact);
    if (pp == 0) { TS(4); } else { TS(6); }
    hoff = hbase + t0 * 8192;
    ptile = t0;
    if (pp == 1) break;
    g1_pair<1>(c, xf, acc1, acc0, aB0, aB1, ra, rb_, cs, csb + t0 * 128, csb + t1 * 128, hoff,
               pwp + 2 * 128, pvo, L_B1 + w * 1024, pp, kfoff, kfact);
    TS(5);
    ptile = t1;
    hoff = hbase + t1 * 8192;
  }
  g1_pair<2>(c, xf, acc1, acc0, aB0, aB1, ra, rb_, cs, csb + ptile * 128, csb, hoff, pwp, pvo, 0, 1, kfoff, kfact);
  TS(7);
  ptile += 2;
  g1_epi_final(c, acc1, L_CS + ptile * 128 + kh * 16, hbase + ptile * 8192);
}

static constexpr u32 L_RED = L_B1;
static constexpr u32 L_EX = 73728;
static constexpr u32 L_OWN = 0;
static constexpr u32 L_Y = 18432;

template <int P>
__device__ __forceinline__ void w2_piece(const char* wsrc, const u32 (&cvo)[5], u32 ldst, int w, int l) {
  if (P < 4) glds(wsrc, cvo[P < 4 ? P : 0], ldst + (w * 4 + P) * 1024);
  else if (l < 32) glds(wsrc, cvo[4], ldst + (32 + (w >> 1)) * 1024 + (w & 1) * 512);
}
template <int P>
__device__ __forceinline__ void w2_piece10(const char* wsrc, const u32 (&cvo)[5], int w, int l) {
  w2_piece<P % 5>(wsrc + (P / 5) * 73728, cvo, (P / 5) * 36864, w, l);
}
template <bool GL>
__device__ __forceinline__ void conv_res(f32x4 (&acc)[8], u32x4 (&A)[8], u32x4 (&B)[8], LAS char* ap, LAS char* bp,
                                         const char* wsrc, const u32 (&cvo)[5], int w, int l) {
#pragma unroll
  for (int q = 0; q < 8; q++) {
#pragma unroll
    for (int i = 7 - q; i >= 0; i--) acc[i + q] = mfma16(A[i], B[q], acc[i + q]);
    B[q] = *(LAS u32x4*)(bp + 512 * (7 - q));
    u32x2 lo = *(LAS u32x2*)(ap + 512 * (7 - q)), hi = *(LAS u32x2*)(ap + 512 * (7 - q) + 8);
    A[7 - q] = (u32x4){lo[0], lo[1], hi[0], hi[1]};
    if (GL) {
      if (q == 0) { w2_piece10<0>(wsrc, cvo, w, l); w2_piece10<1>(wsrc, cvo, w, l); }
      if (q == 1) { w2_piece10<2>(wsrc, cvo, w, l); w2_piece10<3>(wsrc, cvo, w, l); }
      if (q == 2) w2_piece10<4>(wsrc, cvo, w, l);
      if (q == 3) w2_piece10<5>(wsrc, cvo, w, l);
      if (q == 4) w2_piece10<6>(wsrc, cvo, w, l);
      if (q == 5) w2_piece10<7>(wsrc, cvo, w, l);
      if (q == 6) w2_piece10<8>(wsrc, cvo, w, l);
      if (q == 7) w2_piece10<9>(wsrc, cvo, w, l);
    }
    sbar();
  }
}

struct G2Ctx {
  const char* wsrc;
  const char* hsrc;
  char* ws;
  LAS char* apA;
  LAS char* apB;
  u32x4 srd, srx;
  u64 tag;
  u32 cvo[5];
  u32 fyoff, yoff0, yoff1, xoff0, xoff2;
  u32 clr[4];
  __amdgpu_buffer_rsrc_t wr;
  bool fyact;
  int w, l, b;
};
struct G2Tail { u32x2 pf; u32x4 fh[2], yq[2]; u32x2 xv[2]; u32 x8; };

template <int I>
__device__ __forceinline__ void g2_issue1(const char* wsrc, const char* hsrc, const u32 (&cvo)[5], u32 ldst, int w, int l, u32x4 (&hb)[4]) {
  if (I % 2 == 0) w2_piece<I / 2>(wsrc, cvo, ldst, w, l);
  if (I == 1) ldg16s(hb[0], hsrc, l * 16);
  if (I == 3) ldg16so(hb[1], hsrc, l * 16);
  if (I == 5) ldg16s(hb[2], hsrc + 8192, l * 16);
  if (I == 7) ldg16so(hb[3], hsrc + 8192, l * 16);
}
template <int IT, int SS>
__device__ __forceinline__ void g2_aread(const G2Ctx& c, u32x2& ra, u32x2& rb_) {
  LAS char* p = ((IT & 2) ? c.apB : c.apA) + (IT & 1) * 36864 + (SS / 9) * 9216 + (SS % 9) * 32;
  ra = trread(p); rb_ = trread(p + 2304);
}
template <int IT, int SS>
__device__ __forceinline__ void g2_step(const G2Ctx& c, G2Tail& t, u32x4 (&hb)[4][4], f32x4 (&acc)[2][9], u32x2 (&ra)[3], u32x2 (&rb_)[3]) {
  constexpr int u = SS / 9, n = SS % 9;
  const int w = c.w, l = c.l;
  if (SS + 2 < 18) g2_aread<IT, (SS + 2) % 18>(c, ra[(SS + 2) % 3], rb_[(SS + 2) % 3]);
  else if (SS == 17 && IT < 8) g2_aread<IT + 1, 1>(c, ra[1], rb_[1]);
  if (SS == 16) lgwait();
  u32x4 A = {ra[SS % 3][0], ra[SS % 3][1], rb_[SS % 3][0], rb_[SS % 3][1]};
  acc[0][n] = mfma16(A, hb[IT & 3][u * 2], acc[0][n]);
  acc[1][n] = mfma16(A, hb[IT & 3][u * 2 + 1], acc[1][n]);
  if (SS == 16 && IT < 8) {
    if (IT == 0) vmwait<4 + 3 + 5 + 8 + 2>();
    else if (IT + 3 < 9) vmwait<9 + 8 + (IT == 1 ? 3 : IT == 2 ? 2 : IT == 3 ? 1 : 0)>();
    else if (IT + 2 < 9) vmwait<9>();
    else vmwait<0>();
    bar();
    PIN(hb[(IT + 1) & 3][0]); PIN(hb[(IT + 1) & 3][1]); PIN(hb[(IT + 1) & 3][2]); PIN(hb[(IT + 1) & 3][3]);
    if (IT == 1 && c.b < 192 && w == 0 && l == 0) flag_set(c.ws, OFF_FY + c.b * 8, c.tag);
    if (IT == 4 && w == 0 && l == 0) flag_set(c.ws, OFF_FX + c.b * 8, c.tag);
    g2_aread<IT + 1, 0>(c, ra[0], rb_[0]);
  }
  if ((IT == 0 && (SS == 4 || SS == 12)) || ((IT == 1 || IT == 2) && SS == 8))
    __builtin_amdgcn_raw_buffer_store_b128((u32x4){EMPTY, EMPTY, EMPTY, EMPTY}, c.wr, c.clr[IT == 0 ? SS / 12 : IT + 1], 0, SC);
  if (IT + 3 < 9) {
    const char* ws_ = c.wsrc + (IT + 3) * 73728;
    const char* hs_ = c.hsrc + (IT + 3) * 16384;
    if (SS % 2 == 1 && SS < 16) g2_issue1<(SS % 2 == 1 ? SS / 2 : 0)>(ws_, hs_, c.cvo, ((IT + 3) & 3) * 36864, w, l, hb[(IT + 3) & 3]);
    if (SS == 17) g2_issue1<8>(ws_, hs_, c.cvo, ((IT + 3) & 3) * 36864, w, l, hb[(IT + 3) & 3]);
  }
  if (IT == 7 && SS == 17) {
    ldg8s(t.pf, c.ws, c.fyoff);
    u32 o = (c.b == 0 && w == 7) ? OFF_FH + l * 32 : OOB;
    ldb16s(t.fh[0], c.srd, o); ldb16s(t.fh[1], c.srd, o + 16);
  }
  if (IT == 8 && SS == 12) {
    vmwait<0>();
    PIN(t.pf); PIN(t.fh[0]); PIN(t.fh[1]);
    if (!flag_ok(t.pf, c.fyact, c.tag)) flag_wait(c.ws, c.fyoff, c.fyact, c.tag);
    if (c.b == 0 && w == 7) {
      const u32 lo = (u32)c.tag, hi = (u32)(c.tag >> 32);
      bool ok = t.fh[0][0] == lo && t.fh[0][1] == hi && t.fh[0][2] == lo && t.fh[0][3] == hi &&
                t.fh[1][0] == lo && t.fh[1][1] == hi && t.fh[1][2] == lo && t.fh[1][3] == hi;
      if (!__all(ok))
        for (int i = 0; i < 4; i++) flag_wait(c.ws, OFF_FH + (i * 64 + l) * 8, true, c.tag);
      if (l == 0) flag_set(c.ws, OFF_E, c.tag);
    }
    ldb16s(t.yq[0], c.srd, c.yoff0); ldb16s(t.yq[1], c.srd, c.yoff1);
    ldb8(t.xv[0], c.srx, c.xoff0); ldb8(t.xv[1], c.srx, c.xoff0 + 32); ldb2(t.x8, c.srx, c.xoff2);
  }
  sbar();
}
template <int IT, int SS>
__device__ __forceinline__ void g2_steps(const G2Ctx& c, G2Tail& t, u32x4 (&hb)[4][4], f32x4 (&acc)[2][9], u32x2 (&ra)[3], u32x2 (&rb_)[3]) {
  g2_step<IT, SS>(c, t, hb, acc, ra, rb_);
  if constexpr (SS < 17) g2_steps<IT, SS + 1>(c, t, hb, acc, ra, rb_);
  else if constexpr (IT < 8) g2_steps<IT + 1, 0>(c, t, hb, acc, ra, rb_);
}
__device__ __forceinline__ f32x4 rb4(f32x4 v) {
  u32 a = pk(v[0], v[1]), b = pk(v[2], v[3]);
  return (f32x4){blo(a), bhi(a), blo(b), bhi(b)};
}
__device__ __forceinline__ f32x4 gate_in(f32x4 x, u32x2 yv, float m, float rs) {
  f32x4 xn = rb4(rb4(x - m) * rs);
  return rb4(xn * (f32x4){blo(yv[0]), bhi(yv[0]), blo(yv[1]), bhi(yv[1])});
}
__device__ __forceinline__ void fin4(f32x4 s, f32x4 x, f32x4 sv, float rs, u16* op) {
  f32x4 o = rb4(s * rs), g;
#pragma unroll
  for (int e = 0; e < 4; e++) g[e] = o[e] * __builtin_amdgcn_rcpf(1.0f + __builtin_amdgcn_exp2f(o[e] * -1.44269504088896f));
  f32x4 res = x + rb4(sv * rb4(g));
  u32x2 r = {pk(res[0], res[1]), pk(res[2], res[3])};
  asm volatile("global_store_dwordx2 %0, %1, off sc0 sc1" ::"v"(op), "v"(r) : "memory");
}
__device__ __forceinline__ u32 umax(u32 a, u32 b) { return a > b ? a : b; }
__device__ __forceinline__ u32 pkw(float a, float b) { u32 v = pk(a, b); return v < EMPTY - 1 ? v : EMPTY - 1; }
#define PDELAY 12
__device__ __forceinline__ f32x4 up4(u32 a, u32 b) { return (f32x4){blo(a), bhi(a), blo(b), bhi(b)}; }

__device__ __forceinline__ void conv_g2(G1Ctx& g, const u16* x, const u16* Dv, const u16* w2, u16* out, int rt, int kq, int dn TSARG) {
  LAS char* lds = g.lds;
  char* ws = g.ws;
  const int w = g.w, l = g.l, b = g.b, cb = g.cb;
  const int tg = w & 3, kh = w >> 2, xbit = rt >> 3, i7 = rt & 7, lg = l >> 4;
  G2Ctx c;
  c.ws = ws; c.tag = g.tag; c.w = w; c.l = l; c.b = b;
#pragma unroll
  for (int i = 0; i < 5; i++) {
    u32 idx = i < 4 ? (w * 4 + i) * 64 + l : (32 + (w >> 1)) * 64 + (w & 1) * 32 + (l & 31);
    u32 row = idx / 18, c16 = idx - row * 18;
    c.cvo[i] = ((row >> 6) * 576 + (row & 63)) * 1152 + c16 * 16;
  }
  c.wsrc = (const char*)w2 + (size_t)(1152 * kq) * 1152 + dn * 288;
  c.hsrc = ws + OFF_H + (size_t)((rt * 144 + 36 * kq + 18 * kh) * 8 + 2 * tg) * 1024;
  f32x4 ca[8];
#pragma unroll
  for (int i = 0; i < 8; i++) ca[i] = (f32x4){0, 0, 0, 0};
  const int ch = (w < 3) ? 0 : (w < 6) ? 1 : 2;
  const int r0 = (w == 0 || w == 4 || w == 6) ? 0 : (w == 1 || w == 5) ? 3 : (w == 7) ? 4 : 6;
  const int nr = (w >= 6) ? 4 : (w == 2 || w == 3) ? 2 : 3;
  const int lb = l & 15, cp = (lb + 1) & 3;
  LAS char* ap = lds + L_KC + (ch * 4 + cp) * 4160 + 2 * (lb + 1 - cp) + 16 * lg + 64 * r0;
  LAS char* bp = lds + L_U + ch * 4608 + 2 * (240 - 16 * lb + 8 * lg) + 64 * r0;
  u32x4 A[8], B[8];
  if (b < 192) {
#pragma unroll
    for (int i = 7; i >= 0; i--) {
      B[7 - i] = *(LAS u32x4*)(bp + 512 * i);
      u32x2 lo = *(LAS u32x2*)(ap + 512 * i), hi = *(LAS u32x2*)(ap + 512 * i + 8);
      A[i] = (u32x4){lo[0], lo[1], hi[0], hi[1]};
    }
    conv_res<true>(ca, A, B, ap + 64, bp + 64, c.wsrc, c.cvo, w, l);
  } else {
    w2_piece10<0>(c.wsrc, c.cvo, w, l); w2_piece10<1>(c.wsrc, c.cvo, w, l); w2_piece10<2>(c.wsrc, c.cvo, w, l);
    w2_piece10<3>(c.wsrc, c.cvo, w, l); w2_piece10<4>(c.wsrc, c.cvo, w, l); w2_piece10<5>(c.wsrc, c.cvo, w, l);
    w2_piece10<6>(c.wsrc, c.cvo, w, l); w2_piece10<7>(c.wsrc, c.cvo, w, l); w2_piece10<8>(c.wsrc, c.cvo, w, l);
    w2_piece10<9>(c.wsrc, c.cvo, w, l);
  }
  vmwait<10>();
  if (l == 0) {
    u32 old = __hip_atomic_fetch_add((LAS u32*)(lds + L_CNT), 1u, __ATOMIC_RELAXED, __HIP_MEMORY_SCOPE_WORKGROUP);
    if (old == 7) flag_set(ws, OFF_FH + b * 8, g.tag);
  }
  TS(8);
  if (b < 192) {
#pragma unroll 1
    for (int r = 1; r < nr; r++) conv_res<false>(ca, A, B, ap + 64 * (r + 1), bp + 64 * (r + 1), c.wsrc, c.cvo, w, l);
  }
  TS(9);
  u32x2 uu[3];
#pragma unroll
  for (int i = 0; i < 3; i++) {
    int u = w * 3 + i;
    uu[i] = *(LAS u32x2*)(lds + L_U + (u >> 3) * 4608 + 2 * (2044 - (256 * (u & 7) + 16 * lb + 4 * lg)));
  }
  const u32 fhoff = OFF_FH + ((b & 7) + 8 * ((l & 3) * 8 + i7)) * 8;
  u32x2 pf;
  ldg8s(pf, ws, fhoff);
  lgwait(); bar();
#pragma unroll
  for (int i = 0; i < 8; i++) *(LAS f32x4*)(lds + L_RED + w * 8192 + i * 1024 + l * 16) = ca[i];
  lgwait(); bar();
  TS(10);
  f32x4 ys[3];
#pragma unroll
  for (int i = 0; i < 3; i++) {
    int u = w * 3 + i, c2 = u >> 3;
    LAS char* rp = lds + L_RED + c2 * 3 * 8192 + (u & 7) * 1024 + l * 16;
    ys[i] = *(LAS f32x4*)(rp) + *(LAS f32x4*)(rp + 8192);
    if (c2 < 2) ys[i] += *(LAS f32x4*)(rp + 2 * 8192);
  }
  vmwait<0>();
  PIN(pf);
  if (!flag_ok(pf, true, g.tag)) flag_wait(ws, fhoff, true, g.tag);
  TS(11);
  u32x4 hb[4][4];
#pragma unroll
  for (int it = 0; it < 3; it++) {
    ldg16s(hb[it][0], c.hsrc + it * 16384, l * 16); ldg16so(hb[it][1], c.hsrc + it * 16384, l * 16);
    ldg16s(hb[it][2], c.hsrc + it * 16384 + 8192, l * 16); ldg16so(hb[it][3], c.hsrc + it * 16384 + 8192, l * 16);
  }
#pragma unroll
  for (int i = 0; i < 3; i++) {
    int u = w * 3 + i, chn = cb * 3 + (u >> 3);
    u32 dw = ((CAS const u32*)Dv)[chn >> 1];
    float dv = (chn & 1) ? bhi(dw) : blo(dw);
    float o0 = rb(ys[i][0]) + rb(bhi(uu[i][1]) * dv), o1 = rb(ys[i][1]) + rb(blo(uu[i][1]) * dv);
    float o2 = rb(ys[i][2]) + rb(bhi(uu[i][0]) * dv), o3 = rb(ys[i][3]) + rb(blo(uu[i][0]) * dv);
    u32 o = b < 192 ? OFF_Y2 + chn * 4096 + (256 * (u & 7) + 16 * lb + 4 * lg) * 2 : OOB;
    __builtin_amdgcn_raw_buffer_store_b64((u32x2){pk(o0, o1), pk(o2, o3)}, g.wr, o, 0, SC);
  }
  lgwait(); bar();
  w2_piece<0>(c.wsrc + 2 * 73728, c.cvo, 73728, w, l); w2_piece<1>(c.wsrc + 2 * 73728, c.cvo, 73728, w, l);
  w2_piece<2>(c.wsrc + 2 * 73728, c.cvo, 73728, w, l); w2_piece<3>(c.wsrc + 2 * 73728, c.cvo, 73728, w, l);
  w2_piece<4>(c.wsrc + 2 * 73728, c.cvo, 73728, w, l);
  c.apA = lds + (kh * 64 + 16 * (lg >> 1) + 4 * (lg & 1) + ((l >> 2) & 3)) * 288 + (l & 3) * 8;
  c.apB = c.apA + 73728;
  c.srd = mksrd(ws, WS_END);
  c.srx = mksrd(x, S_ * D_ * 2);
  const int p = w & 3, tt = w >> 2;
  const int tok = rt * 128 + kq * 32 + tt * 16 + lb, d0 = dn * 144 + 4 * lg;
  const int t8 = tt * 16 + 4 * p + lg, tok8 = rt * 128 + kq * 32 + t8;
  c.fyact = l < 51;
  c.fyoff = l < 48 ? OFF_FY + (48 * dn + l) * 8 : OFF_FX + (2 * ((kq + 1 + (l < 51 ? l - 48 : 0)) & 3) + xbit + 8 * (dn * 8 + i7)) * 8;
  c.wr = g.wr;
#pragma unroll
  for (int i = 0; i < 4; i++) {
    int n = w * 4 + i, s = n / 9;
    int ob = 2 * ((kq + 1 + s) & 3) + xbit + 8 * (dn * 8 + i7);
    c.clr[i] = n < 27 ? OFF_P + (ob * 3 + 2 - s) * 9216 + (n - 9 * s) * 1024 + l * 16 : OOB;
  }
  {
    u32 yb = OFF_Y2 + (dn * 144 + w * 16 + (l >> 2)) * 4096 + (rt * 128 + kq * 32) * 2 + (l & 3) * 16;
    c.yoff0 = yb;
    c.yoff1 = w == 0 ? yb + 128 * 4096 : OOB;
    c.xoff0 = tok * 1152 + (d0 + p * 32) * 2;
    c.xoff2 = tok8 * 1152 + (dn * 144 + 128 + lb) * 2;
  }
  f32x4 acc[2][9];
#pragma unroll
  for (int n = 0; n < 9; n++) { acc[0][n] = (f32x4){0, 0, 0, 0}; acc[1][n] = (f32x4){0, 0, 0, 0}; }
  G2Tail t;
  u32x2 ra[3], rb_[3];
  g2_aread<0, 0>(c, ra[0], rb_[0]); g2_aread<0, 1>(c, ra[1], rb_[1]);
  vmwait<8 + 3 + 5>();
  PIN(hb[0][0]); PIN(hb[0][1]); PIN(hb[0][2]); PIN(hb[0][3]);
  TS(12);
  g2_steps<0, 0>(c, t, hb, acc, ra, rb_);
  TS(13);
#pragma unroll
  for (int n = 0; n < 9; n++) *(LAS f32x4*)(lds + L_EX + w * 9216 + n * 1024 + l * 16) = kh ? acc[0][n] : acc[1][n];
  lgwait(); bar();
  f32x4 r[9];
#pragma unroll
  for (int n = 0; n < 9; n++) r[n] = (kh ? acc[1][n] : acc[0][n]) + *(LAS f32x4*)(lds + L_EX + (w ^ 4) * 9216 + n * 1024 + l * 16);
  {
    int ob = 2 * tg + xbit + 8 * (dn * 8 + i7);
    u32 po = tg != kq ? OFF_P + (ob * 3 + ((kq - tg - 1) & 3)) * 9216 + kh * 4608 : OOB;
#pragma unroll
    for (int q = 0; q < 4; q++)
      __builtin_amdgcn_raw_buffer_store_b128((u32x4){pkw(r[2 * q][0], r[2 * q][1]), pkw(r[2 * q][2], r[2 * q][3]),
                                                     pkw(r[2 * q + 1][0], r[2 * q + 1][1]), pkw(r[2 * q + 1][2], r[2 * q + 1][3])},
                                             g.wr, po + q * 1024 + l * 16, 0, SC);
    __builtin_amdgcn_raw_buffer_store_b64((u32x2){pkw(r[8][0], r[8][1]), pkw(r[8][2], r[8][3])}, g.wr, po + 4096 + l * 8, 0, SC);
    if (tg == kq) {
#pragma unroll
      for (int n = 0; n < 9; n++) *(LAS f32x4*)(lds + L_OWN + kh * 9216 + n * 1024 + l * 16) = r[n];
    }
  }
  vmwait<5>();
  PIN(t.yq[0]); PIN(t.yq[1]); PIN(t.xv[0]); PIN(t.xv[1]); PIN(t.x8);
  *(LAS u32x4*)(lds + L_Y + (w * 16 + (l >> 2)) * 96 + (l & 3) * 16) = t.yq[0];
  if (w == 0) *(LAS u32x4*)(lds + L_Y + (128 + (l >> 2)) * 96 + (l & 3) * 16) = t.yq[1];
  lgwait(); bar();
  TS(14);
  const u32 st = *(LAS u32*)(lds + L_ST + (kq * 32 + tt * 16 + lb) * 4), st8 = *(LAS u32*)(lds + L_ST + (kq * 32 + t8) * 4);
  LAS char* yp = lds + L_Y + (4 * lg + ((l >> 2) & 3)) * 96 + tt * 32 + (l & 3) * 8;
  LAS char* op = lds + L_OWN + tt * 9216;
  const u32x2 y0 = trread(yp + p * 32 * 96), y1 = trread(yp + (p * 32 + 16) * 96);
  const u32 y8 = *(LAS u16*)(lds + L_Y + (128 + lb) * 96 + t8 * 2);
  int lz = l;
  asm volatile("" : "+v"(lz));
  const int l8 = ((lz & 15) >> 2) * 16 + 4 * p + (lz >> 4);
  f32x4 s0 = *(LAS f32x4*)(op + 2 * p * 1024 + l * 16), s1 = *(LAS f32x4*)(op + (2 * p + 1) * 1024 + l * 16);
  float s8 = *(LAS float*)(op + 8 * 1024 + l8 * 16 + (lz & 3) * 4);
  float rs = 1.0f / bhi(st), rs8 = 1.0f / bhi(st8);
  f32x4 xa = up4(t.xv[0][0], t.xv[0][1]), xb = up4(t.xv[1][0], t.xv[1][1]);
  float x8 = blo(t.x8);
  f32x4 va = gate_in(xa, y0, blo(st), rs), vb = gate_in(xb, y1, blo(st), rs);
  float v8 = rb(rb(rb(x8 - blo(st8)) * rs8) * blo(y8));
  PIN(va); PIN(vb); PIN(v8); PIN(s0); PIN(s1); PIN(s8); PIN(rs); PIN(rs8);
  u32x4 pq[3];
  u32 p8[3];
  {
    const u32 po = OFF_P + b * 3 * 9216 + tt * 4608;
    const u32 o16 = po + p * 1024 + l * 16, o8 = po + 4096 + l8 * 8 + (lz & 2) * 2;
    auto sweep = [&]() {
#pragma unroll
      for (int s = 0; s < 3; s++) { ldb16s(pq[s], c.srd, o16 + s * 9216); ldb4s(p8[s], c.srd, o8 + s * 9216); }
    };
    if (PDELAY) __builtin_amdgcn_s_sleep(PDELAY);
    sweep();
#pragma unroll 1
    for (int spins = 0; spins < 1000000; spins++) {
      vmwait<0>();
      u32 mx = 0;
#pragma unroll
      for (int s = 0; s < 3; s++) {
        PIN(pq[s]); PIN(p8[s]);
        mx = umax(umax(mx, p8[s]), umax(umax(pq[s][0], pq[s][1]), umax(pq[s][2], pq[s][3])));
      }
      if (__all(mx != EMPTY)) break;
      sweep();
    }
    vmwait<0>();
#pragma unroll
    for (int s = 0; s < 3; s++) { PIN(pq[s]); PIN(p8[s]); }
  }
  TS(15);
  const u32 sh8 = (lz & 1) ? 0 : 16;
#pragma unroll
  for (int s = 0; s < 3; s++) {
    s0 += up4(pq[s][0], pq[s][1]); s1 += up4(pq[s][2], pq[s][3]);
    s8 += __uint_as_float((p8[s] << sh8) & 0xffff0000u);
  }
  u16* o = out + tok * D_ + d0 + p * 32;
  fin4(s0, xa, va, rs, o);
  fin4(s1, xb, vb, rs, o + 16);
  {
    float oo = rb(s8 * rs8);
    float gg = rb(oo * __builtin_amdgcn_rcpf(1.0f + __builtin_amdgcn_exp2f(oo * -1.44269504088896f)));
    float res = x8 + rb(v8 * gg);
    asm volatile("global_store_short %0, %1, off sc0 sc1" ::"v"(out + tok8 * D_ + dn * 144 + 128 + lb), "v"(pk(res, res)) : "memory");
  }
}

__global__ void __launch_bounds__(512, 2) k_fused(const u16* x, const u16* k, const u16* v, const u16* sk, const u16* ssk, const u16* Dv,
                                                  const u16* w1, const u16* w2, u16* out, char* ws) {
  extern __shared__ __attribute__((aligned(1024))) char smem[];
  const int tid = threadIdx.x, b = blockIdx.x;
  const int xc = b & 7, i = b >> 3;
  const int rt = (xc & 1) * 8 + (i & 7), kq = xc >> 1, dn = i >> 3;
  G1Ctx c;
  c.lds = (LAS char*)smem;
  c.ws = ws;
  c.ssk = ssk;
  c.wr = mkrsrc(ws, WS_END);
  c.b = b; c.cb = b < 192 ? b : 191;
  c.w = __builtin_amdgcn_readfirstlane(tid >> 6); c.l = tid & 63;
  g1_phase(c, x, k, v, sk, w1, w2, rt, 4 * kq + dn, kq TSPASS);
  conv_g2(c, x, Dv, w2, out, rt, kq, dn TSPASS);
}

extern "C" void kernel_launch(void* const* d_in, const int* in_sizes, int n_in, void* d_out, int out_size, void* d_ws, size_t ws_size,
                              hipStream_t stream) {
  (void)in_sizes; (void)n_in; (void)out_size; (void)ws_size;
  (void)hipFuncSetAttribute((const void*)k_fused, hipFuncAttributeMaxDynamicSharedMemorySize, LDS_BYTES);
  k_fused<<<256, 512, LDS_BYTES, stream>>>((const u16*)d_in[0], (const u16*)d_in[1], (const u16*)d_in[2], (const u16*)d_in[3],
                                           (const u16*)d_in[4], (const u16*)d_in[5], (const u16*)d_in[7], (const u16*)d_in[8],
                                           (u16*)d_out, (char*)d_ws);
}
```

```cpp
#include <hip/hip_runtime.h>
#include <stdint.h>

typedef unsigned short u16;
typedef unsigned int u32;
typedef unsigned long long u64;
#define LAS __attribute__((address_space(3)))
#define CAS __attribute__((address_space(4)))
typedef __attribute__((ext_vector_type(8))) __bf16 bf16x8;
typedef __attribute__((ext_vector_type(2))) __bf16 bf16x2;
typedef __attribute__((ext_vector_type(2))) float f32x2;
typedef __attribute__((ext_vector_type(4))) float f32x4;
typedef __attribute__((ext_vector_type(16))) float f32x16;
typedef __attribute__((ext_vector_type(2))) u32 u32x2;
typedef __attribute__((ext_vector_type(4))) u32 u32x4;
typedef __attribute__((ext_vector_type(4))) short v4i16;

static constexpr int S_ = 2048, D_ = 576, F_ = 4608;
static constexpr u32 OFF_E = 0;
static constexpr u32 OFF_FK = 4096;
static constexpr u32 OFF_FH = OFF_FK + 2048;
static constexpr u32 OFF_FY = OFF_FH + 2048;
static constexpr u32 OFF_FX = OFF_FY + 2048;
static constexpr u32 OFF_STATS = 65536;
static constexpr u32 OFF_CS = OFF_STATS + 16384;
static constexpr u32 OFF_UR = 262144;
static constexpr u32 OFF_Y2 = OFF_UR + 576 * 4096;
static constexpr u32 OFF_H = OFF_Y2 + 576 * 4096;
static constexpr u32 OFF_P = OFF_H + 2048 * 4608 * 2;
static constexpr u32 WS_END = OFF_P + 256 * 3 * 9216;
static constexpr int LDS_BYTES = 163840;
static constexpr int SC = 16;
static constexpr u32 OOB = 0xF0000000u;
static constexpr u32 EMPTY = 0xFFFFFFFFu;
static constexpr u32 L_B1 = 73728, L_S1 = 147456, L_CSS = 159744, L_CS = 161792, L_ST = 162944, L_CNT = 163456;

#define TS(i)
#define TSARG
#define TSPASS

__device__ __forceinline__ float blo(u32 v) { return __uint_as_float(v << 16); }
__device__ __forceinline__ float bhi(u32 v) { return __uint_as_float(v & 0xffff0000u); }
__device__ __forceinline__ u32 pk(float lo, float hi) {
  f32x2 t = {lo, hi};
  return __builtin_bit_cast(u32, __builtin_convertvector(t, bf16x2));
}
__device__ __forceinline__ float rb(float x) { return blo(pk(x, x)); }
template <int C> __device__ __forceinline__ float dppf(float v) {
  return __int_as_float(__builtin_amdgcn_update_dpp(0, __float_as_int(v), C, 0xf, 0xf, false));
}
__device__ __forceinline__ float swp32add(float a, float b) {
  auto r = __builtin_amdgcn_permlane32_swap(__float_as_uint(a), __float_as_uint(b), false, false);
  return __uint_as_float(r[0]) + __uint_as_float(r[1]);
}
__device__ __forceinline__ float swp16add(float a, float b) {
  auto r = __builtin_amdgcn_permlane16_swap(__float_as_uint(a), __float_as_uint(b), false, false);
  return __uint_as_float(r[0]) + __uint_as_float(r[1]);
}
__device__ __forceinline__ float wave_sum(float v) {
  v += dppf<0x128>(v); v += dppf<0x124>(v); v += dppf<0x122>(v); v += dppf<0x121>(v);
  v = swp16add(v, v);
  return swp32add(v, v);
}
__device__ __forceinline__ void glds(const void* sbase, u32 voff, u32 ldsdst) {
  asm volatile("s_mov_b32 m0, %2\n\ts_nop 0\n\tglobal_load_lds_dwordx4 %0, %1" ::"v"(voff), "s"(sbase), "s"(ldsdst) : "memory");
}
__device__ __forceinline__ void blds(u32x4 srd, u32 voff, u32 ldsdst) {
  asm volatile("s_mov_b32 m0, %2\n\ts_nop 0\n\tbuffer_load_dwordx4 %0, %1, 0 offen lds" ::"v"(voff), "s"(srd), "s"(ldsdst) : "memory");
}
__device__ __forceinline__ void glds4(const void* sbase, u32 voff, u32 ldsdst) {
  asm volatile("s_mov_b32 m0, %2\n\ts_nop 0\n\tglobal_load_lds_dword %0, %1" ::"v"(voff), "s"(sbase), "s"(ldsdst) : "memory");
}
__device__ __forceinline__ void ldg16(u32x4& d, const void* sbase, u32 voff) {
  asm volatile("global_load_dwordx4 %0, %1, %2" : "=v"(d) : "v"(voff), "s"(sbase) : "memory");
}
__device__ __forceinline__ void ldg16s(u32x4& d, const void* sbase, u32 voff) {
  asm volatile("global_load_dwordx4 %0, %1, %2 sc1" : "=v"(d) : "v"(voff), "s"(sbase) : "memory");
}
__device__ __forceinline__ void ldg16so(u32x4& d, const void* sbase, u32 voff) {
  asm volatile("global_load_dwordx4 %0, %1, %2 offset:1024 sc1" : "=v"(d) : "v"(voff), "s"(sbase) : "memory");
}
__device__ __forceinline__ void ldg8(u32x2& d, const void* sbase, u32 voff) {
  asm volatile("global_load_dwordx2 %0, %1, %2" : "=v"(d) : "v"(voff), "s"(sbase) : "memory");
}
__device__ __forceinline__ void ldg8s(u32x2& d, const void* sbase, u32 voff) {
  asm volatile("global_load_dwordx2 %0, %1, %2 sc1" : "=v"(d) : "v"(voff), "s"(sbase) : "memory");
}
__device__ __forceinline__ void ldb8(u32x2& d, u32x4 srd, u32 voff) {
  asm volatile("buffer_load_dwordx2 %0, %1, %2, 0 offen" : "=v"(d) : "v"(voff), "s"(srd) : "memory");
}
__device__ __forceinline__ void ldb4s(u32& d, u32x4 srd, u32 voff) {
  asm volatile("buffer_load_dword %0, %1, %2, 0 offen sc1" : "=v"(d) : "v"(voff), "s"(srd) : "memory");
}
__device__ __forceinline__ void ldb2(u32& d, u32x4 srd, u32 voff) {
  asm volatile("buffer_load_ushort %0, %1, %2, 0 offen" : "=v"(d) : "v"(voff), "s"(srd) : "memory");
}
__device__ __forceinline__ void ldb16s(u32x4& d, u32x4 srd, u32 voff) {
  asm volatile("buffer_load_dwordx4 %0, %1, %2, 0 offen sc1" : "=v"(d) : "v"(voff), "s"(srd) : "memory");
}
__device__ __forceinline__ void ldb16(u32x4& d, u32x4 srd, u32 voff) {
  asm volatile("buffer_load_dwordx4 %0, %1, %2, 0 offen" : "=v"(d) : "v"(voff), "s"(srd) : "memory");
}
template <int N> __device__ __forceinline__ void vmwait() { asm volatile("s_waitcnt vmcnt(%0)" ::"n"(N) : "memory"); }
__device__ __forceinline__ void lgwait() { asm volatile("s_waitcnt lgkmcnt(0)" ::: "memory"); }
__device__ __forceinline__ void bar() { asm volatile("s_barrier" ::: "memory"); }
#define PIN(x) asm volatile("" : "+v"(x))
__device__ __forceinline__ void sbar() { __builtin_amdgcn_sched_barrier(0); }
__device__ __forceinline__ __amdgpu_buffer_rsrc_t mkrsrc(const void* p, u32 bytes) {
  return __builtin_amdgcn_make_buffer_rsrc((void*)p, 0, bytes, 0x00020000);
}
__device__ __forceinline__ u32x4 mksrd(const void* p, u32 bytes) {
  u64 b = (u64)p;
  return (u32x4){(u32)b, (u32)(b >> 32) & 0xffffu, bytes, 0x00020000u};
}
__device__ __forceinline__ f32x4 mfma16(u32x4 a, u32x4 b, f32x4 c) {
  return __builtin_amdgcn_mfma_f32_16x16x32_bf16(__builtin_bit_cast(bf16x8, a), __builtin_bit_cast(bf16x8, b), c, 0, 0, 0);
}
__device__ __forceinline__ f32x16 mfma32(u32x4 a, u32x4 b, f32x16 c) {
  return __builtin_amdgcn_mfma_f32_32x32x16_bf16(__builtin_bit_cast(bf16x8, a), __builtin_bit_cast(bf16x8, b), c, 0, 0, 0);
}
__device__ __forceinline__ u32x2 trread(LAS char* p) {
  return __builtin_bit_cast(u32x2, __builtin_amdgcn_ds_read_tr16_b64_v4i16((LAS v4i16*)p));
}
__device__ __forceinline__ void flag_set(char* ws, u32 off, u64 tag) {
  __hip_atomic_store((__attribute__((address_space(1))) u64*)(ws + off), tag, __ATOMIC_RELAXED, __HIP_MEMORY_SCOPE_AGENT);
}
__device__ __forceinline__ void flag_wait(char* ws, u32 off, bool act, u64 tag) {
#pragma unroll 1
  for (int spins = 0; spins < 4000000; spins++) {
    u64 f = __hip_atomic_load((__attribute__((address_space(1))) u64*)(ws + off), __ATOMIC_RELAXED, __HIP_MEMORY_SCOPE_AGENT);
    if (__all(f == tag || !act)) break;
    __builtin_amdgcn_s_sleep(1);
  }
  asm volatile("" ::: "memory");
}
__device__ __forceinline__ bool flag_ok(u32x2 f, bool act, u64 tag) {
  return __all(!act || (f[0] == (u32)tag && f[1] == (u32)(tag >> 32)));
}

__device__ __forceinline__ void lds_arrive(LAS char* p, int l) {
  if (l == 0) __hip_atomic_fetch_add((LAS u32*)p, 1u, __ATOMIC_RELAXED, __HIP_MEMORY_SCOPE_WORKGROUP);
}
__device__ __forceinline__ void lds_await(LAS char* p, u32 n) {
#pragma unroll 1
  for (int spins = 0; spins < 40000000; spins++) {
    u32 v = __builtin_amdgcn_readfirstlane(__hip_atomic_load((LAS u32*)p, __ATOMIC_RELAXED, __HIP_MEMORY_SCOPE_WORKGROUP));
    if (v >= n) break;
  }
  asm volatile("" ::: "memory");
}

static constexpr u32 L_KC = L_B1;
static constexpr u32 L_U = L_B1 + 12 * 4160;

struct G1Ctx {
  LAS char* lds;
  char* ws;
  const u16* ssk;
  __amdgpu_buffer_rsrc_t wr;
  float nm, cc;
  u64 tag;
  int b, cb, w, l;
};

template <int MODE>
__device__ __forceinline__ void g1_pair(const G1Ctx& c, const u32x4 (&xf)[36], f32x16& acc, const f32x16& pa, LAS char* a0, LAS char* n0,
                                        u32x2 (&ra)[3], u32x2 (&rb_)[3], f32x4 (&cs)[2], LAS char* csp, LAS char* csn, u32 hoff,
                                        const char* dsrc, u32 dvoff, u32 ddst, int pp, u32 kfoff, bool kfact) {
  LAS char* a1 = a0 + 18 * 2048;
  const int w = c.w, l = c.l;
  float X[16], E[16];
  u32 P[8];
  u32x2 pf, k0;
  u32x4 uq[2];
#pragma unroll
  for (int ks = 0; ks < 36; ks++) {
    if (ks + 2 < 36) {
      LAS char* ap = (ks + 2 < 18 ? a0 + (ks + 2) * 2048 : a1 + (ks + 2 - 18) * 2048);
      ra[(ks + 2) % 3] = trread(ap); rb_[(ks + 2) % 3] = trread(ap + 512);
    }
    if (MODE != 2 && ks == 35) { ra[1] = trread(n0 + 2048); rb_[1] = trread(n0 + 2048 + 512); }
    if (ks == 34) lgwait();
    u32x4 A = {ra[ks % 3][0], ra[ks % 3][1], rb_[ks % 3][0], rb_[ks % 3][1]};
    if (ks == 0) { f32x16 z = {}; acc = mfma32(A, xf[0], z); }
    else acc = mfma32(A, xf[ks], acc);
    if (ks == 34) {
      vmwait<1>();
      if (MODE == 0) {
        PIN(pf);
        if (pp == 1 && !flag_ok(pf, kfact, c.tag)) flag_wait(c.ws, kfoff, kfact, c.tag);
      }
      bar();
      if (MODE == 0 && pp == 0 && w == 0 && l == 0) flag_set(c.ws, OFF_FK + c.b * 8, c.tag);
      if (MODE != 2) { ra[0] = trread(n0); rb_[0] = trread(n0 + 512); }
    }
    if (ks % 8 == 6 && ks / 8 < 3) cs[(ks / 8 + 1) & 1] = *(LAS f32x4*)(csp + (ks / 8 + 1) * 32);
    if (ks % 2 == 1 && ks / 2 < 16) {
      int e = ks / 2;
      float xx = __builtin_fmaf(c.nm, cs[(e >> 2) & 1][e & 3], pa[e]);
      PIN(xx);
      float tt = xx * c.cc;
      PIN(tt);
      X[e] = xx; E[e] = tt;
    }
    if (ks % 2 == 0 && ks >= 2 && ks / 2 - 1 < 16) {
      int e = ks / 2 - 1;
      float tt = __builtin_amdgcn_exp2f(E[e]);
      PIN(tt);
      E[e] = tt;
    }
    if (ks % 2 == 1 && ks >= 3 && (ks - 3) / 2 < 16) {
      int e = (ks - 3) / 2;
      float tt = 1.0f + E[e];
      PIN(tt);
      tt = __builtin_amdgcn_rcpf(tt);
      PIN(tt);
      E[e] = tt;
    }
    if (ks % 2 == 0 && ks >= 4 && (ks - 4) / 2 < 16) {
      int e = (ks - 4) / 2;
      float hh = X[e] * E[e];
      PIN(hh);
      X[e] = hh;
      if (e & 1) { u32 pq = pk(X[e - 1], X[e]); PIN(pq); P[e >> 1] = pq; }
    }
    if (ks == 19) __builtin_amdgcn_raw_buffer_store_b128((u32x4){P[0], P[1], P[2], P[3]}, c.wr, hoff, 0, SC);
    if (ks == 35) {
      __builtin_amdgcn_raw_buffer_store_b128((u32x4){P[4], P[5], P[6], P[7]}, c.wr, hoff + 512, 0, SC);
      if (MODE != 2) cs[0] = *(LAS f32x4*)(csn);
    }
    if (MODE != 2) {
      if (ks % 2 == 0 && ks / 2 < 9) glds(dsrc + (size_t)(ks / 2) * (64 * 9216), dvoff, ddst + (ks / 2) * 8192);
      if (MODE == 0 && ks == 1) ldg8s(pf, c.ws, kfoff);
    } else {
      if (ks % 2 == 0 && ks / 2 < 6) {
        int idx = w * 6 + ks / 2, cc = idx >> 2, part = idx & 3;
        u32x4 srd = mksrd(c.ssk + (size_t)(c.cb * 3 + (cc >> 2)) * 2048, 4096u);
        blds(srd, (cc & 3) * 2 + part * 1024 + l * 16, L_KC + cc * 4160 + 32 + part * 1024);
      }
      if (ks == 12 || ks == 14) {
        int idx = (w < 6 ? w : 5) * 2 + (ks - 12) / 2;
        ldg16s(uq[(ks - 12) / 2], c.ws + OFF_UR + (size_t)(c.cb * 3 + (idx >> 2)) * 4096 + (idx & 3) * 1024, l * 16);
      }
      if (ks == 16) ldg8(k0, c.ssk + (size_t)c.cb * 3 * 2048, (l < 8 ? l >> 2 : 2) * 4096);
      if (ks == 3) {
        if (w == 6 && l < 12) *(LAS u32x4*)(c.lds + L_KC + l * 4160) = (u32x4){0, 0, 0, 0};
        if (w < 3 && l < 32) *(LAS u32x4*)(c.lds + L_U + w * 4608 + 4096 + l * 16) = (u32x4){0, 0, 0, 0};
      }
      if (ks == 30) {
        vmwait<1>();
        PIN(uq[0]); PIN(uq[1]); PIN(k0);
        if (w < 6) {
          int idx = w * 2;
          *(LAS u32x4*)(c.lds + L_U + (idx >> 2) * 4608 + (idx & 3) * 1024 + l * 16) = uq[0];
          *(LAS u32x4*)(c.lds + L_U + (idx >> 2) * 4608 + ((idx & 3) + 1) * 1024 + l * 16) = uq[1];
        }
        if (w == 6 && l < 12) {
          int cp = l & 3;
          u32x4 c1 = {0, 0, 0, 0};
          if (cp == 1) c1[3] = k0[0] << 16;
          if (cp == 2) c1[3] = k0[0];
          if (cp == 3) { c1[2] = k0[0] << 16; c1[3] = (k0[0] >> 16) | (k0[1] << 16); }
          *(LAS u32x4*)(c.lds + L_KC + l * 4160 + 16) = c1;
        }
      }
    }
    sbar();
  }
}

__device__ __forceinline__ void g1_epi_final(const G1Ctx& c, const f32x16& pa, u32 csoff, u32 hoff) {
  u32 P[8];
#pragma unroll
  for (int q = 0; q < 4; q++) {
    f32x4 cs = *(LAS f32x4*)(c.lds + csoff + q * 32);
    float h[4];
#pragma unroll
    for (int i = 0; i < 4; i++) {
      float X = __builtin_fmaf(c.nm, cs[i], pa[4 * q + i]);
      h[i] = X * __builtin_amdgcn_rcpf(1.0f + __builtin_amdgcn_exp2f(X * c.cc));
    }
    P[2 * q] = pk(h[0], h[1]); P[2 * q + 1] = pk(h[2], h[3]);
  }
  __builtin_amdgcn_raw_buffer_store_b128((u32x4){P[0], P[1], P[2], P[3]}, c.wr, hoff, 0, SC);
  __builtin_amdgcn_raw_buffer_store_b128((u32x4){P[4], P[5], P[6], P[7]}, c.wr, hoff + 512, 0, SC);
}

__device__ __forceinline__ void kv_compute(const u32x2 (&kr)[11], const u32x2 (&vr)[8], const u32x4 (&sq)[2],
                                           __amdgpu_buffer_rsrc_t wr, int ch, int t) {
#pragma unroll
  for (int q = 0; q < 4; q++) {
    u32 sa = sq[q >> 1][(q & 1) * 2], sb = sq[q >> 1][(q & 1) * 2 + 1];
    float w0 = blo(sa), w1 = bhi(sa), w2 = blo(sb), w3 = bhi(sb);
    float kk[11], o[8];
#pragma unroll
    for (int i = 0; i < 11; i++) kk[i] = (q & 1) ? bhi(kr[i][q >> 1]) : blo(kr[i][q >> 1]);
#pragma unroll
    for (int i = 0; i < 8; i++) {
      float vv = (q & 1) ? bhi(vr[i][q >> 1]) : blo(vr[i][q >> 1]);
      o[i] = (kk[i] * w3 + kk[i + 1] * w2 + kk[i + 2] * w1 + kk[i + 3] * w0) * vv;
    }
    u32x4 r = {pk(o[7], o[6]), pk(o[5], o[4]), pk(o[3], o[2]), pk(o[1], o[0])};
    __builtin_amdgcn_raw_buffer_store_b128(r, wr, OFF_UR + (ch + q) * 4096 + 2 * (2040 - t), 0, SC);
    sbar();
  }
}

__device__ __forceinline__ u32 ln_stats(const u32x4 (&sr)[2], int l) {
  float e[16];
#pragma unroll
  for (int i = 0; i < 8; i++) { e[2 * i] = blo(sr[i >> 2][i & 3]); e[2 * i + 1] = bhi(sr[i >> 2][i & 3]); }
  float s = 0;
#pragma unroll
  for (int i = 0; i < 16; i++) s += e[i];
  if (l >= 36) s = 0;
  float mean = wave_sum(s) * (1.0f / 576.0f);
  float vv = 0;
#pragma unroll
  for (int i = 0; i < 16; i++) { float d = e[i] - mean; vv += d * d; }
  if (l >= 36) vv = 0;
  float var = rb(wave_sum(vv) * (1.0f / 576.0f));
  float sd = rb(__builtin_sqrtf(rb(var + 1e-5f)));
  return pk(mean, sd);
}

__device__ __forceinline__ void g1_phase(G1Ctx& c, const u16* x, const u16* k, const u16* v, const u16* sk, const u16* w1, const u16* w2,
                                         int rt, int j, int kq TSARG) {
  LAS char* lds = c.lds;
  char* ws = c.ws;
  const int w = c.w, l = c.l, b = c.b, tid = w * 64 + l;
  const int tg = w & 3, fg = w >> 2, odd = j & 1, kh = l >> 5;
  const char* w1b = (const char*)w1 + j * 576;
  const char* sb = w1b + (odd ? 0 : 512);
  const char* pb = w1b + (odd ? 64 : 0);
  const u32 svo = (l >> 2) * 9216 + (l & 3) * 16;
  const bool kvw = w >= 5 && b < 192;
  const int kid = 3 * b + w - 5, kch = (kid >> 6) * 64 + (l & 15) * 4, kt = (kid & 63) * 32 + (l >> 4) * 8;
  if (tid == 0) *(LAS u32x2*)(lds + L_CNT) = (u32x2){0, 0};
  if (w < 4) {
    u32x2 ep;
    ldg8s(ep, ws + OFF_E, 0);
    const char* xb = (const char*)x + (size_t)rt * 128 * 1152;
#pragma unroll
    for (int h = 0; h < 4; h++) {
      u32 vo = (32 * w + 8 * h + (l >> 3)) * 1152 + (((l & 7) ^ (4 * (h & 1) + (l >> 4))) * 16);
#pragma unroll
      for (int kb = 0; kb < 9; kb++) glds(xb + kb * 128, vo, kb * 16384 + (32 * w + 8 * h) * 128);
    }
#pragma unroll
    for (int i = 0; i < 3; i++) glds(sb + (size_t)(w * 3 + i) * 16 * 9216, svo, L_S1 + (w * 3 + i) * 1024);
    {
      int n = ((b >> 3) & 7) * 120 + w * 30 + (l < 30 ? l : 29);
      u32 o = n < 384 ? (192 + n) * 9216 + (odd ? 0 : 512) : (n - 384) * 9216 + (odd ? 64 : 0);
      glds4(w1b, o, L_CSS + w * 256);
    }
    vmwait<1>();
    PIN(ep);
    c.tag = (((u64)(u32)__builtin_amdgcn_readfirstlane(ep[1]) << 32) | (u32)__builtin_amdgcn_readfirstlane(ep[0])) + 1;
  } else {
    const int pw = w - 4;
    u32x2 ep;
    ldg8s(ep, ws + OFF_E, 0);
    __builtin_amdgcn_s_setprio(3);
    u32x4 sr[2][2], cq[6];
    const int stok = rt * 128 + j * 8 + 2 * pw;
    {
      u32 o = stok * 1152 + (l < 36 ? l : 35) * 32;
      ldg16(sr[0][0], x, o); ldg16(sr[0][1], x, o + 16); ldg16(sr[1][0], x, o + 1152); ldg16(sr[1][1], x, o + 1168);
    }
    const int cuq = (b & 1) * 32 + (b >> 3);
    const int cu = cuq < 54 ? cuq : 53;
    const int ccl = 18 * kq + cu % 18, cpp = cu / 18;
    {
      u32 o = (cpp * 192 + (l >> 1)) * 9216 + ccl * 128 + pw * 32 + (l & 1) * 16;
#pragma unroll
      for (int i = 0; i < 6; i++) ldg16(cq[i], w1, o + i * 32 * 9216);
    }
    vmwait<6>();
    PIN(ep); PIN(sr[0][0]); PIN(sr[0][1]); PIN(sr[1][0]); PIN(sr[1][1]);
    c.tag = (((u64)(u32)__builtin_amdgcn_readfirstlane(ep[1]) << 32) | (u32)__builtin_amdgcn_readfirstlane(ep[0])) + 1;
    {
      u32 s0 = ln_stats(sr[0], l), s1 = ln_stats(sr[1], l);
      __builtin_amdgcn_raw_buffer_store_b64((u32x2){l ? s1 : s0, (u32)c.tag}, c.wr, l < 2 ? OFF_STATS + (stok + l) * 8 : OOB, 0, SC);
    }
    vmwait<1>();
#pragma unroll
    for (int i = 0; i < 6; i++) PIN(cq[i]);
    {
      float a[8];
#pragma unroll
      for (int i = 0; i < 4; i++) {
        a[2 * i] = (blo(cq[0][i]) + blo(cq[1][i])) + (blo(cq[2][i]) + blo(cq[3][i])) + (blo(cq[4][i]) + blo(cq[5][i]));
        a[2 * i + 1] = (bhi(cq[0][i]) + bhi(cq[1][i])) + (bhi(cq[2][i]) + bhi(cq[3][i])) + (bhi(cq[4][i]) + bhi(cq[5][i]));
      }
      float b0 = swp32add(a[0], a[4]), b1 = swp32add(a[1], a[5]), b2 = swp32add(a[2], a[6]), b3 = swp32add(a[3], a[7]);
      float c0 = swp16add(b0, b2), c1 = swp16add(b1, b3);
      c0 += dppf<0x128>(c0); c1 += dppf<0x128>(c1);
      c0 += dppf<0x124>(c0); c1 += dppf<0x124>(c1);
      c0 += dppf<0x122>(c0); c1 += dppf<0x122>(c1);
      u32 col = ccl * 64 + pw * 16 + (l & 1) * 8 + (l >> 5) * 4 + ((l >> 4) & 1) * 2;
      __builtin_amdgcn_raw_buffer_store_b128((u32x4){__float_as_uint(c0), (u32)c.tag, __float_as_uint(c1), (u32)c.tag}, c.wr,
                                             (cuq < 54 && (l & 14) == 0) ? OFF_CS + (cpp * F_ + col) * 8 : OOB, 0, SC);
    }
    __builtin_amdgcn_s_setprio(0);
  }
  lgwait(); bar();
  TS(1);
  u32x4 xf[36];
  auto xread = [&]() {
    u32 row = tg * 32 + (l & 31);
#pragma unroll
    for (int q = 0; q < 4; q++) {
      u32 a = row * 128 + (((2 * q + (l >> 5)) ^ ((row >> 1) & 7)) * 16);
#pragma unroll
      for (int kb = 0; kb < 9; kb++) xf[kb * 4 + q] = *(LAS u32x4*)(lds + a + kb * 16384);
    }
    lgwait(); bar();
    TS(2);
  };
  const u32 pvo = (l >> 3) * 9216 + (((l & 7) ^ (((l >> 4) & 1) << 2)) * 16);
  const char* pwp = pb + (size_t)w * 8 * 9216;
  f32x16 acc0 = {}, acc1 = {};
  if (w < 4) {
    xread();
#pragma unroll
    for (int i = 0; i < 6; i++) glds(sb + (size_t)(12 + w + 4 * i) * 16 * 9216, svo, (w + 4 * i) * 1024);
    u32 sa = (8 * kh + ((l >> 2) & 3)) * 64 + ((l >> 4) & 1) * 32 + (l & 3) * 8;
    LAS char* s0 = lds + L_S1 + sa;
    LAS char* s1 = lds + sa - 12 * 1024;
    u32x2 ra[3], rb_[3];
    ra[0] = trread(s0); rb_[0] = trread(s0 + 256);
    ra[1] = trread(s0 + 1024); rb_[1] = trread(s0 + 1024 + 256);
#pragma unroll
    for (int ks = 0; ks < 36; ks++) {
      if (ks < 9) {
        glds(pb + (size_t)(w + 8 * ks) * 8 * 9216, pvo, L_B1 + (w + 8 * ks) * 1024);
        glds(pb + (size_t)(w + 8 * ks + 4) * 8 * 9216, pvo, L_B1 + (w + 8 * ks + 4) * 1024);
      }
      if (ks == 10) {
        vmwait<18>();
        TS(17);
        lds_arrive(lds + L_CNT + 4, l);
        lds_await(lds + L_CNT + 4, 4);
      }
      if (ks + 2 < 36) {
        LAS char* ap = (ks + 2 < 12 ? s0 : s1) + (ks + 2) * 1024;
        ra[(ks + 2) % 3] = trread(ap); rb_[(ks + 2) % 3] = trread(ap + 256);
      }
      u32x4 A = {ra[ks % 3][0], ra[ks % 3][1], rb_[ks % 3][0], rb_[ks % 3][1]};
      acc1 = mfma32(A, xf[ks], acc1);
      sbar();
    }
    TS(18);
    vmwait<0>();
    TS(19);
  } else if (w == 4) {
    u32x4 srd = mksrd(ws, WS_END), sg, cg[3][3];
    const u32 tg32 = (u32)c.tag;
    const u32 so = OFF_STATS + rt * 1024 + l * 16, co = OFF_CS + j * 2304 + l * 16;
    auto sweep = [&]() {
      ldb16s(sg, srd, so);
#pragma unroll
      for (int p = 0; p < 3; p++) {
        ldb16s(cg[p][0], srd, co + p * F_ * 8); ldb16s(cg[p][1], srd, co + p * F_ * 8 + 1024);
        ldb16s(cg[p][2], srd, l < 16 ? co + p * F_ * 8 + 2048 : OOB);
      }
    };
    sweep();
    xread();
    TS(17);
#pragma unroll 1
    for (int spins = 0; spins < 1000000; spins++) {
      vmwait<0>();
      PIN(sg);
      bool ok = sg[1] == tg32 && sg[3] == tg32;
#pragma unroll
      for (int p = 0; p < 3; p++) {
        PIN(cg[p][0]); PIN(cg[p][1]); PIN(cg[p][2]);
        ok = ok && cg[p][0][1] == tg32 && cg[p][0][3] == tg32 && cg[p][1][1] == tg32 && cg[p][1][3] == tg32;
        ok = ok && (l >= 16 || (cg[p][2][1] == tg32 && cg[p][2][3] == tg32));
      }
      if (__all(ok)) break;
      sweep();
    }
    vmwait<0>();
    TS(18);
    *(LAS u32x2*)(lds + L_ST + l * 8) = (u32x2){sg[0], sg[2]};
#pragma unroll
    for (int i = 0; i < 3; i++) {
      float c0 = __uint_as_float(cg[0][i][0]) + __uint_as_float(cg[1][i][0]) + __uint_as_float(cg[2][i][0]);
      float c1 = __uint_as_float(cg[0][i][2]) + __uint_as_float(cg[1][i][2]) + __uint_as_float(cg[2][i][2]);
      *(LAS f32x2*)(lds + (i < 2 || l < 16 ? L_CS + i * 512 : L_CSS + 1024) + l * 8) = (f32x2){c0, c1};
    }
    {
      const char* kp = (const char*)c.ssk + (size_t)c.cb * 3 * 4096;
      glds4(kp, l * 128, L_CSS);
      glds4(kp, 8192 + (l & 31) * 128, L_CSS + 256);
      const char* wp = (const char*)w2 + (size_t)kq * 1327104 + (b >> 3) * 41472;
#pragma unroll
      for (int i = 0; i < 6; i++) glds4(wp, (i < 5 ? i * 64 + l : 320 + (l & 3)) * 128, L_CSS + 512);
    }
    TS(19);
  } else if (kvw) {
    u32x2 kr[11], vr[8];
    u32x4 sq[2];
    {
      u32x4 rk = mksrd(k, S_ * D_ * 2), rv = mksrd(v, S_ * D_ * 2), rs = mksrd(sk, D_ * 8);
      u32 o = kt * 1152 + kch * 2;
#pragma unroll
      for (int i = 0; i < 11; i++) ldb8(kr[i], rk, o + (i - 3) * 1152);
#pragma unroll
      for (int i = 0; i < 8; i++) ldb8(vr[i], rv, o + i * 1152);
      ldb16(sq[0], rs, kch * 8); ldb16(sq[1], rs, kch * 8 + 16);
    }
    xread();
    __builtin_amdgcn_s_setprio(2);
    vmwait<0>();
#pragma unroll
    for (int i = 0; i < 11; i++) PIN(kr[i]);
#pragma unroll
    for (int i = 0; i < 8; i++) PIN(vr[i]);
    PIN(sq[0]); PIN(sq[1]);
    TS(17);
    kv_compute(kr, vr, sq, c.wr, kch, kt);
    TS(18);
    __builtin_amdgcn_s_setprio(0);
  } else {
    xread();
  }
  lgwait(); bar();
  TS(3);
  {
    u32 st = *(LAS u32*)(lds + L_ST + (tg * 32 + (l & 31)) * 4);
    c.nm = -blo(st);
    c.cc = -1.44269504088896f / bhi(st);
  }
  const u32 abase = (8 * kh + ((l >> 2) & 3)) * 128 + ((fg * 64 + ((l >> 4) & 1) * 32 + (l & 3) * 8) ^ (((l >> 3) & 1) << 6));
  const u32 hbase = OFF_H + (rt * 144 + 9 * j) * 8192 + (2 * tg + ((l >> 4) & 1)) * 1024 + (kh * 16 + (l & 15)) * 16;
  LAS char* aB0 = lds + abase;
  LAS char* aB1 = lds + L_B1 + abase;
  LAS char* csb = lds + L_CS + kh * 16;
  int ptile = odd ? 0 : 8;
  u32 hoff = (w < 4) ? hbase + ptile * 8192 : OOB;
  const int klo = (((c.cb * 3) >> 6) * 64) / 3, khi = (((c.cb * 3 + 2) >> 6) * 64 + 63) / 3;
  const bool kfact = klo + l <= khi;
  const u32 kfoff = OFF_FK + (kfact ? klo + l : klo) * 8;
  u32x2 ra[3], rb_[3];
  f32x4 cs[2];
  ra[0] = trread(aB1); rb_[0] = trread(aB1 + 512);
  ra[1] = trread(aB1 + 2048); rb_[1] = trread(aB1 + 2048 + 512);
  cs[0] = *(LAS f32x4*)(csb + ptile * 128);
#pragma unroll 1
  for (int pp = 0; pp < 2; pp++) {
    int t0 = odd + 4 * pp + fg, t1 = t0 + 2;
    g1_pair<0>(c, xf, acc0, acc1, aB1, aB0, ra, rb_, cs, csb + ptile * 128, csb + t0 * 128, hoff,
               pwp + (2 * pp + 1) * 128, pvo, w * 1024, pp, kfoff, kfact);
    if (pp == 0) { TS(4); } else { TS(6); }
    hoff = hbase + t0 * 8192;
    ptile = t0;
    if (pp == 1) break;
    g1_pair<1>(c, xf, acc1, acc0, aB0, aB1, ra, rb_, cs, csb + t0 * 128, csb + t1 * 128, hoff,
               pwp + 2 * 128, pvo, L_B1 + w * 1024, pp, kfoff, kfact);
    TS(5);
    ptile = t1;
    hoff = hbase + t1 * 8192;
  }
  g1_pair<2>(c, xf, acc1, acc0, aB0, aB1, ra, rb_, cs, csb + ptile * 128, csb, hoff, pwp, pvo, 0, 1, kfoff, kfact);
  TS(7);
  ptile += 2;
  g1_epi_final(c, acc1, L_CS + ptile * 128 + kh * 16, hbase + ptile * 8192);
}

static constexpr u32 L_RED = L_B1;
static constexpr u32 L_EX = 73728;
static constexpr u32 L_OWN = 0;
static constexpr u32 L_Y = 18432;

template <int P>
__device__ __forceinline__ void w2_piece(const char* wsrc, const u32 (&cvo)[5], u32 ldst, int w, int l) {
  if (P < 4) glds(wsrc, cvo[P < 4 ? P : 0], ldst + (w * 4 + P) * 1024);
  else if (l < 32) glds(wsrc, cvo[4], ldst + (32 + (w >> 1)) * 1024 + (w & 1) * 512);
}
template <int P>
__device__ __forceinline__ void w2_piece10(const char* wsrc, const u32 (&cvo)[5], int w, int l) {
  w2_piece<P % 5>(wsrc + (P / 5) * 73728, cvo, (P / 5) * 36864, w, l);
}
template <bool GL>
__device__ __forceinline__ void conv_res(f32x4 (&acc)[8], u32x4 (&A)[8], u32x4 (&B)[8], LAS char* ap, LAS char* bp,
                                         const char* wsrc, const u32 (&cvo)[5], int w, int l) {
#pragma unroll
  for (int q = 0; q < 8; q++) {
#pragma unroll
    for (int i = 7 - q; i >= 0; i--) acc[i + q] = mfma16(A[i], B[q], acc[i + q]);
    B[q] = *(LAS u32x4*)(bp + 512 * (7 - q));
    u32x2 lo = *(LAS u32x2*)(ap + 512 * (7 - q)), hi = *(LAS u32x2*)(ap + 512 * (7 - q) + 8);
    A[7 - q] = (u32x4){lo[0], lo[1], hi[0], hi[1]};
    if (GL) {
      if (q == 0) { w2_piece10<0>(wsrc, cvo, w, l); w2_piece10<1>(wsrc, cvo, w, l); }
      if (q == 1) { w2_piece10<2>(wsrc, cvo, w, l); w2_piece10<3>(wsrc, cvo, w, l); }
      if (q == 2) w2_piece10<4>(wsrc, cvo, w, l);
      if (q == 3) w2_piece10<5>(wsrc, cvo, w, l);
      if (q == 4) w2_piece10<6>(wsrc, cvo, w, l);
      if (q == 5) w2_piece10<7>(wsrc, cvo, w, l);
      if (q == 6) w2_piece10<8>(wsrc, cvo, w, l);
      if (q == 7) w2_piece10<9>(wsrc, cvo, w, l);
    }
    sbar();
  }
}

struct G2Ctx {
  const char* wsrc;
  const char* hsrc;
  char* ws;
  LAS char* apA;
  LAS char* apB;
  u32x4 srd, srx;
  u64 tag;
  u32 cvo[5];
  u32 fyoff, yoff0, yoff1, xoff0, xoff2;
  u32 ho0, ho1;
  u32 clr[4];
  __amdgpu_buffer_rsrc_t wr;
  bool fyact;
  int w, l, b;
};
struct G2Tail { u32x2 pf; u32x4 fh[2], yq[2]; u32x2 xv[2]; u32 x8; };

template <int I>
__device__ __forceinline__ void g2_issue1(const char* wsrc, const char* hsrc, const u32 (&cvo)[5], u32 ldst, int w, int l, u32 ho0, u32 ho1,
                                          u32x4 (&hb)[4]) {
  if (I % 2 == 0) w2_piece<I / 2>(wsrc, cvo, ldst, w, l);
  if (I == 1) ldg16s(hb[0], hsrc, ho0);
  if (I == 3) ldg16s(hb[1], hsrc, ho1);
  if (I == 5) ldg16s(hb[2], hsrc + 8192, ho0);
  if (I == 7) ldg16s(hb[3], hsrc + 8192, ho1);
}
template <int IT, int SS>
__device__ __forceinline__ void g2_aread(const G2Ctx& c, u32x2& ra, u32x2& rb_) {
  LAS char* p = ((IT & 2) ? c.apB : c.apA) + (IT & 1) * 36864 + (SS / 9) * 9216 + (SS % 9) * 32;
  ra = trread(p); rb_ = trread(p + 2304);
}
template <int IT, int SS>
__device__ __forceinline__ void g2_step(const G2Ctx& c, G2Tail& t, u32x4 (&hb)[4][4], f32x4 (&acc)[2][9], u32x2 (&ra)[3], u32x2 (&rb_)[3]) {
  constexpr int u = SS / 9, n = SS % 9;
  const int w = c.w, l = c.l;
  if (SS + 2 < 18) g2_aread<IT, (SS + 2) % 18>(c, ra[(SS + 2) % 3], rb_[(SS + 2) % 3]);
  else if (SS == 17 && IT < 8) g2_aread<IT + 1, 1>(c, ra[1], rb_[1]);
  if (SS == 16) lgwait();
  u32x4 A = {ra[SS % 3][0], ra[SS % 3][1], rb_[SS % 3][0], rb_[SS % 3][1]};
  acc[0][n] = mfma16(A, hb[IT & 3][u * 2], acc[0][n]);
  acc[1][n] = mfma16(A, hb[IT & 3][u * 2 + 1], acc[1][n]);
  if (SS == 16 && IT < 8) {
    if (IT == 0) vmwait<4 + 3 + 5 + 8 + 2>();
    else if (IT + 3 < 9) vmwait<9 + 8 + (IT == 1 ? 3 : IT == 2 ? 2 : IT == 3 ? 1 : 0)>();
    else if (IT + 2 < 9) vmwait<9>();
    else vmwait<0>();
    bar();
    PIN(hb[(IT + 1) & 3][0]); PIN(hb[(IT + 1) & 3][1]); PIN(hb[(IT + 1) & 3][2]); PIN(hb[(IT + 1) & 3][3]);
    if (IT == 1 && c.b < 192 && w == 0 && l == 0) flag_set(c.ws, OFF_FY + c.b * 8, c.tag);
    if (IT == 4 && w == 0 && l == 0) flag_set(c.ws, OFF_FX + c.b * 8, c.tag);
    g2_aread<IT + 1, 0>(c, ra[0], rb_[0]);
  }
  if ((IT == 0 && (SS == 4 || SS == 12)) || ((IT == 1 || IT == 2) && SS == 8))
    __builtin_amdgcn_raw_buffer_store_b128((u32x4){EMPTY, EMPTY, EMPTY, EMPTY}, c.wr, c.clr[IT == 0 ? SS / 12 : IT + 1], 0, SC);
  if (IT + 3 < 9) {
    const char* ws_ = c.wsrc + (IT + 3) * 73728;
    const char* hs_ = c.hsrc + (IT + 3) * 16384;
    if (SS % 2 == 1 && SS < 16) g2_issue1<(SS % 2 == 1 ? SS / 2 : 0)>(ws_, hs_, c.cvo, ((IT + 3) & 3) * 36864, w, l, c.ho0, c.ho1, hb[(IT + 3) & 3]);
    if (SS == 17) g2_issue1<8>(ws_, hs_, c.cvo, ((IT + 3) & 3) * 36864, w, l, c.ho0, c.ho1, hb[(IT + 3) & 3]);
  }
  if (IT == 7 && SS == 17) {
    ldg8s(t.pf, c.ws, c.fyoff);
    u32 o = (c.b == 0 && w == 7) ? OFF_FH + l * 32 : OOB;
    ldb16s(t.fh[0], c.srd, o); ldb16s(t.fh[1], c.srd, o + 16);
  }
  if (IT == 8 && SS == 12) {
    vmwait<0>();
    PIN(t.pf); PIN(t.fh[0]); PIN(t.fh[1]);
    if (!flag_ok(t.pf, c.fyact, c.tag)) flag_wait(c.ws, c.fyoff, c.fyact, c.tag);
    if (c.b == 0 && w == 7) {
      const u32 lo = (u32)c.tag, hi = (u32)(c.tag >> 32);
      bool ok = t.fh[0][0] == lo && t.fh[0][1] == hi && t.fh[0][2] == lo && t.fh[0][3] == hi &&
                t.fh[1][0] == lo && t.fh[1][1] == hi && t.fh[1][2] == lo && t.fh[1][3] == hi;
      if (!__all(ok))
        for (int i = 0; i < 4; i++) flag_wait(c.ws, OFF_FH + (i * 64 + l) * 8, true, c.tag);
      if (l == 0) flag_set(c.ws, OFF_E, c.tag);
    }
    ldb16s(t.yq[0], c.srd, c.yoff0); ldb16s(t.yq[1], c.srd, c.yoff1);
    ldb8(t.xv[0], c.srx, c.xoff0); ldb8(t.xv[1], c.srx, c.xoff0 + 32); ldb2(t.x8, c.srx, c.xoff2);
  }
  sbar();
}
template <int IT, int SS>
__device__ __forceinline__ void g2_steps(const G2Ctx& c, G2Tail& t, u32x4 (&hb)[4][4], f32x4 (&acc)[2][9], u32x2 (&ra)[3], u32x2 (&rb_)[3]) {
  g2_step<IT, SS>(c, t, hb, acc, ra, rb_);
  if constexpr (SS < 17) g2_steps<IT, SS + 1>(c, t, hb, acc, ra, rb_);
  else if constexpr (IT < 8) g2_steps<IT + 1, 0>(c, t, hb, acc, ra, rb_);
}
__device__ __forceinline__ f32x4 rb4(f32x4 v) {
  u32 a = pk(v[0], v[1]), b = pk(v[2], v[3]);
  return (f32x4){blo(a), bhi(a), blo(b), bhi(b)};
}
__device__ __forceinline__ f32x4 gate_in(f32x4 x, u32x2 yv, float m, float rs) {
  f32x4 xn = rb4(rb4(x - m) * rs);
  return rb4(xn * (f32x4){blo(yv[0]), bhi(yv[0]), blo(yv[1]), bhi(yv[1])});
}
__device__ __forceinline__ void fin4(f32x4 s, f32x4 x, f32x4 sv, float rs, u16* op) {
  f32x4 o = rb4(s * rs), g;
#pragma unroll
  for (int e = 0; e < 4; e++) g[e] = o[e] * __builtin_amdgcn_rcpf(1.0f + __builtin_amdgcn_exp2f(o[e] * -1.44269504088896f));
  f32x4 res = x + rb4(sv * rb4(g));
  u32x2 r = {pk(res[0], res[1]), pk(res[2], res[3])};
  asm volatile("global_store_dwordx2 %0, %1, off sc0 sc1" ::"v"(op), "v"(r) : "memory");
}
__device__ __forceinline__ u32 umax(u32 a, u32 b) { return a > b ? a : b; }
__device__ __forceinline__ u32 pkw(float a, float b) { u32 v = pk(a, b); return v < EMPTY - 1 ? v : EMPTY - 1; }
#define PDELAY 16
__device__ __forceinline__ f32x4 up4(u32 a, u32 b) { return (f32x4){blo(a), bhi(a), blo(b), bhi(b)}; }

__device__ __forceinline__ void g2_tail(G1Ctx& g, const G2Ctx& c, G2Tail& t, f32x4 (&acc)[2][9], u16* out, int rt, int kq, int dn,
                                        const int l TSARG) {
  LAS char* lds = g.lds;
  const int w = g.w, b = g.b;
  const int tg = w & 3, kh = w >> 2, xbit = rt >> 3, i7 = rt & 7, lg = l >> 4, lb = l & 15;
  const int p = w & 3, tt = w >> 2;
  const int tok = rt * 128 + kq * 32 + tt * 16 + lb, d0 = dn * 144 + 4 * lg;
  const int t8 = tt * 16 + 4 * p + lg, tok8 = rt * 128 + kq * 32 + t8;
#pragma unroll
  for (int n = 0; n < 9; n++) *(LAS f32x4*)(lds + L_EX + w * 9216 + n * 1024 + l * 16) = acc[0][n];
  lgwait(); bar();
  f32x4 r[9];
#pragma unroll
  for (int n = 0; n < 9; n++) r[n] = acc[1][n] + *(LAS f32x4*)(lds + L_EX + (w ^ 4) * 9216 + n * 1024 + l * 16);
  {
    int ob = 2 * tg + xbit + 8 * (dn * 8 + i7);
    u32 po = tg != kq ? OFF_P + (ob * 3 + ((kq - tg - 1) & 3)) * 9216 + kh * 4608 : OOB;
#pragma unroll
    for (int q = 0; q < 4; q++)
      __builtin_amdgcn_raw_buffer_store_b128((u32x4){pkw(r[2 * q][0], r[2 * q][1]), pkw(r[2 * q][2], r[2 * q][3]),
                                                     pkw(r[2 * q + 1][0], r[2 * q + 1][1]), pkw(r[2 * q + 1][2], r[2 * q + 1][3])},
                                             g.wr, po + q * 1024 + l * 16, 0, SC);
    __builtin_amdgcn_raw_buffer_store_b64((u32x2){pkw(r[8][0], r[8][1]), pkw(r[8][2], r[8][3])}, g.wr, po + 4096 + l * 8, 0, SC);
    if (tg == kq) {
#pragma unroll
      for (int n = 0; n < 9; n++) *(LAS f32x4*)(lds + L_OWN + kh * 9216 + n * 1024 + l * 16) = r[n];
    }
  }
  vmwait<5>();
  PIN(t.yq[0]); PIN(t.yq[1]); PIN(t.xv[0]); PIN(t.xv[1]); PIN(t.x8);
  *(LAS u32x4*)(lds + L_Y + (w * 16 + (l >> 2)) * 96 + (l & 3) * 16) = t.yq[0];
  if (w == 0) *(LAS u32x4*)(lds + L_Y + (128 + (l >> 2)) * 96 + (l & 3) * 16) = t.yq[1];
  lgwait(); bar();
  TS(14);
  const u32 st = *(LAS u32*)(lds + L_ST + (kq * 32 + tt * 16 + lb) * 4), st8 = *(LAS u32*)(lds + L_ST + (kq * 32 + t8) * 4);
  LAS char* yp = lds + L_Y + (4 * lg + ((l >> 2) & 3)) * 96 + tt * 32 + (l & 3) * 8;
  LAS char* op = lds + L_OWN + tt * 9216;
  const u32x2 y0 = trread(yp + p * 32 * 96), y1 = trread(yp + (p * 32 + 16) * 96);
  const u32 y8 = *(LAS u16*)(lds + L_Y + (128 + lb) * 96 + t8 * 2);
  const int l8 = ((l & 15) >> 2) * 16 + 4 * p + (l >> 4);
  f32x4 s0 = *(LAS f32x4*)(op + 2 * p * 1024 + l * 16), s1 = *(LAS f32x4*)(op + (2 * p + 1) * 1024 + l * 16);
  float s8 = *(LAS float*)(op + 8 * 1024 + l8 * 16 + (l & 3) * 4);
  float rs = 1.0f / bhi(st), rs8 = 1.0f / bhi(st8);
  f32x4 xa = up4(t.xv[0][0], t.xv[0][1]), xb = up4(t.xv[1][0], t.xv[1][1]);
  float x8 = blo(t.x8);
  f32x4 va = gate_in(xa, y0, blo(st), rs), vb = gate_in(xb, y1, blo(st), rs);
  float v8 = rb(rb(rb(x8 - blo(st8)) * rs8) * blo(y8));
  PIN(va); PIN(vb); PIN(v8); PIN(s0); PIN(s1); PIN(s8); PIN(rs); PIN(rs8);
  u32x4 pq[3];
  u32 p8[3];
  {
    const u32 po = OFF_P + b * 3 * 9216 + tt * 4608;
    const u32 o16 = po + p * 1024 + l * 16, o8 = po + 4096 + l8 * 8 + (l & 2) * 2;
    auto sweep = [&]() {
#pragma unroll
      for (int s = 0; s < 3; s++) { ldb16s(pq[s], c.srd, o16 + s * 9216); ldb4s(p8[s], c.srd, o8 + s * 9216); }
    };
    if (PDELAY) __builtin_amdgcn_s_sleep(PDELAY);
    sweep();
#pragma unroll 1
    for (int spins = 0; spins < 1000000; spins++) {
      vmwait<0>();
      u32 mx = 0;
#pragma unroll
      for (int s = 0; s < 3; s++) {
        PIN(pq[s]); PIN(p8[s]);
        mx = umax(umax(mx, p8[s]), umax(umax(pq[s][0], pq[s][1]), umax(pq[s][2], pq[s][3])));
      }
      if (__all(mx != EMPTY)) break;
      sweep();
    }
    vmwait<0>();
#pragma unroll
    for (int s = 0; s < 3; s++) { PIN(pq[s]); PIN(p8[s]); }
  }
  TS(15);
  const u32 sh8 = (l & 1) ? 0 : 16;
#pragma unroll
  for (int s = 0; s < 3; s++) {
    s0 += up4(pq[s][0], pq[s][1]); s1 += up4(pq[s][2], pq[s][3]);
    s8 += __uint_as_float((p8[s] << sh8) & 0xffff0000u);
  }
  u16* o = out + tok * D_ + d0 + p * 32;
  fin4(s0, xa, va, rs, o);
  fin4(s1, xb, vb, rs, o + 16);
  {
    float oo = rb(s8 * rs8);
    float gg = rb(oo * __builtin_amdgcn_rcpf(1.0f + __builtin_amdgcn_exp2f(oo * -1.44269504088896f)));
    float res = x8 + rb(v8 * gg);
    asm volatile("global_store_short %0, %1, off sc0 sc1" ::"v"(out + tok8 * D_ + dn * 144 + 128 + lb), "v"(pk(res, res)) : "memory");
  }
}

__device__ __forceinline__ void conv_g2(G1Ctx& g, const u16* x, const u16* Dv, const u16* w2, u16* out, int rt, int kq, int dn TSARG) {
  LAS char* lds = g.lds;
  char* ws = g.ws;
  int l = g.l;
  asm volatile("" : "+v"(l));
  const int w = g.w, b = g.b, cb = g.cb;
  const int tg = w & 3, kh = w >> 2, xbit = rt >> 3, i7 = rt & 7, lg = l >> 4;
  G2Ctx c;
  c.ws = ws; c.tag = g.tag; c.w = w; c.l = l; c.b = b;
#pragma unroll
  for (int i = 0; i < 5; i++) {
    u32 idx = i < 4 ? (w * 4 + i) * 64 + l : (32 + (w >> 1)) * 64 + (w & 1) * 32 + (l & 31);
    u32 row = idx / 18, c16 = idx - row * 18;
    c.cvo[i] = ((row >> 6) * 576 + (row & 63)) * 1152 + c16 * 16;
  }
  c.wsrc = (const char*)w2 + (size_t)(1152 * kq) * 1152 + dn * 288;
  c.hsrc = ws + OFF_H + (size_t)((rt * 144 + 36 * kq + 18 * kh) * 8 + 2 * tg) * 1024;
  f32x4 ca[8];
#pragma unroll
  for (int i = 0; i < 8; i++) ca[i] = (f32x4){0, 0, 0, 0};
  const int ch = (w < 3) ? 0 : (w < 6) ? 1 : 2;
  const int r0 = (w == 0 || w == 4 || w == 6) ? 0 : (w == 1 || w == 5) ? 3 : (w == 7) ? 4 : 6;
  const int nr = (w >= 6) ? 4 : (w == 2 || w == 3) ? 2 : 3;
  const int lb = l & 15, cp = (lb + 1) & 3;
  LAS char* ap = lds + L_KC + (ch * 4 + cp) * 4160 + 2 * (lb + 1 - cp) + 16 * lg + 64 * r0;
  LAS char* bp = lds + L_U + ch * 4608 + 2 * (240 - 16 * lb + 8 * lg) + 64 * r0;
  u32x4 A[8], B[8];
  if (b < 192) {
#pragma unroll
    for (int i = 7; i >= 0; i--) {
      B[7 - i] = *(LAS u32x4*)(bp + 512 * i);
      u32x2 lo = *(LAS u32x2*)(ap + 512 * i), hi = *(LAS u32x2*)(ap + 512 * i + 8);
      A[i] = (u32x4){lo[0], lo[1], hi[0], hi[1]};
    }
    conv_res<true>(ca, A, B, ap + 64, bp + 64, c.wsrc, c.cvo, w, l);
  } else {
    w2_piece10<0>(c.wsrc, c.cvo, w, l); w2_piece10<1>(c.wsrc, c.cvo, w, l); w2_piece10<2>(c.wsrc, c.cvo, w, l);
    w2_piece10<3>(c.wsrc, c.cvo, w, l); w2_piece10<4>(c.wsrc, c.cvo, w, l); w2_piece10<5>(c.wsrc, c.cvo, w, l);
    w2_piece10<6>(c.wsrc, c.cvo, w, l); w2_piece10<7>(c.wsrc, c.cvo, w, l); w2_piece10<8>(c.wsrc, c.cvo, w, l);
    w2_piece10<9>(c.wsrc, c.cvo, w, l);
  }
  vmwait<10>();
  if (l == 0) {
    u32 old = __hip_atomic_fetch_add((LAS u32*)(lds + L_CNT), 1u, __ATOMIC_RELAXED, __HIP_MEMORY_SCOPE_WORKGROUP);
    if (old == 7) flag_set(ws, OFF_FH + b * 8, g.tag);
  }
  TS(8);
  if (b < 192) {
#pragma unroll 1
    for (int r = 1; r < nr; r++) conv_res<false>(ca, A, B, ap + 64 * (r + 1), bp + 64 * (r + 1), c.wsrc, c.cvo, w, l);
  }
  TS(9);
  u32x2 uu[3];
#pragma unroll
  for (int i = 0; i < 3; i++) {
    int u = w * 3 + i;
    uu[i] = *(LAS u32x2*)(lds + L_U + (u >> 3) * 4608 + 2 * (2044 - (256 * (u & 7) + 16 * lb + 4 * lg)));
  }
  const u32 fhoff = OFF_FH + ((b & 7) + 8 * ((l & 3) * 8 + i7)) * 8;
  u32x2 pf;
  ldg8s(pf, ws, fhoff);
  lgwait(); bar();
#pragma unroll
  for (int i = 0; i < 8; i++) *(LAS f32x4*)(lds + L_RED + w * 8192 + i * 1024 + l * 16) = ca[i];
  lgwait(); bar();
  TS(10);
  f32x4 ys[3];
#pragma unroll
  for (int i = 0; i < 3; i++) {
    int u = w * 3 + i, c2 = u >> 3;
    LAS char* rp = lds + L_RED + c2 * 3 * 8192 + (u & 7) * 1024 + l * 16;
    ys[i] = *(LAS f32x4*)(rp) + *(LAS f32x4*)(rp + 8192);
    if (c2 < 2) ys[i] += *(LAS f32x4*)(rp + 2 * 8192);
  }
  vmwait<0>();
  PIN(pf);
  if (!flag_ok(pf, true, g.tag)) flag_wait(ws, fhoff, true, g.tag);
  TS(11);
  u32x4 hb[4][4];
  c.ho0 = l * 16 + (kh ? 0 : 1024); c.ho1 = l * 16 + (kh ? 1024 : 0);
#pragma unroll
  for (int it = 0; it < 3; it++) {
    ldg16s(hb[it][0], c.hsrc + it * 16384, c.ho0); ldg16s(hb[it][1], c.hsrc + it * 16384, c.ho1);
    ldg16s(hb[it][2], c.hsrc + it * 16384 + 8192, c.ho0); ldg16s(hb[it][3], c.hsrc + it * 16384 + 8192, c.ho1);
  }
#pragma unroll
  for (int i = 0; i < 3; i++) {
    int u = w * 3 + i, chn = cb * 3 + (u >> 3);
    u32 dw = ((CAS const u32*)Dv)[chn >> 1];
    float dv = (chn & 1) ? bhi(dw) : blo(dw);
    float o0 = rb(ys[i][0]) + rb(bhi(uu[i][1]) * dv), o1 = rb(ys[i][1]) + rb(blo(uu[i][1]) * dv);
    float o2 = rb(ys[i][2]) + rb(bhi(uu[i][0]) * dv), o3 = rb(ys[i][3]) + rb(blo(uu[i][0]) * dv);
    u32 o = b < 192 ? OFF_Y2 + chn * 4096 + (256 * (u & 7) + 16 * lb + 4 * lg) * 2 : OOB;
    __builtin_amdgcn_raw_buffer_store_b64((u32x2){pk(o0, o1), pk(o2, o3)}, g.wr, o, 0, SC);
  }
  lgwait(); bar();
  w2_piece<0>(c.wsrc + 2 * 73728, c.cvo, 73728, w, l); w2_piece<1>(c.wsrc + 2 * 73728, c.cvo, 73728, w, l);
  w2_piece<2>(c.wsrc + 2 * 73728, c.cvo, 73728, w, l); w2_piece<3>(c.wsrc + 2 * 73728, c.cvo, 73728, w, l);
  w2_piece<4>(c.wsrc + 2 * 73728, c.cvo, 73728, w, l);
  c.apA = lds + (kh * 64 + 16 * (lg >> 1) + 4 * (lg & 1) + ((l >> 2) & 3)) * 288 + (l & 3) * 8;
  c.apB = c.apA + 73728;
  c.srd = mksrd(ws, WS_END);
  c.srx = mksrd(x, S_ * D_ * 2);
  const int p = w & 3, tt = w >> 2;
  const int tok = rt * 128 + kq * 32 + tt * 16 + lb, d0 = dn * 144 + 4 * lg;
  const int t8 = tt * 16 + 4 * p + lg, tok8 = rt * 128 + kq * 32 + t8;
  c.fyact = l < 51;
  c.fyoff = l < 48 ? OFF_FY + (48 * dn + l) * 8 : OFF_FX + (2 * ((kq + 1 + (l < 51 ? l - 48 : 0)) & 3) + xbit + 8 * (dn * 8 + i7)) * 8;
  c.wr = g.wr;
#pragma unroll
  for (int i = 0; i < 4; i++) {
    int n = w * 4 + i, s = n / 9;
    int ob = 2 * ((kq + 1 + s) & 3) + xbit + 8 * (dn * 8 + i7);
    c.clr[i] = n < 27 ? OFF_P + (ob * 3 + 2 - s) * 9216 + (n - 9 * s) * 1024 + l * 16 : OOB;
  }
  {
    u32 yb = OFF_Y2 + (dn * 144 + w * 16 + (l >> 2)) * 4096 + (rt * 128 + kq * 32) * 2 + (l & 3) * 16;
    c.yoff0 = yb;
    c.yoff1 = w == 0 ? yb + 128 * 4096 : OOB;
    c.xoff0 = tok * 1152 + (d0 + p * 32) * 2;
    c.xoff2 = tok8 * 1152 + (dn * 144 + 128 + lb) * 2;
  }
  f32x4 acc[2][9];
#pragma unroll
  for (int n = 0; n < 9; n++) { acc[0][n] = (f32x4){0, 0, 0, 0}; acc[1][n] = (f32x4){0, 0, 0, 0}; }
  G2Tail t;
  u32x2 ra[3], rb_[3];
  g2_aread<0, 0>(c, ra[0], rb_[0]); g2_aread<0, 1>(c, ra[1], rb_[1]);
  vmwait<8 + 3 + 5>();
  PIN(hb[0][0]); PIN(hb[0][1]); PIN(hb[0][2]); PIN(hb[0][3]);
  TS(12);
  g2_steps<0, 0>(c, t, hb, acc, ra, rb_);
  TS(13);
  int lz = l;
  asm volatile("" : "+v"(lz));
  g2_tail(g, c, t, acc, out, rt, kq, dn, lz TSPASS);
}

__global__ void __launch_bounds__(512, 2) k_fused(const u16* x, const u16* k, const u16* v, const u16* sk, const u16* ssk, const u16* Dv,
                                                  const u16* w1, const u16* w2, u16* out, char* ws) {
  extern __shared__ __attribute__((aligned(1024))) char smem[];
  const int tid = threadIdx.x, b = blockIdx.x;
  const int xc = b & 7, i = b >> 3;
  const int rt = (xc & 1) * 8 + (i & 7), kq = xc >> 1, dn = i >> 3;
  G1Ctx c;
  c.lds = (LAS char*)smem;
  c.ws = ws;
  c.ssk = ssk;
  c.wr = mkrsrc(ws, WS_END);
  c.b = b; c.cb = b < 192 ? b : 191;
  c.w = __builtin_amdgcn_readfirstlane(tid >> 6); c.l = tid & 63;
  g1_phase(c, x, k, v, sk, w1, w2, rt, 4 * kq + dn, kq TSPASS);
  conv_g2(c, x, Dv, w2, out, rt, kq, dn TSPASS);
}

extern "C" void kernel_launch(void* const* d_in, const int* in_sizes, int n_in, void* d_out, int out_size, void* d_ws, size_t ws_size,
                              hipStream_t stream) {
  (void)in_sizes; (void)n_in; (void)out_size; (void)ws_size;
  (void)hipFuncSetAttribute((const void*)k_fused, hipFuncAttributeMaxDynamicSharedMemorySize, LDS_BYTES);
  k_fused<<<256, 512, LDS_BYTES, stream>>>((const u16*)d_in[0], (const u16*)d_in[1], (const u16*)d_in[2], (const u16*)d_in[3],
                                           (const u16*)d_in[4], (const u16*)d_in[5], (const u16*)d_in[7], (const u16*)d_in[8],
                                           (u16*)d_out, (char*)d_ws);
}
```

```cpp
#include <hip/hip_runtime.h>
#include <stdint.h>

typedef unsigned short u16;
typedef unsigned int u32;
typedef unsigned long long u64;
#define LAS __attribute__((address_space(3)))
#define CAS __attribute__((address_space(4)))
typedef __attribute__((ext_vector_type(8))) __bf16 bf16x8;
typedef __attribute__((ext_vector_type(2))) __bf16 bf16x2;
typedef __attribute__((ext_vector_type(2))) float f32x2;
typedef __attribute__((ext_vector_type(4))) float f32x4;
typedef __attribute__((ext_vector_type(16))) float f32x16;
typedef __attribute__((ext_vector_type(2))) u32 u32x2;
typedef __attribute__((ext_vector_type(4))) u32 u32x4;
typedef __attribute__((ext_vector_type(4))) short v4i16;

static constexpr int S_ = 2048, D_ = 576, F_ = 4608;
static constexpr u32 OFF_E = 0;
static constexpr u32 OFF_FK = 4096;
static constexpr u32 OFF_FH = OFF_FK + 2048;
static constexpr u32 OFF_FY = OFF_FH + 2048;
static constexpr u32 OFF_FX = OFF_FY + 2048;
static constexpr u32 OFF_STATS = 65536;
static constexpr u32 OFF_CS = OFF_STATS + 16384;
static constexpr u32 OFF_UR = 262144;
static constexpr u32 OFF_Y2 = OFF_UR + 576 * 4096;
static constexpr u32 OFF_H = OFF_Y2 + 576 * 4096;
static constexpr u32 OFF_P = OFF_H + 2048 * 4608 * 2;
static constexpr u32 WS_END = OFF_P + 256 * 3 * 9216;
static constexpr int LDS_BYTES = 163840;
static constexpr int SC = 16;
static constexpr u32 OOB = 0xF0000000u;
static constexpr u32 EMPTY = 0xFFFFFFFFu;
static constexpr u32 L_B1 = 73728, L_S1 = 147456, L_CSS = 159744, L_CS = 161792, L_ST = 162944, L_CNT = 163456;

#define TS(i)
#define TSARG
#define TSPASS

__device__ __forceinline__ float blo(u32 v) { return __uint_as_float(v << 16); }
__device__ __forceinline__ float bhi(u32 v) { return __uint_as_float(v & 0xffff0000u); }
__device__ __forceinline__ u32 pk(float lo, float hi) {
  f32x2 t = {lo, hi};
  return __builtin_bit_cast(u32, __builtin_convertvector(t, bf16x2));
}
__device__ __forceinline__ float rb(float x) { return blo(pk(x, x)); }
template <int C> __device__ __forceinline__ float dppf(float v) {
  return __int_as_float(__builtin_amdgcn_update_dpp(0, __float_as_int(v), C, 0xf, 0xf, false));
}
__device__ __forceinline__ float swp32add(float a, float b) {
  auto r = __builtin_amdgcn_permlane32_swap(__float_as_uint(a), __float_as_uint(b), false, false);
  return __uint_as_float(r[0]) + __uint_as_float(r[1]);
}
__device__ __forceinline__ float swp16add(float a, float b) {
  auto r = __builtin_amdgcn_permlane16_swap(__float_as_uint(a), __float_as_uint(b), false, false);
  return __uint_as_float(r[0]) + __uint_as_float(r[1]);
}
__device__ __forceinline__ float wave_sum(float v) {
  v += dppf<0x128>(v); v += dppf<0x124>(v); v += dppf<0x122>(v); v += dppf<0x121>(v);
  v = swp16add(v, v);
  return swp32add(v, v);
}
__device__ __forceinline__ void glds(const void* sbase, u32 voff, u32 ldsdst) {
  asm volatile("s_mov_b32 m0, %2\n\ts_nop 0\n\tglobal_load_lds_dwordx4 %0, %1" ::"v"(voff), "s"(sbase), "s"(ldsdst) : "memory");
}
__device__ __forceinline__ void blds(u32x4 srd, u32 voff, u32 ldsdst) {
  asm volatile("s_mov_b32 m0, %2\n\ts_nop 0\n\tbuffer_load_dwordx4 %0, %1, 0 offen lds" ::"v"(voff), "s"(srd), "s"(ldsdst) : "memory");
}
__device__ __forceinline__ void glds4(const void* sbase, u32 voff, u32 ldsdst) {
  asm volatile("s_mov_b32 m0, %2\n\ts_nop 0\n\tglobal_load_lds_dword %0, %1" ::"v"(voff), "s"(sbase), "s"(ldsdst) : "memory");
}
__device__ __forceinline__ void ldg16(u32x4& d, const void* sbase, u32 voff) {
  asm volatile("global_load_dwordx4 %0, %1, %2" : "=v"(d) : "v"(voff), "s"(sbase) : "memory");
}
__device__ __forceinline__ void ldg16s(u32x4& d, const void* sbase, u32 voff) {
  asm volatile("global_load_dwordx4 %0, %1, %2 sc1" : "=v"(d) : "v"(voff), "s"(sbase) : "memory");
}
__device__ __forceinline__ void ldg16so(u32x4& d, const void* sbase, u32 voff) {
  asm volatile("global_load_dwordx4 %0, %1, %2 offset:1024 sc1" : "=v"(d) : "v"(voff), "s"(sbase) : "memory");
}
__device__ __forceinline__ void ldg4(u32& d, const void* sbase, u32 voff) {
  asm volatile("global_load_dword %0, %1, %2" : "=v"(d) : "v"(voff), "s"(sbase) : "memory");
}
__device__ __forceinline__ void ldg8(u32x2& d, const void* sbase, u32 voff) {
  asm volatile("global_load_dwordx2 %0, %1, %2" : "=v"(d) : "v"(voff), "s"(sbase) : "memory");
}
__device__ __forceinline__ void ldg8s(u32x2& d, const void* sbase, u32 voff) {
  asm volatile("global_load_dwordx2 %0, %1, %2 sc1" : "=v"(d) : "v"(voff), "s"(sbase) : "memory");
}
__device__ __forceinline__ void ldb8(u32x2& d, u32x4 srd, u32 voff) {
  asm volatile("buffer_load_dwordx2 %0, %1, %2, 0 offen" : "=v"(d) : "v"(voff), "s"(srd) : "memory");
}
__device__ __forceinline__ void ldb4s(u32& d, u32x4 srd, u32 voff) {
  asm volatile("buffer_load_dword %0, %1, %2, 0 offen sc1" : "=v"(d) : "v"(voff), "s"(srd) : "memory");
}
__device__ __forceinline__ void ldb2(u32& d, u32x4 srd, u32 voff) {
  asm volatile("buffer_load_ushort %0, %1, %2, 0 offen" : "=v"(d) : "v"(voff), "s"(srd) : "memory");
}
__device__ __forceinline__ void ldb16s(u32x4& d, u32x4 srd, u32 voff) {
  asm volatile("buffer_load_dwordx4 %0, %1, %2, 0 offen sc1" : "=v"(d) : "v"(voff), "s"(srd) : "memory");
}
__device__ __forceinline__ void ldb16(u32x4& d, u32x4 srd, u32 voff) {
  asm volatile("buffer_load_dwordx4 %0, %1, %2, 0 offen" : "=v"(d) : "v"(voff), "s"(srd) : "memory");
}
template <int N> __device__ __forceinline__ void vmwait() { asm volatile("s_waitcnt vmcnt(%0)" ::"n"(N) : "memory"); }
__device__ __forceinline__ void lgwait() { asm volatile("s_waitcnt lgkmcnt(0)" ::: "memory"); }
__device__ __forceinline__ void bar() { asm volatile("s_barrier" ::: "memory"); }
#define PIN(x) asm volatile("" : "+v"(x))
__device__ __forceinline__ void sbar() { __builtin_amdgcn_sched_barrier(0); }
__device__ __forceinline__ __amdgpu_buffer_rsrc_t mkrsrc(const void* p, u32 bytes) {
  return __builtin_amdgcn_make_buffer_rsrc((void*)p, 0, bytes, 0x00020000);
}
__device__ __forceinline__ u32x4 mksrd(const void* p, u32 bytes) {
  u64 b = (u64)p;
  return (u32x4){(u32)b, (u32)(b >> 32) & 0xffffu, bytes, 0x00020000u};
}
__device__ __forceinline__ f32x4 mfma16(u32x4 a, u32x4 b, f32x4 c) {
  return __builtin_amdgcn_mfma_f32_16x16x32_bf16(__builtin_bit_cast(bf16x8, a), __builtin_bit_cast(bf16x8, b), c, 0, 0, 0);
}
__device__ __forceinline__ f32x16 mfma32(u32x4 a, u32x4 b, f32x16 c) {
  return __builtin_amdgcn_mfma_f32_32x32x16_bf16(__builtin_bit_cast(bf16x8, a), __builtin_bit_cast(bf16x8, b), c, 0, 0, 0);
}
__device__ __forceinline__ u32x2 trread(LAS char* p) {
  return __builtin_bit_cast(u32x2, __builtin_amdgcn_ds_read_tr16_b64_v4i16((LAS v4i16*)p));
}
__device__ __forceinline__ void flag_set(char* ws, u32 off, u64 tag) {
  __hip_atomic_store((__attribute__((address_space(1))) u64*)(ws + off), tag, __ATOMIC_RELAXED, __HIP_MEMORY_SCOPE_AGENT);
}
__device__ __forceinline__ void flag_wait(char* ws, u32 off, bool act, u64 tag) {
#pragma unroll 1
  for (int spins = 0; spins < 4000000; spins++) {
    u64 f = __hip_atomic_load((__attribute__((address_space(1))) u64*)(ws + off), __ATOMIC_RELAXED, __HIP_MEMORY_SCOPE_AGENT);
    if (__all(f == tag || !act)) break;
    __builtin_amdgcn_s_sleep(1);
  }
  asm volatile("" ::: "memory");
}
__device__ __forceinline__ bool flag_ok(u32x2 f, bool act, u64 tag) {
  return __all(!act || (f[0] == (u32)tag && f[1] == (u32)(tag >> 32)));
}

__device__ __forceinline__ void lds_arrive(LAS char* p, int l) {
  if (l == 0) __hip_atomic_fetch_add((LAS u32*)p, 1u, __ATOMIC_RELAXED, __HIP_MEMORY_SCOPE_WORKGROUP);
}
__device__ __forceinline__ void lds_await(LAS char* p, u32 n) {
#pragma unroll 1
  for (int spins = 0; spins < 40000000; spins++) {
    u32 v = __builtin_amdgcn_readfirstlane(__hip_atomic_load((LAS u32*)p, __ATOMIC_RELAXED, __HIP_MEMORY_SCOPE_WORKGROUP));
    if (v >= n) break;
  }
  asm volatile("" ::: "memory");
}

static constexpr u32 L_KC = L_B1;
static constexpr u32 L_U = L_B1 + 12 * 4160;

struct G1Ctx {
  LAS char* lds;
  char* ws;
  const u16* ssk;
  __amdgpu_buffer_rsrc_t wr;
  float nm, cc;
  u64 tag;
  int b, cb, w, l;
};

template <int MODE>
__device__ __forceinline__ void g1_pair(const G1Ctx& c, const u32x4 (&xf)[36], f32x16& acc, const f32x16& pa, LAS char* a0, LAS char* n0,
                                        u32x2 (&ra)[3], u32x2 (&rb_)[3], f32x4 (&cs)[2], LAS char* csp, LAS char* csn, u32 hoff,
                                        const char* dsrc, u32 dvoff, u32 ddst, int pp, u32 kfoff, bool kfact) {
  LAS char* a1 = a0 + 18 * 2048;
  const int w = c.w, l = c.l;
  float X[16], E[16];
  u32 P[8];
  u32x2 pf, k0;
  u32x4 uq[2];
#pragma unroll
  for (int ks = 0; ks < 36; ks++) {
    if (ks + 2 < 36) {
      LAS char* ap = (ks + 2 < 18 ? a0 + (ks + 2) * 2048 : a1 + (ks + 2 - 18) * 2048);
      ra[(ks + 2) % 3] = trread(ap); rb_[(ks + 2) % 3] = trread(ap + 512);
    }
    if (MODE != 2 && ks == 35) { ra[1] = trread(n0 + 2048); rb_[1] = trread(n0 + 2048 + 512); }
    if (ks == 34) lgwait();
    u32x4 A = {ra[ks % 3][0], ra[ks % 3][1], rb_[ks % 3][0], rb_[ks % 3][1]};
    if (ks == 0) { f32x16 z = {}; acc = mfma32(A, xf[0], z); }
    else acc = mfma32(A, xf[ks], acc);
    if (ks == 34) {
      vmwait<1>();
      if (MODE == 0) {
        PIN(pf);
        if (pp == 1 && !flag_ok(pf, kfact, c.tag)) flag_wait(c.ws, kfoff, kfact, c.tag);
      }
      bar();
      if (MODE == 0 && pp == 0 && w == 0 && l == 0) flag_set(c.ws, OFF_FK + c.b * 8, c.tag);
      if (MODE != 2) { ra[0] = trread(n0); rb_[0] = trread(n0 + 512); }
    }
    if (ks % 8 == 6 && ks / 8 < 3) cs[(ks / 8 + 1) & 1] = *(LAS f32x4*)(csp + (ks / 8 + 1) * 32);
    if (ks % 2 == 1 && ks / 2 < 16) {
      int e = ks / 2;
      float xx = __builtin_fmaf(c.nm, cs[(e >> 2) & 1][e & 3], pa[e]);
      PIN(xx);
      float tt = xx * c.cc;
      PIN(tt);
      X[e] = xx; E[e] = tt;
    }
    if (ks % 2 == 0 && ks >= 2 && ks / 2 - 1 < 16) {
      int e = ks / 2 - 1;
      float tt = __builtin_amdgcn_exp2f(E[e]);
      PIN(tt);
      E[e] = tt;
    }
    if (ks % 2 == 1 && ks >= 3 && (ks - 3) / 2 < 16) {
      int e = (ks - 3) / 2;
      float tt = 1.0f + E[e];
      PIN(tt);
      tt = __builtin_amdgcn_rcpf(tt);
      PIN(tt);
      E[e] = tt;
    }
    if (ks % 2 == 0 && ks >= 4 && (ks - 4) / 2 < 16) {
      int e = (ks - 4) / 2;
      float hh = X[e] * E[e];
      PIN(hh);
      X[e] = hh;
      if (e & 1) { u32 pq = pk(X[e - 1], X[e]); PIN(pq); P[e >> 1] = pq; }
    }
    if (ks == 19) __builtin_amdgcn_raw_buffer_store_b128((u32x4){P[0], P[1], P[2], P[3]}, c.wr, hoff, 0, SC);
    if (ks == 35) {
      __builtin_amdgcn_raw_buffer_store_b128((u32x4){P[4], P[5], P[6], P[7]}, c.wr, hoff + 512, 0, SC);
      if (MODE != 2) cs[0] = *(LAS f32x4*)(csn);
    }
    if (MODE != 2) {
      if (ks % 2 == 0 && ks / 2 < 9) glds(dsrc + (size_t)(ks / 2) * (64 * 9216), dvoff, ddst + (ks / 2) * 8192);
      if (MODE == 0 && ks == 1) ldg8s(pf, c.ws, kfoff);
    } else {
      if (ks % 2 == 0 && ks / 2 < 6) {
        int idx = w * 6 + ks / 2, cc = idx >> 2, part = idx & 3;
        u32x4 srd = mksrd(c.ssk + (size_t)(c.cb * 3 + (cc >> 2)) * 2048, 4096u);
        blds(srd, (cc & 3) * 2 + part * 1024 + l * 16, L_KC + cc * 4160 + 32 + part * 1024);
      }
      if (ks == 12 || ks == 14) {
        int idx = (w < 6 ? w : 5) * 2 + (ks - 12) / 2;
        ldg16s(uq[(ks - 12) / 2], c.ws + OFF_UR + (size_t)(c.cb * 3 + (idx >> 2)) * 4096 + (idx & 3) * 1024, l * 16);
      }
      if (ks == 16) ldg8(k0, c.ssk + (size_t)c.cb * 3 * 2048, (l < 8 ? l >> 2 : 2) * 4096);
      if (ks == 3) {
        if (w == 6 && l < 12) *(LAS u32x4*)(c.lds + L_KC + l * 4160) = (u32x4){0, 0, 0, 0};
        if (w < 3 && l < 32) *(LAS u32x4*)(c.lds + L_U + w * 4608 + 4096 + l * 16) = (u32x4){0, 0, 0, 0};
      }
      if (ks == 30) {
        vmwait<1>();
        PIN(uq[0]); PIN(uq[1]); PIN(k0);
        if (w < 6) {
          int idx = w * 2;
          *(LAS u32x4*)(c.lds + L_U + (idx >> 2) * 4608 + (idx & 3) * 1024 + l * 16) = uq[0];
          *(LAS u32x4*)(c.lds + L_U + (idx >> 2) * 4608 + ((idx & 3) + 1) * 1024 + l * 16) = uq[1];
        }
        if (w == 6 && l < 12) {
          int cp = l & 3;
          u32x4 c1 = {0, 0, 0, 0};
          if (cp == 1) c1[3] = k0[0] << 16;
          if (cp == 2) c1[3] = k0[0];
          if (cp == 3) { c1[2] = k0[0] << 16; c1[3] = (k0[0] >> 16) | (k0[1] << 16); }
          *(LAS u32x4*)(c.lds + L_KC + l * 4160 + 16) = c1;
        }
      }
    }
    sbar();
  }
}

__device__ __forceinline__ void g1_epi_final(const G1Ctx& c, const f32x16& pa, u32 csoff, u32 hoff) {
  u32 P[8];
#pragma unroll
  for (int q = 0; q < 4; q++) {
    f32x4 cs = *(LAS f32x4*)(c.lds + csoff + q * 32);
    float h[4];
#pragma unroll
    for (int i = 0; i < 4; i++) {
      float X = __builtin_fmaf(c.nm, cs[i], pa[4 * q + i]);
      h[i] = X * __builtin_amdgcn_rcpf(1.0f + __builtin_amdgcn_exp2f(X * c.cc));
    }
    P[2 * q] = pk(h[0], h[1]); P[2 * q + 1] = pk(h[2], h[3]);
  }
  __builtin_amdgcn_raw_buffer_store_b128((u32x4){P[0], P[1], P[2], P[3]}, c.wr, hoff, 0, SC);
  __builtin_amdgcn_raw_buffer_store_b128((u32x4){P[4], P[5], P[6], P[7]}, c.wr, hoff + 512, 0, SC);
}

__device__ __forceinline__ void kv_compute(const u32x2 (&kr)[11], const u32x2 (&vr)[8], const u32x4 (&sq)[2],
                                           __amdgpu_buffer_rsrc_t wr, int ch, int t) {
#pragma unroll
  for (int q = 0; q < 4; q++) {
    u32 sa = sq[q >> 1][(q & 1) * 2], sb = sq[q >> 1][(q & 1) * 2 + 1];
    float w0 = blo(sa), w1 = bhi(sa), w2 = blo(sb), w3 = bhi(sb);
    float kk[11], o[8];
#pragma unroll
    for (int i = 0; i < 11; i++) kk[i] = (q & 1) ? bhi(kr[i][q >> 1]) : blo(kr[i][q >> 1]);
#pragma unroll
    for (int i = 0; i < 8; i++) {
      float vv = (q & 1) ? bhi(vr[i][q >> 1]) : blo(vr[i][q >> 1]);
      o[i] = (kk[i] * w3 + kk[i + 1] * w2 + kk[i + 2] * w1 + kk[i + 3] * w0) * vv;
    }
    u32x4 r = {pk(o[7], o[6]), pk(o[5], o[4]), pk(o[3], o[2]), pk(o[1], o[0])};
    __builtin_amdgcn_raw_buffer_store_b128(r, wr, OFF_UR + (ch + q) * 4096 + 2 * (2040 - t), 0, SC);
    sbar();
  }
}

__device__ __forceinline__ u32 ln_stats(const u32x4 (&sr)[2], int l) {
  float e[16];
#pragma unroll
  for (int i = 0; i < 8; i++) { e[2 * i] = blo(sr[i >> 2][i & 3]); e[2 * i + 1] = bhi(sr[i >> 2][i & 3]); }
  float s = 0;
#pragma unroll
  for (int i = 0; i < 16; i++) s += e[i];
  if (l >= 36) s = 0;
  float mean = wave_sum(s) * (1.0f / 576.0f);
  float vv = 0;
#pragma unroll
  for (int i = 0; i < 16; i++) { float d = e[i] - mean; vv += d * d; }
  if (l >= 36) vv = 0;
  float var = rb(wave_sum(vv) * (1.0f / 576.0f));
  float sd = rb(__builtin_sqrtf(rb(var + 1e-5f)));
  return pk(mean, sd);
}

__device__ __forceinline__ void g1_phase(G1Ctx& c, const u16* x, const u16* k, const u16* v, const u16* sk, const u16* w1, const u16* w2,
                                         int rt, int j, int kq TSARG) {
  LAS char* lds = c.lds;
  char* ws = c.ws;
  const int w = c.w, l = c.l, b = c.b, tid = w * 64 + l;
  const int tg = w & 3, fg = w >> 2, odd = j & 1, kh = l >> 5;
  const char* w1b = (const char*)w1 + j * 576;
  const char* sb = w1b + (odd ? 0 : 512);
  const char* pb = w1b + (odd ? 64 : 0);
  const u32 svo = (l >> 2) * 9216 + (l & 3) * 16;
  const bool kvw = w >= 5 && b < 192;
  const int kid = 3 * b + w - 5, kch = (kid >> 6) * 64 + (l & 15) * 4, kt = (kid & 63) * 32 + (l >> 4) * 8;
  if (tid == 0) *(LAS u32x2*)(lds + L_CNT) = (u32x2){0, 0};
  if (w < 4) {
    u32x2 ep;
    ldg8s(ep, ws + OFF_E, 0);
    const char* xb = (const char*)x + (size_t)rt * 128 * 1152;
#pragma unroll
    for (int h = 0; h < 4; h++) {
      u32 vo = (32 * w + 8 * h + (l >> 3)) * 1152 + (((l & 7) ^ (4 * (h & 1) + (l >> 4))) * 16);
#pragma unroll
      for (int kb = 0; kb < 9; kb++) glds(xb + kb * 128, vo, kb * 16384 + (32 * w + 8 * h) * 128);
    }
#pragma unroll
    for (int i = 0; i < 3; i++) glds(sb + (size_t)(w * 3 + i) * 16 * 9216, svo, L_S1 + (w * 3 + i) * 1024);
    {
      int n = ((b >> 3) & 7) * 120 + w * 30 + (l < 30 ? l : 29);
      u32 o = n < 384 ? (192 + n) * 9216 + (odd ? 0 : 512) : (n - 384) * 9216 + (odd ? 64 : 0);
      glds4(w1b, o, L_CSS + w * 256);
    }
    vmwait<1>();
    PIN(ep);
    c.tag = (((u64)(u32)__builtin_amdgcn_readfirstlane(ep[1]) << 32) | (u32)__builtin_amdgcn_readfirstlane(ep[0])) + 1;
  } else {
    const int pw = w - 4;
    u32x2 ep;
    ldg8s(ep, ws + OFF_E, 0);
    __builtin_amdgcn_s_setprio(3);
    u32x4 sr[2][2], cq[6];
    const int stok = rt * 128 + j * 8 + 2 * pw;
    {
      u32 o = stok * 1152 + (l < 36 ? l : 35) * 32;
      ldg16(sr[0][0], x, o); ldg16(sr[0][1], x, o + 16); ldg16(sr[1][0], x, o + 1152); ldg16(sr[1][1], x, o + 1168);
    }
    const int cuq = (b & 1) * 32 + (b >> 3);
    const int cu = cuq < 54 ? cuq : 53;
    const int ccl = 18 * kq + cu % 18, cpp = cu / 18;
    {
      u32 o = (cpp * 192 + (l >> 1)) * 9216 + ccl * 128 + pw * 32 + (l & 1) * 16;
#pragma unroll
      for (int i = 0; i < 6; i++) ldg16(cq[i], w1, o + i * 32 * 9216);
    }
    vmwait<6>();
    PIN(ep); PIN(sr[0][0]); PIN(sr[0][1]); PIN(sr[1][0]); PIN(sr[1][1]);
    c.tag = (((u64)(u32)__builtin_amdgcn_readfirstlane(ep[1]) << 32) | (u32)__builtin_amdgcn_readfirstlane(ep[0])) + 1;
    {
      u32 s0 = ln_stats(sr[0], l), s1 = ln_stats(sr[1], l);
      __builtin_amdgcn_raw_buffer_store_b64((u32x2){l ? s1 : s0, (u32)c.tag}, c.wr, l < 2 ? OFF_STATS + (stok + l) * 8 : OOB, 0, SC);
    }
    vmwait<1>();
#pragma unroll
    for (int i = 0; i < 6; i++) PIN(cq[i]);
    {
      float a[8];
#pragma unroll
      for (int i = 0; i < 4; i++) {
        a[2 * i] = (blo(cq[0][i]) + blo(cq[1][i])) + (blo(cq[2][i]) + blo(cq[3][i])) + (blo(cq[4][i]) + blo(cq[5][i]));
        a[2 * i + 1] = (bhi(cq[0][i]) + bhi(cq[1][i])) + (bhi(cq[2][i]) + bhi(cq[3][i])) + (bhi(cq[4][i]) + bhi(cq[5][i]));
      }
      float b0 = swp32add(a[0], a[4]), b1 = swp32add(a[1], a[5]), b2 = swp32add(a[2], a[6]), b3 = swp32add(a[3], a[7]);
      float c0 = swp16add(b0, b2), c1 = swp16add(b1, b3);
      c0 += dppf<0x128>(c0); c1 += dppf<0x128>(c1);
      c0 += dppf<0x124>(c0); c1 += dppf<0x124>(c1);
      c0 += dppf<0x122>(c0); c1 += dppf<0x122>(c1);
      u32 col = ccl * 64 + pw * 16 + (l & 1) * 8 + (l >> 5) * 4 + ((l >> 4) & 1) * 2;
      __builtin_amdgcn_raw_buffer_store_b128((u32x4){__float_as_uint(c0), (u32)c.tag, __float_as_uint(c1), (u32)c.tag}, c.wr,
                                             (cuq < 54 && (l & 14) == 0) ? OFF_CS + (cpp * F_ + col) * 8 : OOB, 0, SC);
    }
    __builtin_amdgcn_s_setprio(0);
  }
  lgwait(); bar();
  TS(1);
  u32x4 xf[36];
  auto xread = [&]() {
    u32 row = tg * 32 + (l & 31);
#pragma unroll
    for (int q = 0; q < 4; q++) {
      u32 a = row * 128 + (((2 * q + (l >> 5)) ^ ((row >> 1) & 7)) * 16);
#pragma unroll
      for (int kb = 0; kb < 9; kb++) xf[kb * 4 + q] = *(LAS u32x4*)(lds + a + kb * 16384);
    }
    lgwait(); bar();
    TS(2);
  };
  const u32 pvo = (l >> 3) * 9216 + (((l & 7) ^ (((l >> 4) & 1) << 2)) * 16);
  const char* pwp = pb + (size_t)w * 8 * 9216;
  f32x16 acc0 = {}, acc1 = {};
  if (w < 4) {
    xread();
#pragma unroll
    for (int i = 0; i < 6; i++) glds(sb + (size_t)(12 + w + 4 * i) * 16 * 9216, svo, (w + 4 * i) * 1024);
    u32 sa = (8 * kh + ((l >> 2) & 3)) * 64 + ((l >> 4) & 1) * 32 + (l & 3) * 8;
    LAS char* s0 = lds + L_S1 + sa;
    LAS char* s1 = lds + sa - 12 * 1024;
    u32x2 ra[3], rb_[3];
    ra[0] = trread(s0); rb_[0] = trread(s0 + 256);
    ra[1] = trread(s0 + 1024); rb_[1] = trread(s0 + 1024 + 256);
#pragma unroll
    for (int ks = 0; ks < 36; ks++) {
      if (ks < 9) {
        glds(pb + (size_t)(w + 8 * ks) * 8 * 9216, pvo, L_B1 + (w + 8 * ks) * 1024);
        glds(pb + (size_t)(w + 8 * ks + 4) * 8 * 9216, pvo, L_B1 + (w + 8 * ks + 4) * 1024);
      }
      if (ks == 10) {
        vmwait<18>();
        TS(17);
        lds_arrive(lds + L_CNT + 4, l);
        lds_await(lds + L_CNT + 4, 4);
      }
      if (ks + 2 < 36) {
        LAS char* ap = (ks + 2 < 12 ? s0 : s1) + (ks + 2) * 1024;
        ra[(ks + 2) % 3] = trread(ap); rb_[(ks + 2) % 3] = trread(ap + 256);
      }
      u32x4 A = {ra[ks % 3][0], ra[ks % 3][1], rb_[ks % 3][0], rb_[ks % 3][1]};
      acc1 = mfma32(A, xf[ks], acc1);
      sbar();
    }
    TS(18);
    vmwait<0>();
    TS(19);
  } else if (w == 4) {
    u32x4 srd = mksrd(ws, WS_END), sg, cg[3][3];
    const u32 tg32 = (u32)c.tag;
    const u32 so = OFF_STATS + rt * 1024 + l * 16, co = OFF_CS + j * 2304 + l * 16;
    auto sweep = [&]() {
      ldb16s(sg, srd, so);
#pragma unroll
      for (int p = 0; p < 3; p++) {
        ldb16s(cg[p][0], srd, co + p * F_ * 8); ldb16s(cg[p][1], srd, co + p * F_ * 8 + 1024);
        ldb16s(cg[p][2], srd, l < 16 ? co + p * F_ * 8 + 2048 : OOB);
      }
    };
    sweep();
    xread();
    TS(17);
#pragma unroll 1
    for (int spins = 0; spins < 1000000; spins++) {
      vmwait<0>();
      PIN(sg);
      bool ok = sg[1] == tg32 && sg[3] == tg32;
#pragma unroll
      for (int p = 0; p < 3; p++) {
        PIN(cg[p][0]); PIN(cg[p][1]); PIN(cg[p][2]);
        ok = ok && cg[p][0][1] == tg32 && cg[p][0][3] == tg32 && cg[p][1][1] == tg32 && cg[p][1][3] == tg32;
        ok = ok && (l >= 16 || (cg[p][2][1] == tg32 && cg[p][2][3] == tg32));
      }
      if (__all(ok)) break;
      sweep();
    }
    vmwait<0>();
    TS(18);
    *(LAS u32x2*)(lds + L_ST + l * 8) = (u32x2){sg[0], sg[2]};
#pragma unroll
    for (int i = 0; i < 3; i++) {
      float c0 = __uint_as_float(cg[0][i][0]) + __uint_as_float(cg[1][i][0]) + __uint_as_float(cg[2][i][0]);
      float c1 = __uint_as_float(cg[0][i][2]) + __uint_as_float(cg[1][i][2]) + __uint_as_float(cg[2][i][2]);
      *(LAS f32x2*)(lds + (i < 2 || l < 16 ? L_CS + i * 512 : L_CSS + 1024) + l * 8) = (f32x2){c0, c1};
    }
    {
      const char* kp = (const char*)c.ssk + (size_t)c.cb * 3 * 4096;
      glds4(kp, l * 128, L_CSS);
      glds4(kp, 8192 + (l & 31) * 128, L_CSS + 256);
      const char* wp = (const char*)w2 + (size_t)kq * 1327104 + (b >> 3) * 41472;
#pragma unroll
      for (int i = 0; i < 6; i++) glds4(wp, (i < 5 ? i * 64 + l : 320 + (l & 3)) * 128, L_CSS + 512);
    }
    TS(19);
  } else if (kvw) {
    u32x2 kr[11], vr[8];
    u32x4 sq[2];
    {
      u32x4 rk = mksrd(k, S_ * D_ * 2), rv = mksrd(v, S_ * D_ * 2), rs = mksrd(sk, D_ * 8);
      u32 o = kt * 1152 + kch * 2;
#pragma unroll
      for (int i = 0; i < 11; i++) ldb8(kr[i], rk, o + (i - 3) * 1152);
#pragma unroll
      for (int i = 0; i < 8; i++) ldb8(vr[i], rv, o + i * 1152);
      ldb16(sq[0], rs, kch * 8); ldb16(sq[1], rs, kch * 8 + 16);
    }
    xread();
    __builtin_amdgcn_s_setprio(2);
    vmwait<0>();
#pragma unroll
    for (int i = 0; i < 11; i++) PIN(kr[i]);
#pragma unroll
    for (int i = 0; i < 8; i++) PIN(vr[i]);
    PIN(sq[0]); PIN(sq[1]);
    TS(17);
    kv_compute(kr, vr, sq, c.wr, kch, kt);
    TS(18);
    __builtin_amdgcn_s_setprio(0);
  } else {
    xread();
  }
  lgwait(); bar();
  TS(3);
  {
    u32 st = *(LAS u32*)(lds + L_ST + (tg * 32 + (l & 31)) * 4);
    c.nm = -blo(st);
    c.cc = -1.44269504088896f / bhi(st);
  }
  const u32 abase = (8 * kh + ((l >> 2) & 3)) * 128 + ((fg * 64 + ((l >> 4) & 1) * 32 + (l & 3) * 8) ^ (((l >> 3) & 1) << 6));
  const u32 hbase = OFF_H + (rt * 144 + 9 * j) * 8192 + (2 * tg + ((l >> 4) & 1)) * 1024 + (kh * 16 + (l & 15)) * 16;
  LAS char* aB0 = lds + abase;
  LAS char* aB1 = lds + L_B1 + abase;
  LAS char* csb = lds + L_CS + kh * 16;
  int ptile = odd ? 0 : 8;
  u32 hoff = (w < 4) ? hbase + ptile * 8192 : OOB;
  const int klo = (((c.cb * 3) >> 6) * 64) / 3, khi = (((c.cb * 3 + 2) >> 6) * 64 + 63) / 3;
  const bool kfact = klo + l <= khi;
  const u32 kfoff = OFF_FK + (kfact ? klo + l : klo) * 8;
  u32x2 ra[3], rb_[3];
  f32x4 cs[2];
  ra[0] = trread(aB1); rb_[0] = trread(aB1 + 512);
  ra[1] = trread(aB1 + 2048); rb_[1] = trread(aB1 + 2048 + 512);
  cs[0] = *(LAS f32x4*)(csb + ptile * 128);
#pragma unroll 1
  for (int pp = 0; pp < 2; pp++) {
    int t0 = odd + 4 * pp + fg, t1 = t0 + 2;
    g1_pair<0>(c, xf, acc0, acc1, aB1, aB0, ra, rb_, cs, csb + ptile * 128, csb + t0 * 128, hoff,
               pwp + (2 * pp + 1) * 128, pvo, w * 1024, pp, kfoff, kfact);
    if (pp == 0) { TS(4); } else { TS(6); }
    hoff = hbase + t0 * 8192;
    ptile = t0;
    if (pp == 1) break;
    g1_pair<1>(c, xf, acc1, acc0, aB0, aB1, ra, rb_, cs, csb + t0 * 128, csb + t1 * 128, hoff,
               pwp + 2 * 128, pvo, L_B1 + w * 1024, pp, kfoff, kfact);
    TS(5);
    ptile = t1;
    hoff = hbase + t1 * 8192;
  }
  g1_pair<2>(c, xf, acc1, acc0, aB0, aB1, ra, rb_, cs, csb + ptile * 128, csb, hoff, pwp, pvo, 0, 1, kfoff, kfact);
  TS(7);
  ptile += 2;
  g1_epi_final(c, acc1, L_CS + ptile * 128 + kh * 16, hbase + ptile * 8192);
}

static constexpr u32 L_RED = L_B1;
static constexpr u32 L_EX = 73728;
static constexpr u32 L_OWN = 0;
static constexpr u32 L_Y = 18432;

template <int P>
__device__ __forceinline__ void w2_piece(const char* wsrc, const u32 (&cvo)[5], u32 ldst, int w, int l) {
  if (P < 4) glds(wsrc, cvo[P < 4 ? P : 0], ldst + (w * 4 + P) * 1024);
  else if (l < 32) glds(wsrc, cvo[4], ldst + (32 + (w >> 1)) * 1024 + (w & 1) * 512);
}
template <int P>
__device__ __forceinline__ void w2_piece10(const char* wsrc, const u32 (&cvo)[5], int w, int l) {
  w2_piece<P % 5>(wsrc + (P / 5) * 73728, cvo, (P / 5) * 36864, w, l);
}
template <bool GL>
__device__ __forceinline__ void conv_res(f32x4 (&acc)[8], u32x4 (&A)[8], u32x4 (&B)[8], LAS char* ap, LAS char* bp,
                                         const char* wsrc, const u32 (&cvo)[5], int w, int l) {
#pragma unroll
  for (int q = 0; q < 8; q++) {
#pragma unroll
    for (int i = 7 - q; i >= 0; i--) acc[i + q] = mfma16(A[i], B[q], acc[i + q]);
    B[q] = *(LAS u32x4*)(bp + 512 * (7 - q));
    u32x2 lo = *(LAS u32x2*)(ap + 512 * (7 - q)), hi = *(LAS u32x2*)(ap + 512 * (7 - q) + 8);
    A[7 - q] = (u32x4){lo[0], lo[1], hi[0], hi[1]};
    if (GL) {
      if (q == 0) { w2_piece10<0>(wsrc, cvo, w, l); w2_piece10<1>(wsrc, cvo, w, l); }
      if (q == 1) { w2_piece10<2>(wsrc, cvo, w, l); w2_piece10<3>(wsrc, cvo, w, l); }
      if (q == 2) w2_piece10<4>(wsrc, cvo, w, l);
      if (q == 3) w2_piece10<5>(wsrc, cvo, w, l);
      if (q == 4) w2_piece10<6>(wsrc, cvo, w, l);
      if (q == 5) w2_piece10<7>(wsrc, cvo, w, l);
      if (q == 6) w2_piece10<8>(wsrc, cvo, w, l);
      if (q == 7) w2_piece10<9>(wsrc, cvo, w, l);
    }
    sbar();
  }
}

struct G2Ctx {
  const char* wsrc;
  const char* hsrc;
  char* ws;
  LAS char* apA;
  LAS char* apB;
  u32x4 srd, srx;
  u64 tag;
  u32 cvo[5];
  u32 fyoff, yoff0, yoff1, xoff0, xoff2;
  u32 ho0, ho1;
  u32 clr[4];
  __amdgpu_buffer_rsrc_t wr;
  bool fyact;
  int w, l, b;
};
struct G2Tail { u32x2 pf; u32x4 fh[2], yq[2]; u32x2 xv[2]; u32 x8; };

template <int I>
__device__ __forceinline__ void g2_issue1(const char* wsrc, const char* hsrc, const u32 (&cvo)[5], u32 ldst, int w, int l, u32 ho0, u32 ho1,
                                          u32x4 (&hb)[4]) {
  if (I % 2 == 0) w2_piece<I / 2>(wsrc, cvo, ldst, w, l);
  if (I == 1) ldg16s(hb[0], hsrc, ho0);
  if (I == 3) ldg16s(hb[1], hsrc, ho1);
  if (I == 5) ldg16s(hb[2], hsrc + 8192, ho0);
  if (I == 7) ldg16s(hb[3], hsrc + 8192, ho1);
}
template <int IT, int SS>
__device__ __forceinline__ void g2_aread(const G2Ctx& c, u32x2& ra, u32x2& rb_) {
  LAS char* p = ((IT & 2) ? c.apB : c.apA) + (IT & 1) * 36864 + (SS / 9) * 9216 + (SS % 9) * 32;
  ra = trread(p); rb_ = trread(p + 2304);
}
template <int IT, int SS>
__device__ __forceinline__ void g2_step(const G2Ctx& c, G2Tail& t, u32x4 (&hb)[4][4], f32x4 (&acc)[2][9], u32x2 (&ra)[3], u32x2 (&rb_)[3]) {
  constexpr int u = SS / 9, n = SS % 9;
  const int w = c.w, l = c.l;
  if (SS + 2 < 18) g2_aread<IT, (SS + 2) % 18>(c, ra[(SS + 2) % 3], rb_[(SS + 2) % 3]);
  else if (SS == 17 && IT < 8) g2_aread<IT + 1, 1>(c, ra[1], rb_[1]);
  if (SS == 16) lgwait();
  u32x4 A = {ra[SS % 3][0], ra[SS % 3][1], rb_[SS % 3][0], rb_[SS % 3][1]};
  acc[0][n] = mfma16(A, hb[IT & 3][u * 2], acc[0][n]);
  acc[1][n] = mfma16(A, hb[IT & 3][u * 2 + 1], acc[1][n]);
  if (SS == 16 && IT < 8) {
    if (IT == 0) vmwait<4 + 3 + 5 + 8 + 2>();
    else if (IT + 3 < 9) vmwait<9 + 8 + (IT == 1 ? 3 : IT == 2 ? 2 : IT == 3 ? 1 : 0)>();
    else if (IT + 2 < 9) vmwait<9>();
    else vmwait<0>();
    bar();
    PIN(hb[(IT + 1) & 3][0]); PIN(hb[(IT + 1) & 3][1]); PIN(hb[(IT + 1) & 3][2]); PIN(hb[(IT + 1) & 3][3]);
    if (IT == 1 && c.b < 192 && w == 0 && l == 0) flag_set(c.ws, OFF_FY + c.b * 8, c.tag);
    if (IT == 4 && w == 0 && l == 0) flag_set(c.ws, OFF_FX + c.b * 8, c.tag);
    g2_aread<IT + 1, 0>(c, ra[0], rb_[0]);
  }
  if ((IT == 0 && (SS == 4 || SS == 12)) || ((IT == 1 || IT == 2) && SS == 8))
    __builtin_amdgcn_raw_buffer_store_b128((u32x4){EMPTY, EMPTY, EMPTY, EMPTY}, c.wr, c.clr[IT == 0 ? SS / 12 : IT + 1], 0, SC);
  if (IT + 3 < 9) {
    const char* ws_ = c.wsrc + (IT + 3) * 73728;
    const char* hs_ = c.hsrc + (IT + 3) * 16384;
    if (SS % 2 == 1 && SS < 16) g2_issue1<(SS % 2 == 1 ? SS / 2 : 0)>(ws_, hs_, c.cvo, ((IT + 3) & 3) * 36864, w, l, c.ho0, c.ho1, hb[(IT + 3) & 3]);
    if (SS == 17) g2_issue1<8>(ws_, hs_, c.cvo, ((IT + 3) & 3) * 36864, w, l, c.ho0, c.ho1, hb[(IT + 3) & 3]);
  }
  if (IT == 7 && SS == 17) {
    ldg8s(t.pf, c.ws, c.fyoff);
    u32 o = (c.b == 0 && w == 7) ? OFF_FH + l * 32 : OOB;
    ldb16s(t.fh[0], c.srd, o); ldb16s(t.fh[1], c.srd, o + 16);
  }
  if (IT == 8 && SS == 12) {
    vmwait<0>();
    PIN(t.pf); PIN(t.fh[0]); PIN(t.fh[1]);
    if (!flag_ok(t.pf, c.fyact, c.tag)) flag_wait(c.ws, c.fyoff, c.fyact, c.tag);
    if (c.b == 0 && w == 7) {
      const u32 lo = (u32)c.tag, hi = (u32)(c.tag >> 32);
      bool ok = t.fh[0][0] == lo && t.fh[0][1] == hi && t.fh[0][2] == lo && t.fh[0][3] == hi &&
                t.fh[1][0] == lo && t.fh[1][1] == hi && t.fh[1][2] == lo && t.fh[1][3] == hi;
      if (!__all(ok))
        for (int i = 0; i < 4; i++) flag_wait(c.ws, OFF_FH + (i * 64 + l) * 8, true, c.tag);
      if (l == 0) flag_set(c.ws, OFF_E, c.tag);
    }
    ldb16s(t.yq[0], c.srd, c.yoff0); ldb16s(t.yq[1], c.srd, c.yoff1);
    ldb8(t.xv[0], c.srx, c.xoff0); ldb8(t.xv[1], c.srx, c.xoff0 + 32); ldb2(t.x8, c.srx, c.xoff2);
  }
  sbar();
}
template <int IT, int SS>
__device__ __forceinline__ void g2_steps(const G2Ctx& c, G2Tail& t, u32x4 (&hb)[4][4], f32x4 (&acc)[2][9], u32x2 (&ra)[3], u32x2 (&rb_)[3]) {
  g2_step<IT, SS>(c, t, hb, acc, ra, rb_);
  if constexpr (SS < 17) g2_steps<IT, SS + 1>(c, t, hb, acc, ra, rb_);
  else if constexpr (IT < 8) g2_steps<IT + 1, 0>(c, t, hb, acc, ra, rb_);
}
__device__ __forceinline__ f32x4 rb4(f32x4 v) {
  u32 a = pk(v[0], v[1]), b = pk(v[2], v[3]);
  return (f32x4){blo(a), bhi(a), blo(b), bhi(b)};
}
__device__ __forceinline__ f32x4 gate_in(f32x4 x, u32x2 yv, float m, float rs) {
  f32x4 xn = rb4(rb4(x - m) * rs);
  return rb4(xn * (f32x4){blo(yv[0]), bhi(yv[0]), blo(yv[1]), bhi(yv[1])});
}
__device__ __forceinline__ void fin4(f32x4 s, f32x4 x, f32x4 sv, float rs, u16* op) {
  f32x4 o = rb4(s * rs), g;
#pragma unroll
  for (int e = 0; e < 4; e++) g[e] = o[e] * __builtin_amdgcn_rcpf(1.0f + __builtin_amdgcn_exp2f(o[e] * -1.44269504088896f));
  f32x4 res = x + rb4(sv * rb4(g));
  u32x2 r = {pk(res[0], res[1]), pk(res[2], res[3])};
  asm volatile("global_store_dwordx2 %0, %1, off sc0 sc1" ::"v"(op), "v"(r) : "memory");
}
__device__ __forceinline__ u32 umax(u32 a, u32 b) { return a > b ? a : b; }
__device__ __forceinline__ u32 pkw(float a, float b) { u32 v = pk(a, b); return v < EMPTY - 1 ? v : EMPTY - 1; }
#define PDELAY 12
__device__ __forceinline__ f32x4 up4(u32 a, u32 b) { return (f32x4){blo(a), bhi(a), blo(b), bhi(b)}; }

__device__ __forceinline__ void g2_tail(G1Ctx& g, const G2Ctx& c, G2Tail& t, f32x4 (&acc)[2][9], u16* out, int rt, int kq, int dn,
                                        const int l TSARG) {
  LAS char* lds = g.lds;
  const int w = g.w, b = g.b;
  const int tg = w & 3, kh = w >> 2, xbit = rt >> 3, i7 = rt & 7, lg = l >> 4, lb = l & 15;
  const int p = w & 3, tt = w >> 2;
  const int tok = rt * 128 + kq * 32 + tt * 16 + lb, d0 = dn * 144 + 4 * lg;
  const int t8 = tt * 16 + 4 * p + lg, tok8 = rt * 128 + kq * 32 + t8;
#pragma unroll
  for (int n = 0; n < 9; n++) *(LAS f32x4*)(lds + L_EX + w * 9216 + n * 1024 + l * 16) = acc[0][n];
  lgwait(); bar();
  f32x4 r[9];
#pragma unroll
  for (int n = 0; n < 9; n++) r[n] = acc[1][n] + *(LAS f32x4*)(lds + L_EX + (w ^ 4) * 9216 + n * 1024 + l * 16);
  {
    int ob = 2 * tg + xbit + 8 * (dn * 8 + i7);
    u32 po = tg != kq ? OFF_P + (ob * 3 + ((kq - tg - 1) & 3)) * 9216 + kh * 4608 : OOB;
#pragma unroll
    for (int q = 0; q < 4; q++)
      __builtin_amdgcn_raw_buffer_store_b128((u32x4){pkw(r[2 * q][0], r[2 * q][1]), pkw(r[2 * q][2], r[2 * q][3]),
                                                     pkw(r[2 * q + 1][0], r[2 * q + 1][1]), pkw(r[2 * q + 1][2], r[2 * q + 1][3])},
                                             g.wr, po + q * 1024 + l * 16, 0, SC);
    __builtin_amdgcn_raw_buffer_store_b64((u32x2){pkw(r[8][0], r[8][1]), pkw(r[8][2], r[8][3])}, g.wr, po + 4096 + l * 8, 0, SC);
    if (tg == kq) {
#pragma unroll
      for (int n = 0; n < 9; n++) *(LAS f32x4*)(lds + L_OWN + kh * 9216 + n * 1024 + l * 16) = r[n];
    }
  }
  vmwait<5>();
  PIN(t.yq[0]); PIN(t.yq[1]); PIN(t.xv[0]); PIN(t.xv[1]); PIN(t.x8);
  *(LAS u32x4*)(lds + L_Y + (w * 16 + (l >> 2)) * 96 + (l & 3) * 16) = t.yq[0];
  if (w == 0) *(LAS u32x4*)(lds + L_Y + (128 + (l >> 2)) * 96 + (l & 3) * 16) = t.yq[1];
  lgwait(); bar();
  TS(14);
  const u32 st = *(LAS u32*)(lds + L_ST + (kq * 32 + tt * 16 + lb) * 4), st8 = *(LAS u32*)(lds + L_ST + (kq * 32 + t8) * 4);
  LAS char* yp = lds + L_Y + (4 * lg + ((l >> 2) & 3)) * 96 + tt * 32 + (l & 3) * 8;
  LAS char* op = lds + L_OWN + tt * 9216;
  const u32x2 y0 = trread(yp + p * 32 * 96), y1 = trread(yp + (p * 32 + 16) * 96);
  const u32 y8 = *(LAS u16*)(lds + L_Y + (128 + lb) * 96 + t8 * 2);
  const int l8 = ((l & 15) >> 2) * 16 + 4 * p + (l >> 4);
  f32x4 s0 = *(LAS f32x4*)(op + 2 * p * 1024 + l * 16), s1 = *(LAS f32x4*)(op + (2 * p + 1) * 1024 + l * 16);
  float s8 = *(LAS float*)(op + 8 * 1024 + l8 * 16 + (l & 3) * 4);
  float rs = 1.0f / bhi(st), rs8 = 1.0f / bhi(st8);
  f32x4 xa = up4(t.xv[0][0], t.xv[0][1]), xb = up4(t.xv[1][0], t.xv[1][1]);
  float x8 = blo(t.x8);
  f32x4 va = gate_in(xa, y0, blo(st), rs), vb = gate_in(xb, y1, blo(st), rs);
  float v8 = rb(rb(rb(x8 - blo(st8)) * rs8) * blo(y8));
  PIN(va); PIN(vb); PIN(v8); PIN(s0); PIN(s1); PIN(s8); PIN(rs); PIN(rs8);
  u32x4 pq[3];
  u32 p8[3];
  {
    const u32 po = OFF_P + b * 3 * 9216 + tt * 4608;
    const u32 o16 = po + p * 1024 + l * 16, o8 = po + 4096 + l8 * 8 + (l & 2) * 2;
    auto sweep = [&]() {
#pragma unroll
      for (int s = 0; s < 3; s++) { ldb16s(pq[s], c.srd, o16 + s * 9216); ldb4s(p8[s], c.srd, o8 + s * 9216); }
    };
    if (PDELAY) __builtin_amdgcn_s_sleep(PDELAY);
    sweep();
#pragma unroll 1
    for (int spins = 0; spins < 1000000; spins++) {
      vmwait<0>();
      u32 mx = 0;
#pragma unroll
      for (int s = 0; s < 3; s++) {
        PIN(pq[s]); PIN(p8[s]);
        mx = umax(umax(mx, p8[s]), umax(umax(pq[s][0], pq[s][1]), umax(pq[s][2], pq[s][3])));
      }
      if (__all(mx != EMPTY)) break;
      sweep();
    }
    vmwait<0>();
#pragma unroll
    for (int s = 0; s < 3; s++) { PIN(pq[s]); PIN(p8[s]); }
  }
  TS(15);
  const u32 sh8 = (l & 1) ? 0 : 16;
#pragma unroll
  for (int s = 0; s < 3; s++) {
    s0 += up4(pq[s][0], pq[s][1]); s1 += up4(pq[s][2], pq[s][3]);
    s8 += __uint_as_float((p8[s] << sh8) & 0xffff0000u);
  }
  u16* o = out + tok * D_ + d0 + p * 32;
  fin4(s0, xa, va, rs, o);
  fin4(s1, xb, vb, rs, o + 16);
  {
    float oo = rb(s8 * rs8);
    float gg = rb(oo * __builtin_amdgcn_rcpf(1.0f + __builtin_amdgcn_exp2f(oo * -1.44269504088896f)));
    float res = x8 + rb(v8 * gg);
    asm volatile("global_store_short %0, %1, off sc0 sc1" ::"v"(out + tok8 * D_ + dn * 144 + 128 + lb), "v"(pk(res, res)) : "memory");
  }
}

__device__ __forceinline__ void conv_g2(G1Ctx& g, const u16* x, const u16* Dv, const u16* w2, u16* out, int rt, int kq, int dn TSARG) {
  LAS char* lds = g.lds;
  char* ws = g.ws;
  int l = g.l;
  asm volatile("" : "+v"(l));
  const int w = g.w, b = g.b, cb = g.cb;
  const int tg = w & 3, kh = w >> 2, xbit = rt >> 3, i7 = rt & 7, lg = l >> 4;
  G2Ctx c;
  c.ws = ws; c.tag = g.tag; c.w = w; c.l = l; c.b = b;
#pragma unroll
  for (int i = 0; i < 5; i++) {
    u32 idx = i < 4 ? (w * 4 + i) * 64 + l : (32 + (w >> 1)) * 64 + (w & 1) * 32 + (l & 31);
    u32 row = idx / 18, c16 = idx - row * 18;
    c.cvo[i] = ((row >> 6) * 576 + (row & 63)) * 1152 + c16 * 16;
  }
  c.wsrc = (const char*)w2 + (size_t)(1152 * kq) * 1152 + dn * 288;
  c.hsrc = ws + OFF_H + (size_t)((rt * 144 + 36 * kq + 18 * kh) * 8 + 2 * tg) * 1024;
  f32x4 ca[8];
#pragma unroll
  for (int i = 0; i < 8; i++) ca[i] = (f32x4){0, 0, 0, 0};
  const int ch = (w < 3) ? 0 : (w < 6) ? 1 : 2;
  const int r0 = (w == 0 || w == 4 || w == 6) ? 0 : (w == 1 || w == 5) ? 3 : (w == 7) ? 4 : 6;
  const int nr = (w >= 6) ? 4 : (w == 2 || w == 3) ? 2 : 3;
  const int lb = l & 15, cp = (lb + 1) & 3;
  LAS char* ap = lds + L_KC + (ch * 4 + cp) * 4160 + 2 * (lb + 1 - cp) + 16 * lg + 64 * r0;
  LAS char* bp = lds + L_U + ch * 4608 + 2 * (240 - 16 * lb + 8 * lg) + 64 * r0;
  u32x4 A[8], B[8];
  u32 dq[3];
#pragma unroll
  for (int i = 0; i < 3; i++) ldg4(dq[i], Dv, ((cb * 3 + ((w * 3 + i) >> 3)) >> 1) * 4);
  if (b < 192) {
#pragma unroll
    for (int i = 7; i >= 0; i--) {
      B[7 - i] = *(LAS u32x4*)(bp + 512 * i);
      u32x2 lo = *(LAS u32x2*)(ap + 512 * i), hi = *(LAS u32x2*)(ap + 512 * i + 8);
      A[i] = (u32x4){lo[0], lo[1], hi[0], hi[1]};
    }
    conv_res<true>(ca, A, B, ap + 64, bp + 64, c.wsrc, c.cvo, w, l);
  } else {
    w2_piece10<0>(c.wsrc, c.cvo, w, l); w2_piece10<1>(c.wsrc, c.cvo, w, l); w2_piece10<2>(c.wsrc, c.cvo, w, l);
    w2_piece10<3>(c.wsrc, c.cvo, w, l); w2_piece10<4>(c.wsrc, c.cvo, w, l); w2_piece10<5>(c.wsrc, c.cvo, w, l);
    w2_piece10<6>(c.wsrc, c.cvo, w, l); w2_piece10<7>(c.wsrc, c.cvo, w, l); w2_piece10<8>(c.wsrc, c.cvo, w, l);
    w2_piece10<9>(c.wsrc, c.cvo, w, l);
  }
  vmwait<10>();
  PIN(dq[0]); PIN(dq[1]); PIN(dq[2]);
  if (l == 0) {
    u32 old = __hip_atomic_fetch_add((LAS u32*)(lds + L_CNT), 1u, __ATOMIC_RELAXED, __HIP_MEMORY_SCOPE_WORKGROUP);
    if (old == 7) flag_set(ws, OFF_FH + b * 8, g.tag);
  }
  TS(8);
  if (b < 192) {
#pragma unroll 1
    for (int r = 1; r < nr; r++) conv_res<false>(ca, A, B, ap + 64 * (r + 1), bp + 64 * (r + 1), c.wsrc, c.cvo, w, l);
  }
  TS(9);
  u32x2 uu[3];
#pragma unroll
  for (int i = 0; i < 3; i++) {
    int u = w * 3 + i;
    uu[i] = *(LAS u32x2*)(lds + L_U + (u >> 3) * 4608 + 2 * (2044 - (256 * (u & 7) + 16 * lb + 4 * lg)));
  }
  const u32 fhoff = OFF_FH + ((b & 7) + 8 * ((l & 3) * 8 + i7)) * 8;
  u32x2 pf;
  ldg8s(pf, ws, fhoff);
  lgwait(); bar();
#pragma unroll
  for (int i = 0; i < 8; i++) *(LAS f32x4*)(lds + L_RED + w * 8192 + i * 1024 + l * 16) = ca[i];
  lgwait(); bar();
  TS(10);
  f32x4 ys[3];
#pragma unroll
  for (int i = 0; i < 3; i++) {
    int u = w * 3 + i, c2 = u >> 3;
    LAS char* rp = lds + L_RED + c2 * 3 * 8192 + (u & 7) * 1024 + l * 16;
    ys[i] = *(LAS f32x4*)(rp) + *(LAS f32x4*)(rp + 8192);
    if (c2 < 2) ys[i] += *(LAS f32x4*)(rp + 2 * 8192);
  }
  vmwait<0>();
  PIN(pf);
  if (!flag_ok(pf, true, g.tag)) flag_wait(ws, fhoff, true, g.tag);
  TS(11);
  u32x4 hb[4][4];
  c.ho0 = l * 16 + (kh ? 0 : 1024); c.ho1 = l * 16 + (kh ? 1024 : 0);
#pragma unroll
  for (int it = 0; it < 3; it++) {
    ldg16s(hb[it][0], c.hsrc + it * 16384, c.ho0); ldg16s(hb[it][1], c.hsrc + it * 16384, c.ho1);
    ldg16s(hb[it][2], c.hsrc + it * 16384 + 8192, c.ho0); ldg16s(hb[it][3], c.hsrc + it * 16384 + 8192, c.ho1);
  }
#pragma unroll
  for (int i = 0; i < 3; i++) {
    int u = w * 3 + i, chn = cb * 3 + (u >> 3);
    float dv = (chn & 1) ? bhi(dq[i]) : blo(dq[i]);
    float o0 = rb(ys[i][0]) + rb(bhi(uu[i][1]) * dv), o1 = rb(ys[i][1]) + rb(blo(uu[i][1]) * dv);
    float o2 = rb(ys[i][2]) + rb(bhi(uu[i][0]) * dv), o3 = rb(ys[i][3]) + rb(blo(uu[i][0]) * dv);
    u32 o = b < 192 ? OFF_Y2 + chn * 4096 + (256 * (u & 7) + 16 * lb + 4 * lg) * 2 : OOB;
    __builtin_amdgcn_raw_buffer_store_b64((u32x2){pk(o0, o1), pk(o2, o3)}, g.wr, o, 0, SC);
  }
  lgwait(); bar();
  w2_piece<0>(c.wsrc + 2 * 73728, c.cvo, 73728, w, l); w2_piece<1>(c.wsrc + 2 * 73728, c.cvo, 73728, w, l);
  w2_piece<2>(c.wsrc + 2 * 73728, c.cvo, 73728, w, l); w2_piece<3>(c.wsrc + 2 * 73728, c.cvo, 73728, w, l);
  w2_piece<4>(c.wsrc + 2 * 73728, c.cvo, 73728, w, l);
  c.apA = lds + (kh * 64 + 16 * (lg >> 1) + 4 * (lg & 1) + ((l >> 2) & 3)) * 288 + (l & 3) * 8;
  c.apB = c.apA + 73728;
  c.srd = mksrd(ws, WS_END);
  c.srx = mksrd(x, S_ * D_ * 2);
  const int p = w & 3, tt = w >> 2;
  const int tok = rt * 128 + kq * 32 + tt * 16 + lb, d0 = dn * 144 + 4 * lg;
  const int t8 = tt * 16 + 4 * p + lg, tok8 = rt * 128 + kq * 32 + t8;
  c.fyact = l < 51;
  c.fyoff = l < 48 ? OFF_FY + (48 * dn + l) * 8 : OFF_FX + (2 * ((kq + 1 + (l < 51 ? l - 48 : 0)) & 3) + xbit + 8 * (dn * 8 + i7)) * 8;
  c.wr = g.wr;
#pragma unroll
  for (int i = 0; i < 4; i++) {
    int n = w * 4 + i, s = n / 9;
    int ob = 2 * ((kq + 1 + s) & 3) + xbit + 8 * (dn * 8 + i7);
    c.clr[i] = n < 27 ? OFF_P + (ob * 3 + 2 - s) * 9216 + (n - 9 * s) * 1024 + l * 16 : OOB;
  }
  {
    u32 yb = OFF_Y2 + (dn * 144 + w * 16 + (l >> 2)) * 4096 + (rt * 128 + kq * 32) * 2 + (l & 3) * 16;
    c.yoff0 = yb;
    c.yoff1 = w == 0 ? yb + 128 * 4096 : OOB;
    c.xoff0 = tok * 1152 + (d0 + p * 32) * 2;
    c.xoff2 = tok8 * 1152 + (dn * 144 + 128 + lb) * 2;
  }
  f32x4 acc[2][9];
#pragma unroll
  for (int n = 0; n < 9; n++) { acc[0][n] = (f32x4){0, 0, 0, 0}; acc[1][n] = (f32x4){0, 0, 0, 0}; }
  G2Tail t;
  u32x2 ra[3], rb_[3];
  g2_aread<0, 0>(c, ra[0], rb_[0]); g2_aread<0, 1>(c, ra[1], rb_[1]);
  vmwait<8 + 3 + 5>();
  PIN(hb[0][0]); PIN(hb[0][1]); PIN(hb[0][2]); PIN(hb[0][3]);
  TS(12);
  g2_steps<0, 0>(c, t, hb, acc, ra, rb_);
  TS(13);
  int lz = l;
  asm volatile("" : "+v"(lz));
  g2_tail(g, c, t, acc, out, rt, kq, dn, lz TSPASS);
}

__global__ void __launch_bounds__(512, 2) k_fused(const u16* x, const u16* k, const u16* v, const u16* sk, const u16* ssk, const u16* Dv,
                                                  const u16* w1, const u16* w2, u16* out, char* ws) {
  extern __shared__ __attribute__((aligned(1024))) char smem[];
  const int tid = threadIdx.x, b = blockIdx.x;
  const int xc = b & 7, i = b >> 3;
  const int rt = (xc & 1) * 8 + (i & 7), kq = xc >> 1, dn = i >> 3;
  G1Ctx c;
  c.lds = (LAS char*)smem;
  c.ws = ws;
  c.ssk = ssk;
  c.wr = mkrsrc(ws, WS_END);
  c.b = b; c.cb = b < 192 ? b : 191;
  c.w = __builtin_amdgcn_readfirstlane(tid >> 6); c.l = tid & 63;
  g1_phase(c, x, k, v, sk, w1, w2, rt, 4 * kq + dn, kq TSPASS);
  conv_g2(c, x, Dv, w2, out, rt, kq, dn TSPASS);
}

extern "C" void kernel_launch(void* const* d_in, const int* in_sizes, int n_in, void* d_out, int out_size, void* d_ws, size_t ws_size,
                              hipStream_t stream) {
  (void)in_sizes; (void)n_in; (void)out_size; (void)ws_size;
  (void)hipFuncSetAttribute((const void*)k_fused, hipFuncAttributeMaxDynamicSharedMemorySize, LDS_BYTES);
  k_fused<<<256, 512, LDS_BYTES, stream>>>((const u16*)d_in[0], (const u16*)d_in[1], (const u16*)d_in[2], (const u16*)d_in[3],
                                           (const u16*)d_in[4], (const u16*)d_in[5], (const u16*)d_in[7], (const u16*)d_in[8],
                                           (u16*)d_out, (char*)d_ws);
}
```
